```python
import torch

def _softmax(x, axis=-1):
    return torch.softmax(x, dim=axis)

def _gelu(x):
    return torch.nn.functional.gelu(x, approximate='tanh')

def _silu(x):
    return torch.nn.functional.silu(x)

def _sigmoid(x):
    return torch.sigmoid(x)

def _layernorm(x, axis=-1, eps=1e-05):
    m = x.mean(dim=axis, keepdim=True)
    v = x.var(dim=axis, keepdim=True, unbiased=False)
    return (x - m) / torch.sqrt(v + eps)

def _scatter_add(tgt, idx, src, axis=0):
    return tgt.clone().index_add_(axis, idx, src)

def _segment_sum(src, seg, n):
    out = torch.zeros((n,) + src.shape[1:], dtype=src.dtype)
    return out.index_add_(0, seg, src)

def _pad(x, pw):
    flat = tuple((v for pair in reversed(pw) for v in pair))
    return torch.nn.functional.pad(x, flat)
kernel_name = 'concat_heads__spectral_gfnet__memory_aug_recurrent_mem_tr__prompt'

def setup_inputs():

    _w = lambda a: (lambda t: t.to(torch.bfloat16) if t.is_floating_point() else t)(torch.as_tensor(a))

    def _A_setup():
        B, S, D = (1, 2048, 640)
        Df = D // 2 + 1
        return {'x': _w((lambda *s: torch.randn(*s, dtype=torch.bfloat16) * 0.05)(B, S, D)), 'w_r': _w((lambda *s: torch.randn(*s, dtype=torch.bfloat16) * 3.2)(S, Df) * 0.02), 'w_i': _w((lambda *s: torch.randn(*s, dtype=torch.bfloat16) * 3.2)(S, Df) * 0.02)}
    _A_in = _A_setup()

    def _B_setup():
        B, S, Dm = (1, 2048, 640)
        H, Dh = (32, 128)
        M, seg = (8, 32)
        seg = min(seg, S)
        S = S // seg * seg
        return {'x': _w((lambda *s: torch.randn(*s, dtype=torch.bfloat16) * 0.05)(B, S, Dm)), 'mem0': _w((lambda *s: torch.randn(*s, dtype=torch.bfloat16) * 0.4)(B, M, Dm)), 'Wq': _w((lambda *s: torch.randn(*s, dtype=torch.bfloat16) * 6.4)(Dm, H, Dh) * Dm ** (-0.5)), 'Wk': _w((lambda *s: torch.randn(*s, dtype=torch.bfloat16) * 6.4)(Dm, H, Dh) * Dm ** (-0.5)), 'Wv': _w((lambda *s: torch.randn(*s, dtype=torch.bfloat16) * 0.4)(Dm, H, Dh) * Dm ** (-0.5)), 'Wo': _w((lambda *s: torch.randn(*s, dtype=torch.bfloat16) * 0.4)(H, Dh, Dm) * (H * Dh) ** (-0.5)), 'seg_len': seg}
    _B_in = _B_setup()
    return {'A_x': _A_in['x'], 'A_w_r': _A_in['w_r'], 'A_w_i': _A_in['w_i'], 'B_x': _B_in['x'], 'B_mem0': _B_in['mem0'], 'B_Wq': _B_in['Wq'], 'B_Wk': _B_in['Wk'], 'B_Wv': _B_in['Wv'], 'B_Wo': _B_in['Wo'], 'B_seg_len': _B_in['seg_len']}

def reference(A_x, A_w_r, A_w_i, B_x, B_mem0, B_Wq, B_Wk, B_Wv, B_Wo, B_seg_len):

    def _A_ref(x, w_r, w_i):
        B, S, D = x.shape
        X = (lambda x, axes=(-2, -1): torch.fft.rfft2(x.float(), dim=axes))(x, axes=(-2, -1))
        W = w_r + 1j * w_i
        X = X * W
        y = (lambda x, s, axes=(-2, -1): torch.fft.irfft2(x, s=s, dim=axes).to(torch.bfloat16))(X, (S, D), axes=(-2, -1))
        return y

    def _B_ref(x, mem0, Wq, Wk, Wv, Wo, seg_len):
        B, S, Dm = x.shape
        H, Dh = (Wq.shape[1], Wq.shape[2])
        M = mem0.shape[1]
        scale = Dh ** (-0.5)
        n_seg = S // seg_len
        L = M + seg_len
        idx = torch.arange(L)
        mask = (idx[:, None] < M) | (idx[:, None] >= idx[None, :])
        mem = mem0
        outs = []
        for s in range(n_seg):
            xs = x[:, s * seg_len:(s + 1) * seg_len, :].clone()
            h = torch.concatenate([mem, xs], axis=1)
            q = torch.einsum('btd,dhe->bhte', h, Wq)
            k = torch.einsum('btd,dhe->bhte', h, Wk)
            v = torch.einsum('btd,dhe->bhte', h, Wv)
            scores = torch.einsum('bhqe,bhke->bhqk', q, k) * scale
            scores = torch.where(mask[None, None], scores, -1000000000.0)
            p = _softmax(scores, axis=-1)
            o = torch.einsum('bhqk,bhke->bhqe', p, v)
            o = torch.einsum('bhte,hed->btd', o, Wo)
            mem = o[:, :M, :].clone()
            outs.append(o[:, M:, :])
        return torch.concatenate(outs, axis=1)
    a = _A_ref(A_x, A_w_r, A_w_i)
    b = _B_ref(B_x, B_mem0, B_Wq, B_Wk, B_Wv, B_Wo, B_seg_len)
    return torch.concatenate([a, b], axis=-1)
```

```cpp
#include <hip/hip_runtime.h>
#include <hip/hip_bf16.h>
#include <stdint.h>

typedef unsigned short u16;
typedef unsigned int u32;
typedef unsigned long long u64;
typedef __bf16 bf16x2 __attribute__((ext_vector_type(2)));
typedef __bf16 bf16x8 __attribute__((ext_vector_type(8)));
typedef __bf16 bf16x16 __attribute__((ext_vector_type(16)));
typedef float f32x4 __attribute__((ext_vector_type(4)));
typedef u32 u32x2 __attribute__((ext_vector_type(2)));
typedef u32 u32x4 __attribute__((ext_vector_type(4)));
typedef u32 u32x8 __attribute__((ext_vector_type(8)));
typedef __amdgpu_buffer_rsrc_t rsrc_t;
#define DEV __device__ __forceinline__

constexpr int NSEG = 64;
constexpr u32 MB = 1u << 20;
constexpr u32 OFF_COMM = 0;
constexpr u32 OFF_WF = 6 * MB;
constexpr u32 OFF_WOF = 21 * MB;
constexpr u32 OFF_BF = 26 * MB;
constexpr u32 OFF_XF = 51 * MB;
constexpr u32 OFF_AXF = 54 * MB;
constexpr u32 OFF_KX = 57 * MB;
constexpr u32 OFF_QX = 73 * MB;
constexpr u32 OFF_UX = 89 * MB;
constexpr u32 OFF_UM = 169 * MB;
constexpr u32 OFF_PXX = 189 * MB;
constexpr u32 OFF_PXM = 193 * MB;
constexpr u32 OFF_FX = 194 * MB;
constexpr u32 OFF_FY = 200 * MB;
constexpr u32 OFF_FLAGS = 206 * MB;
constexpr u32 OFF_FW1 = 208 * MB;
constexpr u32 OFF_FW3 = 210 * MB;
constexpr u32 WS_BYTES = 212 * MB;
constexpr u32 STEP_BYTES = 65536;
constexpr u32 SENT = 0xFFFFFFFFu;
constexpr u32 SPIN_CAP = 1u << 16;

struct Params {
  const u16* ax; const u16* wr; const u16* wi;
  const u16* x; const u16* mem0; const u16* wq; const u16* wk; const u16* wv; const u16* wo;
  u16* out; unsigned char* ws;
};

DEV rsrc_t mk_rsrc(const void* p, u32 bytes) { return __builtin_amdgcn_make_buffer_rsrc((void*)p, 0, bytes, 0x00020000); }
DEV u32x4 ld16(rsrc_t r, u32 vo, u32 so) { return __builtin_amdgcn_raw_buffer_load_b128(r, vo, so, 0); }
DEV u32x4 ld16c(rsrc_t r, u32 vo, u32 so) { return __builtin_amdgcn_raw_buffer_load_b128(r, vo, so, 16); }
DEV void st16(rsrc_t r, u32 vo, u32 so, u32x4 v) { __builtin_amdgcn_raw_buffer_store_b128(v, r, vo, so, 0); }
DEV void st16c(rsrc_t r, u32 vo, u32 so, u32x4 v) { __builtin_amdgcn_raw_buffer_store_b128(v, r, vo, so, 16); }
DEV void st8c(rsrc_t r, u32 vo, u32 so, u32x2 v) { __builtin_amdgcn_raw_buffer_store_b64(v, r, vo, so, 16); }
DEV u32 pk2(float a, float b) { bf16x2 v = {(__bf16)a, (__bf16)b}; return __builtin_bit_cast(u32, v); }
DEV u32 pk2s(float a, float b) { u32 v = pk2(a, b); return v < 0xFFFFFFFEu ? v : 0xFFFFFFFEu; }
DEV float bfr(float a) { return (float)(__bf16)a; }
DEV f32x4 mfma(u32x4 a, u32x4 b, f32x4 c) {
  return __builtin_amdgcn_mfma_f32_16x16x32_bf16(__builtin_bit_cast(bf16x8, a), __builtin_bit_cast(bf16x8, b), c, 0, 0, 0);
}
DEV f32x4 smfma(u32x4 a, u32x8 b, f32x4 c, int idx) {
  return __builtin_amdgcn_smfmac_f32_16x16x64_bf16(__builtin_bit_cast(bf16x8, a), __builtin_bit_cast(bf16x16, b), c, idx, 0, 0);
}
DEV u32x8 cat(u32x4 a, u32x4 b) { return __builtin_shufflevector(a, b, 0, 1, 2, 3, 4, 5, 6, 7); }
DEV u32 comm_base(const unsigned char* ws) { return (u32)((2 * MB - ((uintptr_t)ws & (2 * MB - 1))) & (2 * MB - 1)); }
DEV u64 stamp_cyc() { u64 t; asm volatile("s_memtime %0\n\ts_waitcnt lgkmcnt(0)" : "=s"(t) :: "memory"); return t; }
DEV u32 umax3(u32 a, u32 b, u32 c) { u32 m = a > b ? a : b; return m > c ? m : c; }

#define CSTAMP(ev)
#define CDECL
#define CDUMP
#define STAMP(ev)
#define STAMPV(ev, val)
#define COUNT(ev, val)
__global__ __launch_bounds__(512) void k_prep(Params p) {
  extern __shared__ __attribute__((aligned(16))) unsigned char lds[];
  const int b = blockIdx.x, tid = threadIdx.x, w = tid >> 6, l = tid & 63, c16 = l & 15, g = l >> 4;
  const rsrc_t ws = mk_rsrc(p.ws, WS_BYTES);
  {
    const u32 cb = comm_base(p.ws);
    for (int i = 0; i < 2; ++i) {
      u32 o = (u32)(b * 1024 + i * 512 + tid) * 16;
      u32x4 v = {SENT, SENT, SENT, SENT};
      if (o < STEP_BYTES) {
        u32 L = (o >> 14) * 8192 + (o & 8191);
        if (L < 10240) {
          int vv = L >> 7, t = (L >> 4) & 7;
          const u16* m = p.mem0 + t * 640 + 16 * (vv >> 1) + 2 * (vv & 1);
          for (int a = 0; a < 4; ++a) { u32 q = *(const u32*)(m + 4 * a); v[a] = q < 0xFFFFFFFEu ? q : 0xFFFFFFFEu; }
        }
      }
      st16c(ws, cb + o, 0, v);
    }
  }
  if (b < 5) st16c(ws, OFF_FLAGS + (u32)(b * 512 + tid) * 16, 0, (u32x4){0, 0, 0, 0});
  for (int q = b * 512 + tid; q < 2 * 51200; q += 256 * 512) {
    const int which = q / 51200, r = q % 51200, tile = r / 1280, ks = (r % 1280) >> 6, ln = r & 63;
    u32x4 hi, lo;
    float val[8];
#pragma unroll
    for (int j = 0; j < 8; ++j) {
      const int a = 16 * tile + (ln & 15), bb = 32 * ks + 8 * (ln >> 4) + j;
      const int kp = which ? bb : a, d = which ? a : bb;
      const int kk = (kp == 1) ? 320 : (kp >> 1);
      float sn, cs;
      sincospif((float)((kk * d) % 640) * (1.0f / 320.0f), &sn, &cs);
      float x = (kp & 1) && kp != 1 ? -sn : cs;
      if (which && kp > 1) x *= 2.0f;
      val[j] = x;
    }
#pragma unroll
    for (int j = 0; j < 4; ++j) {
      hi[j] = pk2(val[2 * j], val[2 * j + 1]);
      lo[j] = pk2(val[2 * j] - __uint_as_float(hi[j] << 16), val[2 * j + 1] - __uint_as_float(hi[j] & 0xFFFF0000u));
    }
    const u32 o = (which ? OFF_FW3 : OFF_FW1) + (u32)((tile * 40 + ks) * 1024 + ln * 16);
    st16(ws, o, 0, hi);
    st16(ws, o + 20480, 0, lo);
  }
  for (int q = b * 512 + tid; q < 2 * 163840; q += 256 * 512) {
    int which = q / 163840, r0 = q % 163840;
    int seg = r0 / 2560, r = r0 % 2560, tt = r / 1280, ks = (r % 1280) >> 6, ln = r & 63;
    const u16* src = (which ? p.ax : p.x) + (32 * seg + 16 * tt + (ln & 15)) * 640 + 32 * ks + 8 * (ln >> 4);
    st16(ws, (which ? OFF_AXF : OFF_XF) + (u32)r0 * 16, 0, *(const u32x4*)src);
  }
  {
    const int h = b >> 3, part = b & 7;
    u16* wos = (u16*)lds;
    for (int c = tid; c < 1280; c += 512) {
      int e = c / 10, cc = c % 10;
      *(u32x4*)(wos + e * 88 + cc * 8) = *(const u32x4*)(p.wo + (h * 128 + e) * 640 + part * 80 + cc * 8);
    }
    __syncthreads();
    for (int f = w; f < 20; f += 8) {
      int ct = f >> 2, ks = f & 3;
      u32x4 v;
      for (int i = 0; i < 4; ++i) {
        int e0 = 32 * ks + 16 * (i >> 1) + 4 * g + 2 * (i & 1);
        v[i] = (u32)wos[e0 * 88 + 16 * ct + c16] | ((u32)wos[(e0 + 1) * 88 + 16 * ct + c16] << 16);
      }
      st16(ws, OFF_WOF + (u32)(((h * 40 + 5 * part + ct) * 4 + ks) * 64 + l) * 16, 0, v);
    }
    u32x4 bf[5][4];
#pragma unroll
    for (int ct = 0; ct < 5; ++ct)
#pragma unroll
      for (int ks = 0; ks < 4; ++ks)
#pragma unroll
        for (int i = 0; i < 4; ++i) {
          int e0 = 32 * ks + 8 * g + 2 * i;
          bf[ct][ks][i] = (u32)wos[e0 * 88 + 16 * ct + c16] | ((u32)wos[(e0 + 1) * 88 + 16 * ct + c16] << 16);
        }
    for (int blk = w; blk < 20; blk += 8) {
      u32x4 a[2][4];
#pragma unroll
      for (int t = 0; t < 2; ++t)
#pragma unroll
        for (int ks = 0; ks < 4; ++ks) {
          int d = 32 * blk + 8 * (c16 >> 2) + 4 * t + (c16 & 3);
          a[t][ks] = *(const u32x4*)(p.wv + d * 4096 + h * 128 + 32 * ks + 8 * g);
        }
#pragma unroll
      for (int ct = 0; ct < 5; ++ct) {
        f32x4 c0 = {0, 0, 0, 0}, c1 = {0, 0, 0, 0};
#pragma unroll
        for (int ks = 0; ks < 4; ++ks) { c0 = mfma(a[0][ks], bf[ct][ks], c0); c1 = mfma(a[1][ks], bf[ct][ks], c1); }
        u32x4 o = {pk2(c0[0], c0[1]), pk2(c0[2], c0[3]), pk2(c1[0], c1[1]), pk2(c1[2], c1[3])};
        int G = 5 * part + ct, v = 2 * G + ((c16 >> 1) & 1), j = 2 * (c16 >> 2) + (c16 & 1);
        st16(ws, OFF_BF + (u32)((((v * 16 + (h >> 1)) * 20 + blk) * 64) + 16 * g + 8 * (h & 1) + j) * 16, 0, o);
      }
    }
  }
  {
    u16* ts = (u16*)(lds + 24576);
    for (int t = b; t < 1920; t += 256) {
      int mat = t / 640, rem = t % 640, h2 = rem / 20, ks = rem % 20;
      const u16* W = mat == 0 ? p.wq : (mat == 1 ? p.wk : p.wv);
      int row = tid >> 4, cc = tid & 15;
      u32x4 v = *(const u32x4*)(W + (32 * ks + row) * 4096 + h2 * 128 + cc * 8);
      __syncthreads();
      *(u32x4*)(ts + row * 136 + cc * 8) = v;
      __syncthreads();
      u32x4 o;
      for (int i = 0; i < 4; ++i)
        o[i] = (u32)ts[(8 * g + 2 * i) * 136 + 16 * w + c16] | ((u32)ts[(8 * g + 2 * i + 1) * 136 + 16 * w + c16] << 16);
      st16(ws, OFF_WF + (u32)(((((mat * 32 + h2) * 8 + w) * 20 + ks) * 64) + l) * 16, 0, o);
    }
  }
}

__global__ __launch_bounds__(512) void k_init(Params p) {
  const int b = blockIdx.x, tid = threadIdx.x;
  const rsrc_t ws = mk_rsrc(p.ws, WS_BYTES);
  const u32 cb = comm_base(p.ws);
  for (int i = 0; i < 2; ++i) {
    u32 o = (u32)(b * 1024 + i * 512 + tid) * 16;
    u32x4 v = {SENT, SENT, SENT, SENT};
    if (o >= STEP_BYTES) st16c(ws, cb + o, 0, v);
  }
}
__global__ __launch_bounds__(512, 2) void k_bg(Params p) {
  extern __shared__ __attribute__((aligned(16))) unsigned char lds[];
  const int b = blockIdx.x, tid = threadIdx.x, l = tid & 63, c16 = l & 15, g = l >> 4;
  const int w = __builtin_amdgcn_readfirstlane(tid >> 6);
  const rsrc_t ws = mk_rsrc(p.ws, WS_BYTES);
  const int xcd = b & 7, idx = b >> 3, h = xcd * 4 + (idx >> 3), type = (idx >> 2) & 1, quarter = idx & 3;
  if (type == 0) {
    const int mat = w >> 2, et0 = 2 * (w & 3);
    u32x4 wt[2][20];
#pragma unroll
    for (int i = 0; i < 2; ++i)
#pragma unroll
      for (int ks = 0; ks < 20; ++ks) wt[i][ks] = ld16(ws, l * 16, OFF_WF + (u32)((((mat * 32 + h) * 8 + et0 + i) * 20 + ks) * 1024));
    for (int seg = quarter; seg < NSEG; seg += 4) {
      __syncthreads();
#pragma unroll
      for (int i = 0; i < 5; ++i) *(u32x4*)(lds + (i * 512 + tid) * 16) = ld16(ws, tid * 16, OFF_XF + (u32)(seg * 40960 + i * 8192));
      __syncthreads();
      f32x4 acc[2][2] = {};
#pragma unroll
      for (int ks = 0; ks < 20; ++ks)
#pragma unroll
        for (int tt = 0; tt < 2; ++tt) {
          u32x4 xb = *(const u32x4*)(lds + (tt * 20 + ks) * 1024 + l * 16);
          acc[0][tt] = mfma(wt[0][ks], xb, acc[0][tt]);
          acc[1][tt] = mfma(wt[1][ks], xb, acc[1][tt]);
        }
#pragma unroll
      for (int tt = 0; tt < 2; ++tt) {
        u32x4 o = {pk2(acc[0][tt][0], acc[0][tt][1]), pk2(acc[0][tt][2], acc[0][tt][3]), pk2(acc[1][tt][0], acc[1][tt][1]), pk2(acc[1][tt][2], acc[1][tt][3])};
        st16c(ws, l * 16, (mat ? OFF_KX : OFF_QX) + (u32)((((seg * 32 + h) * 2 + tt) * 4 + (w & 3)) * 1024), o);
      }
    }
  } else {
    u32x4 wv[20], wo[5][4];
#pragma unroll
    for (int ks = 0; ks < 20; ++ks) wv[ks] = ld16(ws, l * 16, OFF_WF + (u32)((((2 * 32 + h) * 8 + w) * 20 + ks) * 1024));
#pragma unroll
    for (int ct = 0; ct < 5; ++ct)
#pragma unroll
      for (int ks = 0; ks < 4; ++ks) wo[ct][ks] = ld16(ws, l * 16, OFF_WOF + (u32)(((h * 40 + 5 * w + ct) * 4 + ks) * 1024));
    unsigned char* vs = lds + 40960;
    const u32 uoff = (u32)(((c16 >> 1) & 1) * 16384 + h * 512 + g * 128 + (2 * (c16 >> 2) + (c16 & 1)) * 16);
    for (int seg = quarter; seg < NSEG; seg += 4) {
      __syncthreads();
#pragma unroll
      for (int i = 0; i < 5; ++i) *(u32x4*)(lds + (i * 512 + tid) * 16) = ld16(ws, tid * 16, OFF_XF + (u32)(seg * 40960 + i * 8192));
      __syncthreads();
      f32x4 av[2] = {};
#pragma unroll
      for (int ks = 0; ks < 20; ++ks)
#pragma unroll
        for (int tt = 0; tt < 2; ++tt) av[tt] = mfma(wv[ks], *(const u32x4*)(lds + (tt * 20 + ks) * 1024 + l * 16), av[tt]);
#pragma unroll
      for (int tt = 0; tt < 2; ++tt) {
        u32x2 o = {pk2(av[tt][0], av[tt][1]), pk2(av[tt][2], av[tt][3])};
        *(u32x2*)(vs + ((tt * 4 + (w >> 1)) * 64 + l) * 16 + (w & 1) * 8) = o;
      }
      __syncthreads();
      u32x4 vf[2][4];
#pragma unroll
      for (int tt = 0; tt < 2; ++tt)
#pragma unroll
        for (int ks = 0; ks < 4; ++ks) vf[tt][ks] = *(const u32x4*)(vs + ((tt * 4 + ks) * 64 + l) * 16);
#pragma unroll
      for (int ct = 0; ct < 5; ++ct) {
        f32x4 c0 = {0, 0, 0, 0}, c1 = {0, 0, 0, 0};
#pragma unroll
        for (int ks = 0; ks < 4; ++ks) { c0 = mfma(vf[0][ks], wo[ct][ks], c0); c1 = mfma(vf[1][ks], wo[ct][ks], c1); }
        u32x4 o = {pk2(c0[0], c0[1]), pk2(c0[2], c0[3]), pk2(c1[0], c1[1]), pk2(c1[2], c1[3])};
        st16c(ws, uoff, OFF_UX + (u32)((seg * 80 + 2 * (5 * w + ct)) * 16384), o);
      }
    }
  }
}

DEV float fmx(float a, float b) { return __builtin_elementwise_maximum(a, b); }
#define SCHED_FENCE() __builtin_amdgcn_sched_barrier(0)
DEV float dpp_ror8(float x) { return __uint_as_float(__builtin_amdgcn_update_dpp(0, (int)__float_as_uint(x), 0x128, 0xF, 0xF, true)); }
DEV float red_max(float m) {
  m = fmx(m, dpp_ror8(m));
  auto a = __builtin_amdgcn_permlane16_swap(__float_as_uint(m), __float_as_uint(m), false, false);
  m = fmx(__uint_as_float(a[0]), __uint_as_float(a[1]));
  auto b = __builtin_amdgcn_permlane32_swap(__float_as_uint(m), __float_as_uint(m), false, false);
  return fmx(__uint_as_float(b[0]), __uint_as_float(b[1]));
}
DEV float red_sum(float m) {
  m = m + dpp_ror8(m);
  auto a = __builtin_amdgcn_permlane16_swap(__float_as_uint(m), __float_as_uint(m), false, false);
  m = __uint_as_float(a[0]) + __uint_as_float(a[1]);
  auto b = __builtin_amdgcn_permlane32_swap(__float_as_uint(m), __float_as_uint(m), false, false);
  return __uint_as_float(b[0]) + __uint_as_float(b[1]);
}
DEV float red_max_g(float m) {
  auto a = __builtin_amdgcn_permlane16_swap(__float_as_uint(m), __float_as_uint(m), false, false);
  m = fmx(__uint_as_float(a[0]), __uint_as_float(a[1]));
  auto b = __builtin_amdgcn_permlane32_swap(__float_as_uint(m), __float_as_uint(m), false, false);
  return fmx(__uint_as_float(b[0]), __uint_as_float(b[1]));
}
DEV float red_sum_g(float m) {
  auto a = __builtin_amdgcn_permlane16_swap(__float_as_uint(m), __float_as_uint(m), false, false);
  m = __uint_as_float(a[0]) + __uint_as_float(a[1]);
  auto b = __builtin_amdgcn_permlane32_swap(__float_as_uint(m), __float_as_uint(m), false, false);
  return __uint_as_float(b[0]) + __uint_as_float(b[1]);
}
DEV void st4c(rsrc_t r, u32 vo, u32 so, u32 v) { __builtin_amdgcn_raw_buffer_store_b32(v, r, vo, so, 16); }

DEV u32 calibrate(rsrc_t ws, u32 cb, int idx, int l, u32* dbgp) {
  u32 res = 0;
  const u32 vo = (l == 0) ? 0u : 0x80000000u;
  for (int page = 0; page < 2; ++page) {
    u32 best[2] = {~0u, ~0u};
    for (int rep = 0; rep < 3; ++rep)
      for (int par = 0; par < 2; ++par) {
        const u32 so = cb + (u32)(page * 32 + idx / 12) * STEP_BYTES + 16384 + (u32)par * 8192 + 6144 + (u32)(idx % 12 + 4) * 128;
        u64 t0 = stamp_cyc();
        st4c(ws, vo, so, 0u);
        asm volatile("s_waitcnt vmcnt(0)" ::: "memory");
        u64 t1 = stamp_cyc();
        u32 d = (u32)(t1 - t0);
        if (par == 0) best[0] = d < best[0] ? d : best[0]; else best[1] = d < best[1] ? d : best[1];
      }
    if (best[1] < best[0]) res |= 1u << page;
  }
  return res;
}

#define D1 124
#define D2 10
#define D3 50
#define D4 10
#define DV 25
#define ROT 1
#define PSLEEP 10
#define DL 5
#define SV(x) (x)
#define SS(x) (x)
#define KSLEEP 0
#define DX 35
DEV u32 rtime() { return (u32)__builtin_amdgcn_s_memrealtime(); }
DEV void wait_until(u32 T) {
  u32 r = rtime();
  if ((int)(T - r) < 28) {
#pragma nounroll
    for (int i = 0; i < 64 && (int)(T - r) > 2; ++i) r = rtime();
    return;
  }
#pragma nounroll
  for (int i = 0; i < 4096 && (int)(T - r) > 35; ++i) r = rtime();
  u32 n;
  asm volatile(
    "s_memrealtime s[80:81]\n\ts_nop 2\n\ts_memrealtime s[82:83]\n\ts_nop 2\n\ts_memrealtime s[84:85]\n\ts_nop 2\n\ts_memrealtime s[86:87]\n\ts_nop 2\n\t"
    "s_memrealtime s[88:89]\n\ts_nop 2\n\ts_memrealtime s[90:91]\n\ts_nop 2\n\ts_memrealtime s[92:93]\n\ts_nop 2\n\ts_memrealtime s[94:95]\n\ts_waitcnt lgkmcnt(0)\n\t"
    "s_mov_b32 s81, 0\n\t"
    "s_cmp_eq_u32 s82, s80\n\ts_addc_u32 s81, s81, 0\n\t"
    "s_cmp_eq_u32 s84, s80\n\ts_addc_u32 s81, s81, 0\n\t"
    "s_cmp_eq_u32 s86, s80\n\ts_addc_u32 s81, s81, 0\n\t"
    "s_cmp_eq_u32 s88, s80\n\ts_addc_u32 s81, s81, 0\n\t"
    "s_cmp_eq_u32 s90, s80\n\ts_addc_u32 s81, s81, 0\n\t"
    "s_cmp_eq_u32 s92, s80\n\ts_addc_u32 s81, s81, 0\n\t"
    "s_cmp_eq_u32 s94, s80\n\ts_addc_u32 s81, s81, 0\n\t"
    "s_sub_u32 %0, %1, s80\n\t"
    "s_mul_i32 %0, %0, 3\n\t"
    "s_lshl1_add_u32 %0, s81, %0\n\t"
    "s_sub_i32 %0, %0, 64\n\t"
    "s_max_i32 %0, %0, 0\n\t"
    "s_min_i32 %0, %0, 63\n\t"
    "s_bitcmp1_b32 %0, 0\n\ts_cbranch_scc0 1f\n\ts_nop 5\n\t1:\n\t"
    "s_bitcmp1_b32 %0, 1\n\ts_cbranch_scc0 2f\n\ts_nop 7\n\t2:\n\t"
    "s_bitcmp1_b32 %0, 2\n\ts_cbranch_scc0 3f\n\ts_nop 11\n\t3:\n\t"
    "s_bitcmp1_b32 %0, 3\n\ts_cbranch_scc0 4f\n\ts_nop 15\n\ts_nop 3\n\t4:\n\t"
    "s_bitcmp1_b32 %0, 4\n\ts_cbranch_scc0 5f\n\ts_nop 15\n\ts_nop 15\n\ts_nop 3\n\t5:\n\t"
    "s_bitcmp1_b32 %0, 5\n\ts_cbranch_scc0 6f\n\ts_nop 15\n\ts_nop 15\n\ts_nop 15\n\ts_nop 15\n\ts_nop 3\n\t6:\n\t"
    : "=&s"(n) : "s"(T)
    : "scc", "memory", "s80", "s81", "s82", "s83", "s84", "s85", "s86", "s87", "s88", "s89", "s90", "s91", "s92", "s93", "s94", "s95");
}
#define FSTAMP(ev)
#define FVAL(ev, val)
constexpr u32 OFF_LATE = OFF_FLAGS;
DEV u32x2 load_late(rsrc_t ws, int s, int l) { return __builtin_amdgcn_raw_buffer_load_b64(ws, (u32)l * 8, OFF_LATE + (u32)s * 512, 16); }
template <int CTRL> DEV int dpp_i(int x) { return __builtin_amdgcn_update_dpp(x, x, CTRL, 0xF, 0xF, false); }
DEV u32 eff_time(u32x2 t, u32 Rm) {
  if (__all((t.x | t.y) == 0)) return Rm;
  const u32 ref = rtime();
  int d = max(t.x ? (int)(t.x - ref) : -(1 << 30), t.y ? (int)(t.y - ref) : -(1 << 30));
  d = max(d, dpp_i<0x121>(d));
  d = max(d, dpp_i<0x122>(d));
  d = max(d, dpp_i<0x124>(d));
  d = max(d, dpp_i<0x128>(d));
  int d0 = __builtin_amdgcn_readlane(d, 0), d1 = __builtin_amdgcn_readlane(d, 16), d2 = __builtin_amdgcn_readlane(d, 32);
  d0 = max(d0, max(d1, d2));
  return ref + (u32)d0 + D4 + DX;
}

DEV u32 ld4c(rsrc_t r, u32 vo, u32 so) { return __builtin_amdgcn_raw_buffer_load_b32(r, vo, so, 16); }
DEV u32 gather_mem(rsrc_t ws, u32 sbp, int wr, unsigned char* dA, unsigned char* dB, int l, bool& dead) {
  u32x4 a = ld16c(ws, (u32)l * 16, sbp + (u32)wr * 1024);
  u32 b = ld4c(ws, (u32)l * 4, sbp + 16384 + (u32)wr * 256);
  SCHED_FENCE();
  *(u32x4*)dA = a;
  *(u32*)dB = b;
  u32 m = umax3(umax3(a.x, a.y, a.z), a.w, b);
  u32 spins = 0;
  if (__builtin_expect(__any(m == SENT), 0)) {
#pragma nounroll
    while (!dead) {
      ++spins;
      asm volatile("" ::: "memory");
      a = ld16c(ws, (u32)l * 16, sbp + (u32)wr * 1024);
      b = ld4c(ws, (u32)l * 4, sbp + 16384 + (u32)wr * 256);
      m = umax3(umax3(a.x, a.y, a.z), a.w, b);
      if (__all(m != SENT)) break;
      if (spins > SPIN_CAP) dead = true;
    }
    *(u32x4*)dA = a;
    *(u32*)dB = b;
  }
  return spins;
}

constexpr float CEXP = 0.08838834764831845f * 1.4426950408889634f;
constexpr float NEGS = -1.0e9f / 0.08838834764831845f;

constexpr int QS = 288;
DEV void project2(const unsigned char* memf, const u32x8 (&wt)[2][10], int sidx, int l, unsigned char* dst) {
  u32x4 a[10];
#pragma unroll
  for (int ks = 0; ks < 10; ++ks) a[ks] = *(const u32x4*)(memf + ks * 1024 + l * 16);
  SCHED_FENCE();
  f32x4 c0 = {0, 0, 0, 0}, c1 = {0, 0, 0, 0};
#pragma unroll
  for (int ks = 0; ks < 10; ++ks) {
    c0 = smfma(a[ks], wt[0][ks], c0, sidx);
    c1 = smfma(a[ks], wt[1][ks], c1, sidx);
  }
  *(__bf16*)(dst) = (__bf16)(c0[0] + c0[1]);
  *(__bf16*)(dst + QS) = (__bf16)(c0[2] + c0[3]);
  *(__bf16*)(dst + 8) = (__bf16)(c1[0] + c1[1]);
  *(__bf16*)(dst + 8 + QS) = (__bf16)(c1[2] + c1[3]);
}

DEV void score_role(const Params& p, rsrc_t ws, u32 cb, int h, unsigned char* lds) {
  const int tid = threadIdx.x, l = tid & 63, c16 = l & 15, g = l >> 4;
  const int w = __builtin_amdgcn_readfirstlane(tid >> 6);
  unsigned char* memf = lds;
  unsigned char* qs = lds + 10240;
  unsigned char* kms = qs + 8 * QS;
  u32* ctl = (u32*)(lds + 15360);
  unsigned char* kq = lds + 16384;
  u32x8 wt[2][10];
#pragma unroll
  for (int i = 0; i < 2; ++i)
#pragma unroll
    for (int ks = 0; ks < 10; ++ks) {
      u32 o = OFF_WF + (u32)(((((w >> 2) * 32 + h) * 8 + 2 * (w & 3) + i) * 20 + 2 * ks) * 1024);
      wt[i][ks] = cat(ld16(ws, l * 16, o), ld16(ws, l * 16 + 1024, o));
    }
  {
    u32x4 a = ld16c(ws, tid * 16, OFF_KX + (u32)(h * 8192));
    u32x4 b = ld16c(ws, tid * 16, OFF_QX + (u32)(h * 8192));
    *(u32x4*)(kq + tid * 16) = a;
    *(u32x4*)(kq + 8192 + tid * 16) = b;
  }
  __builtin_amdgcn_s_waitcnt(0x0F70);
  __syncthreads();
  if (w == 7) { u32 r = calibrate(ws, cb, h, l, (u32*)(lds + 92160) + 63 * 8 + 4); if (l == 0) ctl[0] = r; }
  __syncthreads();
  const u32 parmask = __builtin_amdgcn_readfirstlane(ctl[0]);
  const int sidx = (l & 1) ? 0xEEEE : 0x4444;
  unsigned char* qk_w = qs + (w >> 2) * 8 * QS + 2 * g * QS + (32 * (w & 3) + 8 * (c16 >> 2) + (c16 & 3)) * 2;
  const unsigned char* km_r = kms + (2 * (c16 >> 2) + (c16 & 1)) * QS + g * 16;
  const int wr = (w + ROT * (h)) & 7;
  unsigned char* gA = memf + wr * 1024 + g * 256 + (l & 7) * 32 + ((l >> 3) & 1) * 16;
  unsigned char* gB = memf + 8192 + wr * 256 + ((l >> 2) & 7) * 32 + (l >> 5) * 16 + (l & 3) * 4;
  bool dead = false;
  if (w == 0) {
    const unsigned char* q_r = qs + (c16 & 7) * QS + g * 16;
    const bool hb = c16 >= 8;
    const u32 xo = (u32)(((h >> 1) & 7) * 1024 + g * 256 + (h & 1) * 128 + (c16 & 7) * 16 + (c16 >> 3) * 8);
    const u32 mo = (u32)((c16 >> 3) * 8192 + h * 128 + (c16 & 7) * 16 + g * 4);
    for (int s = 0; s < NSEG; ++s) {
      const u32 sb = cb + (u32)s * STEP_BYTES;
      const u32 par = ((parmask >> (s >> 5)) & 1) * 8192;
      const unsigned char* kxs = kq + (s & 1) * 16384;
      CDECL;
      __syncthreads();
      CSTAMP(0);
      { u32 sp = gather_mem(ws, sb + par, wr, gA, gB, l, dead); COUNT(5, sp); }
      CSTAMP(1);
      __syncthreads();
      CSTAMP(2);
      STAMP(2);
      project2(memf, wt, sidx, l, qk_w);
      CSTAMP(3);
      u32x4 kx[8];
#pragma unroll
      for (int i = 0; i < 8; ++i) kx[i] = *(const u32x4*)(kxs + i * 1024 + l * 16);
      __syncthreads();
      STAMP(6);
      CSTAMP(4);
      if (s == NSEG - 1) break;
      u32x4 qf[4], km[4];
#pragma unroll
      for (int ks = 0; ks < 4; ++ks) { qf[ks] = *(const u32x4*)(q_r + ks * 64); km[ks] = *(const u32x4*)(km_r + ks * 64); }
      SCHED_FENCE();
      f32x4 s0 = {0, 0, 0, 0}, s1 = {0, 0, 0, 0}, s2 = {0, 0, 0, 0};
#pragma unroll
      for (int ks = 0; ks < 4; ++ks) {
        s1 = mfma(kx[ks], qf[ks], s1);
        s2 = mfma(kx[4 + ks], qf[ks], s2);
        s0 = mfma(km[ks], qf[ks], s0);
      }
      float v[4];
#pragma unroll
      for (int r = 0; r < 4; ++r) v[r] = hb ? s2[r] : s1[r];
      float m = fmx(fmx(fmx(v[0], v[1]), v[2]), fmx(fmx(v[3], s0[0]), s0[1]));
      m = red_max(m);
      const float mc = -m * CEXP;
#pragma unroll
      for (int r = 0; r < 4; ++r) v[r] = __builtin_amdgcn_exp2f(fmaf(v[r], CEXP, mc));
      const float e0 = __builtin_amdgcn_exp2f(fmaf(s0[0], CEXP, mc)), e1 = __builtin_amdgcn_exp2f(fmaf(s0[1], CEXP, mc));
      float sum = ((v[0] + v[1]) + (v[2] + v[3])) + (hb ? e1 : e0);
      sum = red_sum(sum);
      const float inv = __builtin_amdgcn_rcpf(sum);
      u32x2 px = {pk2s(v[0] * inv, v[1] * inv), pk2s(v[2] * inv, v[3] * inv)};
      const u32 pm = pk2s(e0 * inv, e1 * inv);
      const u32 xs = sb + (u32)(2 + (h >> 4)) * 16384;
      st8c(ws, xo, xs + par, px);
      st4c(ws, mo, sb + 16384 + 2048, pm);
      st8c(ws, xo, xs + (par ^ 8192), px);
      FSTAMP(0);
      STAMP(4);
      CSTAMP(6);
      CDUMP;
    }
  } else if (w < 3) {
    const int tx = 16 * (w - 1) + c16;
    for (int s = 0; s < NSEG; ++s) {
      const unsigned char* kxs = kq + (s & 1) * 16384;
      const u32 sb = cb + (u32)s * STEP_BYTES;
      const u32 par = ((parmask >> (s >> 5)) & 1) * 8192;
      __syncthreads();
      { u32 sp = gather_mem(ws, sb + par, wr, gA, gB, l, dead); COUNT(5, sp); }
      __syncthreads();
      project2(memf, wt, sidx, l, qk_w);
      __syncthreads();
      f32x4 s0 = {0, 0, 0, 0}, s1 = {0, 0, 0, 0}, s2 = {0, 0, 0, 0};
#pragma unroll
      for (int ks = 0; ks < 4; ++ks) {
        u32x4 qf = *(const u32x4*)(kxs + 8192 + ((w - 1) * 4 + ks) * 1024 + l * 16);
        s0 = mfma(*(const u32x4*)(km_r + ks * 64), qf, s0);
        s1 = mfma(*(const u32x4*)(kxs + ks * 1024 + l * 16), qf, s1);
        s2 = mfma(*(const u32x4*)(kxs + (4 + ks) * 1024 + l * 16), qf, s2);
      }
      float m = fmx(s0[0], s0[1]);
#pragma unroll
      for (int r = 0; r < 4; ++r) {
        s1[r] = (4 * g + r <= tx) ? s1[r] : NEGS;
        s2[r] = (16 + 4 * g + r <= tx) ? s2[r] : NEGS;
        m = fmx(m, fmx(s1[r], s2[r]));
      }
      m = red_max_g(m);
      const float mc = -m * CEXP;
      const float e0 = __builtin_amdgcn_exp2f(fmaf(s0[0], CEXP, mc)), e1 = __builtin_amdgcn_exp2f(fmaf(s0[1], CEXP, mc));
      float sum = e0 + e1;
#pragma unroll
      for (int r = 0; r < 4; ++r) {
        s1[r] = __builtin_amdgcn_exp2f(fmaf(s1[r], CEXP, mc));
        s2[r] = __builtin_amdgcn_exp2f(fmaf(s2[r], CEXP, mc));
        sum += s1[r] + s2[r];
      }
      sum = red_sum_g(sum);
      const float inv = __builtin_amdgcn_rcpf(sum);
      u32x4 px = {pk2(s1[0] * inv, s1[1] * inv), pk2(s1[2] * inv, s1[3] * inv), pk2(s2[0] * inv, s2[1] * inv), pk2(s2[2] * inv, s2[3] * inv)};
      st16c(ws, l * 16, OFF_PXX + (u32)(((s * 32 + h) * 2 + (w - 1)) * 1024), px);
      st4c(ws, c16 * 16 + g * 4, OFF_PXM + (u32)((((s * 2 + (w - 1)) * 32 + h) * 16) * 16), pk2(e0 * inv, e1 * inv));
    }
  } else {
    u32 Rm = 0;
    for (int s = 0; s < NSEG; ++s) {
      const u32 sb = cb + (u32)s * STEP_BYTES;
      const u32 par = ((parmask >> (s >> 5)) & 1) * 8192;
      if (s >= 2 && w == 3) wait_until(Rm);
      __syncthreads();
      { u32 sp = gather_mem(ws, sb + par, wr, gA, gB, l, dead); COUNT(5, sp); }
      __syncthreads();
      u32x2 tg;
      if (w == 3) tg = load_late(ws, s, l);
      project2(memf, wt, sidx, l, qk_w);
      __syncthreads();
      if (w == 3) { STAMPV(0, Rm); FVAL(1, Rm); Rm = eff_time(tg, Rm) + D1 + D2 + D3 + D4; u32x2 warm = load_late(ws, s + 1, l); asm volatile("" :: "v"(warm)); }
      else {
        const int sn = SS(s + 1 < NSEG ? s + 1 : s);
        __builtin_amdgcn_s_sleep(KSLEEP);
        unsigned char* kn = kq + ((s + 1) & 1) * 16384 + (w - 4) * 4096 + l * 16;
        u32x4 pk[4];
#pragma unroll
        for (int i = 0; i < 4; ++i) pk[i] = ld16c(ws, l * 16 + i * 1024, ((w & 2) ? OFF_QX : OFF_KX) + (u32)((sn * 32 + h) * 8192 + (w & 1) * 4096));
#pragma unroll
        for (int i = 0; i < 4; ++i) *(u32x4*)(kn + i * 1024) = pk[i];
      }
    }
  }
}

DEV u32x4 finish_mem(f32x4 acc) {
  auto x = __builtin_amdgcn_permlane32_swap(__float_as_uint(acc[0]), __float_as_uint(acc[2]), false, false);
  auto y = __builtin_amdgcn_permlane32_swap(__float_as_uint(acc[1]), __float_as_uint(acc[3]), false, false);
  float t0, t1;
  asm("s_nop 1\n\tv_add_f32_dpp %0, %2, %3 row_ror:8 row_mask:0xf bank_mask:0xf\n\tv_add_f32_dpp %1, %4, %5 row_ror:8 row_mask:0xf bank_mask:0xf"
      : "=&v"(t0), "=&v"(t1) : "v"(x[1]), "v"(x[0]), "v"(y[1]), "v"(y[0]));
  const u32 d = pk2s(t0, t1);
  auto s16 = __builtin_amdgcn_permlane16_swap(d, d, false, false);
  auto sa = __builtin_amdgcn_permlane32_swap(s16[0], s16[0], false, false);
  auto sb = __builtin_amdgcn_permlane32_swap(s16[1], s16[1], false, false);
  return (u32x4){sa[0], sa[1], sb[0], sb[1]};
}

DEV void value_role(const Params& p, rsrc_t ws, u32 cb, int v, unsigned char* lds) {
  const int tid = threadIdx.x, l = tid & 63, c16 = l & 15, g = l >> 4;
  const int w = __builtin_amdgcn_readfirstlane(tid >> 6);
  unsigned char* memf = lds;
  unsigned char* um = lds + 10240;
  u32* ctl = (u32*)(lds + 15360);
  unsigned char* w0s = lds + 16384;
  const int sidx = (l & 1) ? 0xEEEE : 0x4444;
  const u32 um_w = (u32)((4 * w + (c16 >> 3)) * 128 + (c16 & 7) * 16 + g * 4);
  const int wr = (w + ROT * v) & 7;
  unsigned char* gA = memf + wr * 1024 + g * 256 + (l & 7) * 32 + ((l >> 3) & 1) * 16;
  unsigned char* gB = memf + 8192 + wr * 256 + ((l >> 2) & 7) * 32 + (l >> 5) * 16 + (l & 3) * 4;
  bool dead = false;
  if (w == 0) {
#pragma unroll
    for (int i = 0; i < 40; ++i) *(u32x4*)(w0s + i * 1024 + l * 16) = ld16(ws, l * 16, OFF_BF + (u32)((v * 16 * 20 + i) * 1024));
    if (l == 0) ctl[2] = 0;
    __syncthreads();
    __syncthreads();
    const u32 parmask = __builtin_amdgcn_readfirstlane(ctl[0]);
    const u32 offX = (u32)((c16 >> 3) * 512 + g * 128 + (c16 & 7) * 16);
    const u32 L = (u32)(v * 128 + (c16 & 7) * 16);
    const u32 pub = (c16 < 8 && !(g & 1)) ? (L >> 13) * 16384 + (u32)(g >> 1) * 8192 + (L & 8191) : 0x80000000u;
    u32 Rm = 0;
    const int prot = ROT * v;
    for (int s = 0; s < NSEG; ++s) {
      const u32 sb = cb + (u32)s * STEP_BYTES;
      const u32 par = ((parmask >> (s >> 5)) & 1) * 8192;
      CDECL;
      u32x4 uf[20];
#pragma unroll
      for (int i = 0; i < 16; ++i)
        uf[i] = ld16c(ws, offX + (i & 3) * 1024, OFF_UX + (u32)((SV(s) * 80 + v) * 16384) + (u32)(((i >> 2) + prot) & 3) * 4096);
      SCHED_FENCE();
      if (s >= 2) wait_until(Rm + DV);
      __syncthreads();
      { u32 sp = gather_mem(ws, sb + par, wr, gA, gB, l, dead); COUNT(5, sp); if (sp && l == 0) ctl[2] = (u32)s + 1; }
      __syncthreads();
      FVAL(4, rtime());
      STAMP(0);
      const u32x2 tg = load_late(ws, s, l);
      const u32 Rp = eff_time(tg, Rm) + D1 + D2;
      Rm = Rp + D3 + D4;
      if (s == NSEG - 1) { __syncthreads(); break; }
      bool ontime = s >= 1 && (int)(Rp - rtime()) > 0;
      u32 so[5];
#pragma unroll
      for (int i = 0; i < 4; ++i) { const u32 qd = (u32)(i + prot) & 3; so[i] = sb + par + (2 + (qd >> 1)) * 16384 + (qd & 1) * 4096; }
      so[4] = sb + par + 16384 + 2048;
      asm volatile("" : "+s"(so[0]), "+s"(so[1]), "+s"(so[2]), "+s"(so[3]), "+s"(so[4]));
      CSTAMP(0);
      FVAL(0, rtime());
      FVAL(1, Rp - D1 - D2);
      FVAL(2, Rp);
      SCHED_FENCE();
      if (s >= 1) wait_until(Rp);
      SCHED_FENCE();
      STAMP(1);
      CSTAMP(1);
      u32x4 pf[20];
#pragma unroll
      for (int i = 0; i < 20; ++i) { pf[i] = ld16c(ws, (u32)l * 16 + (i & 3) * 1024, so[i >> 2]); SCHED_FENCE(); }
      __syncthreads();
      FVAL(5, rtime());
#pragma unroll
      for (int q = 0; q < 4; ++q) uf[16 + q] = *(const u32x4*)(um + q * 1024 + l * 16);
      SCHED_FENCE();
      f32x4 acc = {0, 0, 0, 0};
#pragma unroll
      for (int i = 0; i < 20; ++i) { acc = mfma(uf[i], pf[i], acc); SCHED_FENCE(); }
      CSTAMP(2);
      u32x4 o = finish_mem(acc);
      if (__builtin_expect(__any(acc[0] != acc[0]), 0)) {
        u32 spins = 0;
#pragma nounroll
        while (!dead) {
          u32 m = 0;
#pragma unroll
          for (int i = 0; i < 20; ++i) m = umax3(umax3(m, pf[i].x, pf[i].y), pf[i].z, pf[i].w);
          if (__all(m != SENT)) break;
          ontime = false;
          if (++spins > SPIN_CAP) dead = true;
          asm volatile("" ::: "memory");
#pragma unroll
          for (int i = 0; i < 20; ++i) pf[i] = ld16c(ws, (u32)l * 16 + (i & 3) * 1024, so[i >> 2]);
        }
        STAMPV(4, spins);
        acc = (f32x4){0, 0, 0, 0};
#pragma unroll
        for (int i = 0; i < 20; ++i) acc = mfma(uf[i], pf[i], acc);
        o = finish_mem(acc);
      }
      if (__builtin_expect(!ontime, 0)) st4c(ws, l == 0 ? 0u : 0x80000000u, OFF_LATE + (u32)(s + 1) * 512 + (u32)v * 4, (rtime() + DL) | 1u);
      st16c(ws, pub, sb + STEP_BYTES, o);
      FSTAMP(3);
      CSTAMP(3);
      STAMP(3);
      CDUMP;
    }
  } else {
    u32x8 wb[2][10];
#pragma unroll
    for (int t = 0; t < 2; ++t)
#pragma unroll
      for (int ks = 0; ks < 10; ++ks) {
        u32 o = OFF_BF + (u32)(((v * 16 + 2 * w + t) * 20 + 2 * ks) * 1024);
        wb[t][ks] = cat(ld16(ws, l * 16, o), ld16(ws, l * 16 + 1024, o));
      }
    __builtin_amdgcn_s_waitcnt(0x0F70);
    __syncthreads();
    if (w == 7) { u32 r = calibrate(ws, cb, 32 + v, l, (u32*)(lds + 92160) + 63 * 8 + 4); if (l == 0) ctl[0] = r; }
    __syncthreads();
    const u32 parmask = __builtin_amdgcn_readfirstlane(ctl[0]);
    if (w == 4) {
      for (int s = 0; s < NSEG; ++s) {
        const u32 sb = cb + (u32)s * STEP_BYTES;
        const u32 par = ((parmask >> (s >> 5)) & 1) * 8192;
        u32x8 sb0[3], sb1[3];
#pragma unroll
        for (int ks = 0; ks < 2; ++ks) {
          sb0[ks] = cat(*(const u32x4*)(w0s + (2 * ks) * 1024 + l * 16), *(const u32x4*)(w0s + (2 * ks + 1) * 1024 + l * 16));
          sb1[ks] = cat(*(const u32x4*)(w0s + (20 + 2 * ks) * 1024 + l * 16), *(const u32x4*)(w0s + (21 + 2 * ks) * 1024 + l * 16));
        }
        __syncthreads();
        { u32 sp = gather_mem(ws, sb + par, wr, gA, gB, l, dead); COUNT(5, sp); if (sp && l == 0) ctl[2] = (u32)s + 1; }
        __syncthreads();
        u32x4 a[3];
        a[0] = *(const u32x4*)(memf + l * 16);
        a[1] = *(const u32x4*)(memf + 1024 + l * 16);
        f32x4 a0 = {0, 0, 0, 0}, a1 = {0, 0, 0, 0}, a2 = {0, 0, 0, 0}, a3 = {0, 0, 0, 0};
#pragma unroll
        for (int ks = 0; ks < 10; ++ks) {
          if (ks + 2 < 10) {
            const int kn = ks + 2;
            a[kn % 3] = *(const u32x4*)(memf + kn * 1024 + l * 16);
            sb0[kn % 3] = cat(*(const u32x4*)(w0s + (2 * kn) * 1024 + l * 16), *(const u32x4*)(w0s + (2 * kn + 1) * 1024 + l * 16));
            sb1[kn % 3] = cat(*(const u32x4*)(w0s + (20 + 2 * kn) * 1024 + l * 16), *(const u32x4*)(w0s + (21 + 2 * kn) * 1024 + l * 16));
          }
          SCHED_FENCE();
          a0 = smfma(a[ks % 3], wb[0][ks], a0, sidx);
          a1 = smfma(a[ks % 3], wb[1][ks], a1, sidx);
          a2 = smfma(a[ks % 3], sb0[ks % 3], a2, sidx);
          a3 = smfma(a[ks % 3], sb1[ks % 3], a3, sidx);
          SCHED_FENCE();
        }
        *(u32*)(um + um_w) = pk2(a0[0] + a0[1], a0[2] + a0[3]);
        *(u32*)(um + um_w + 256) = pk2(a1[0] + a1[1], a1[2] + a1[3]);
        *(u32*)(um + um_w - 2048) = pk2(a2[0] + a2[1], a2[2] + a2[3]);
        *(u32*)(um + um_w - 2048 + 256) = pk2(a3[0] + a3[1], a3[2] + a3[3]);
        __syncthreads();
      }
    } else {
      for (int s = 0; s < NSEG; ++s) {
        const u32 sb = cb + (u32)s * STEP_BYTES;
        const u32 par = ((parmask >> (s >> 5)) & 1) * 8192;
        __syncthreads();
        { u32 sp = gather_mem(ws, sb + par, wr, gA, gB, l, dead); COUNT(5, sp); if (sp && l == 0) ctl[2] = (u32)s + 1; }
        __syncthreads();
        u32x4 a[10];
#pragma unroll
        for (int ks = 0; ks < 10; ++ks) a[ks] = *(const u32x4*)(memf + ks * 1024 + l * 16);
        SCHED_FENCE();
        f32x4 a0 = {0, 0, 0, 0}, a1 = {0, 0, 0, 0};
#pragma unroll
        for (int ks = 0; ks < 10; ++ks) {
          a0 = smfma(a[ks], wb[0][ks], a0, sidx);
          a1 = smfma(a[ks], wb[1][ks], a1, sidx);
        }
        *(u32*)(um + um_w) = pk2(a0[0] + a0[1], a0[2] + a0[3]);
        *(u32*)(um + um_w + 256) = pk2(a1[0] + a1[1], a1[2] + a1[3]);
        __syncthreads();
        __builtin_amdgcn_s_sleep(PSLEEP);
        if (w == 7) { u32x2 warm = load_late(ws, s + 1, l); asm volatile("" :: "v"(warm)); }
        if (w == 1 || w == 2) {
#pragma unroll
          for (int i = 0; i < 2; ++i) {
            u32 o = (u32)(((w - 1) * 2 + i) * 1024 + l * 16);
            st16c(ws, o, OFF_UM + (u32)((s * 80 + v) * 4096), *(const u32x4*)(um + o));
          }
        }
      }
    }
  }
}

__global__ __launch_bounds__(512, 2) void k_chain(Params p) {
  extern __shared__ __attribute__((aligned(16))) unsigned char lds[];
  const rsrc_t ws = mk_rsrc(p.ws, WS_BYTES);
  const u32 cb = comm_base(p.ws);
  const int b = blockIdx.x, idx = (b & 7) * 32 + (b >> 3);
  if (idx < 32) score_role(p, ws, cb, idx, lds);
  else if (idx < 112) value_role(p, ws, cb, idx - 32, lds);
}

__global__ __launch_bounds__(512, 2) void k_out(Params p) {
  extern __shared__ __attribute__((aligned(16))) unsigned char lds[];
  const int tid = threadIdx.x, l = tid & 63, c16 = l & 15, g = l >> 4;
  const int w = __builtin_amdgcn_readfirstlane(tid >> 6);
  const rsrc_t ws = mk_rsrc(p.ws, WS_BYTES);
  const int s = blockIdx.x / 5, cg = blockIdx.x % 5, G = 8 * cg + w;
#pragma unroll
  for (int i = 0; i < 8; ++i) *(u32x4*)(lds + (i * 512 + tid) * 16) = ld16c(ws, tid * 16, OFF_PXX + (u32)(s * 65536 + i * 8192));
#pragma unroll
  for (int i = 0; i < 2; ++i) *(u32x4*)(lds + 65536 + (i * 512 + tid) * 16) = ld16c(ws, tid * 16, OFF_PXM + (u32)(s * 16384 + i * 8192));
  __syncthreads();
  const int pp = (c16 >> 1) & 1, j = 2 * (c16 >> 2) + (c16 & 1);
  const u32 ox = (u32)(pp * 16384 + g * 128 + j * 16), om = (u32)(pp * 4096 + g * 128 + j * 16);
  f32x4 acc[2] = {};
#pragma unroll 8
  for (int h = 0; h < 32; ++h) {
    u32x4 a = ld16c(ws, ox + h * 512, OFF_UX + (u32)((s * 80 + 2 * G) * 16384));
#pragma unroll
    for (int tt = 0; tt < 2; ++tt) acc[tt] = mfma(a, *(const u32x4*)(lds + (h * 2 + tt) * 1024 + l * 16), acc[tt]);
  }
#pragma unroll
  for (int q = 0; q < 8; ++q) {
    u32x4 a = ld16c(ws, om + q * 512, OFF_UM + (u32)((s * 80 + 2 * G) * 4096));
#pragma unroll
    for (int tt = 0; tt < 2; ++tt) acc[tt] = mfma(a, *(const u32x4*)(lds + 65536 + ((tt * 32 + 4 * q + g) * 16 + c16) * 16), acc[tt]);
  }
#pragma unroll
  for (int tt = 0; tt < 2; ++tt) {
    u32x2 o = {pk2(acc[tt][0], acc[tt][1]), pk2(acc[tt][2], acc[tt][3])};
    *(u32x2*)(p.out + (32 * s + 16 * tt + c16) * 1280 + 640 + 16 * G + 4 * g) = o;
  }
}

typedef float f32x2 __attribute__((ext_vector_type(2)));
DEV f32x2 cmul(f32x2 a, f32x2 b) { return (f32x2){a.x * b.x - a.y * b.y, a.x * b.y + a.y * b.x}; }
DEV f32x2 cmulc(f32x2 a, f32x2 b) { return (f32x2){a.x * b.x + a.y * b.y, a.y * b.x - a.x * b.y}; }
template <bool INV> DEV f32x2 rot90(f32x2 a) { return INV ? (f32x2){-a.y, a.x} : (f32x2){a.y, -a.x}; }
template <bool INV> DEV void fft4(f32x2& a, f32x2& b, f32x2& c, f32x2& d) {
  f32x2 s0 = a + c, d0 = a - c, s1 = b + d, d1 = rot90<INV>(b - d);
  a = s0 + s1; b = d0 + d1; c = s0 - s1; d = d0 - d1;
}
template <bool INV> DEV f32x2 twc(f32x2 a, float c, float s) {
  return INV ? (f32x2){a.x * c - a.y * s, a.y * c + a.x * s} : (f32x2){a.x * c + a.y * s, a.y * c - a.x * s};
}
template <bool INV> DEV void fft16(f32x2 (&v)[16]) {
  constexpr float C1 = 0.92387953251128674f, S1 = 0.38268343236508977f, R = 0.70710678118654752f;
#pragma unroll
  for (int i = 0; i < 4; ++i) fft4<INV>(v[i], v[i + 4], v[i + 8], v[i + 12]);
  v[5] = twc<INV>(v[5], C1, S1);  v[9] = twc<INV>(v[9], R, R);     v[13] = twc<INV>(v[13], S1, C1);
  v[6] = twc<INV>(v[6], R, R);    v[10] = rot90<INV>(v[10]);       v[14] = twc<INV>(v[14], -R, R);
  v[7] = twc<INV>(v[7], S1, C1);  v[11] = twc<INV>(v[11], -R, R);  v[15] = twc<INV>(v[15], -C1, -S1);
#pragma unroll
  for (int k1 = 0; k1 < 4; ++k1) fft4<INV>(v[4 * k1], v[4 * k1 + 1], v[4 * k1 + 2], v[4 * k1 + 3]);
  f32x2 t;
#define SWP(a, b) t = v[a]; v[a] = v[b]; v[b] = t;
  SWP(1, 4) SWP(2, 8) SWP(3, 12) SWP(6, 9) SWP(7, 13) SWP(11, 14)
#undef SWP
}
template <bool INV> DEV void fft8(f32x2 (&v)[8]) {
  constexpr float R = 0.70710678118654752f;
  fft4<INV>(v[0], v[2], v[4], v[6]);
  fft4<INV>(v[1], v[3], v[5], v[7]);
  f32x2 o1 = twc<INV>(v[3], R, R), o2 = rot90<INV>(v[5]), o3 = twc<INV>(v[7], -R, R), o0 = v[1];
  f32x2 e0 = v[0], e1 = v[2], e2 = v[4], e3 = v[6];
  v[0] = e0 + o0; v[4] = e0 - o0; v[1] = e1 + o1; v[5] = e1 - o1; v[2] = e2 + o2; v[6] = e2 - o2; v[3] = e3 + o3; v[7] = e3 - o3;
}
DEV float bf2f(u16 x) { return __uint_as_float((u32)x << 16); }
DEV f32x2 ld8c(rsrc_t r, u32 vo, u32 so) { return __builtin_bit_cast(f32x2, __builtin_amdgcn_raw_buffer_load_b64(r, vo, so, 16)); }

DEV void fft_cols(const Params& p, rsrc_t ws, int q, unsigned char* lds) {
  const int tid = threadIdx.x, c = tid >> 7, t = tid & 127;
  f32x2* tw = (f32x2*)lds;
  f32x2* buf = (f32x2*)(lds + 16384) + c * (16 * 136);
  f32x2* fin = (f32x2*)(lds + 16384);
  __syncthreads();
  for (int m = tid; m < 2048; m += 512) { float sn, cs; sincospif((float)m * (1.0f / 1024.0f), &sn, &cs); tw[m] = (f32x2){cs, -sn}; }
  f32x2 keep[16];
  const int npass = (q == 0) ? 2 : 1;
  for (int pass = npass - 1; pass >= 0; --pass) {
    const int col = (pass == 1) ? 320 : 4 * q + c;
    f32x2 v[16];
#pragma unroll
    for (int a = 0; a < 16; ++a)
      v[a] = ld8c(ws, (u32)(t * 16 + (c & 1) * 8), OFF_FX + (u32)((2 * q + (c >> 1)) * 32768 + a * 2048));
    if (q == 0 && c == 0) {
#pragma unroll
      for (int a = 0; a < 16; ++a) v[a] = (f32x2){pass ? v[a].y : v[a].x, 0.f};
    }
    u32 wreg[16];
#pragma unroll
    for (int i = 0; i < 2; ++i)
#pragma unroll
      for (int kc = 0; kc < 8; ++kc) {
        const int pp = t + 128 * i, k = (pp >> 4) + 16 * (pp & 15) + 256 * kc;
        wreg[i * 8 + kc] = (u32)p.wr[k * 321 + col] | ((u32)p.wi[k * 321 + col] << 16);
      }
    __syncthreads();
    fft16<false>(v);
#pragma unroll
    for (int ka = 0; ka < 16; ++ka) buf[ka * 136 + t] = ka ? cmul(v[ka], tw[t * ka]) : v[ka];
    __syncthreads();
    const int ka2 = t >> 3, c2 = t & 7;
#pragma unroll
    for (int b = 0; b < 16; ++b) v[b] = buf[ka2 * 136 + 8 * b + c2];
    fft16<false>(v);
#pragma unroll
    for (int kb = 0; kb < 16; ++kb) buf[ka2 * 136 + 8 * kb + c2] = kb ? cmul(v[kb], tw[16 * c2 * kb]) : v[kb];
    __syncthreads();
#pragma unroll
    for (int i = 0; i < 2; ++i) {
      const int pp = t + 128 * i;
      f32x2* e = buf + (pp >> 4) * 136 + 8 * (pp & 15);
      f32x2 u[8];
#pragma unroll
      for (int j = 0; j < 8; ++j) u[j] = e[j];
      fft8<false>(u);
#pragma unroll
      for (int kc = 0; kc < 8; ++kc) {
        const u32 wv = wreg[i * 8 + kc];
        const f32x2 wf = {__uint_as_float(wv << 16) * (1.0f / (2048.0f * 640.0f)), __uint_as_float(wv & 0xFFFF0000u) * (1.0f / (2048.0f * 640.0f))};
        u[kc] = cmul(u[kc], wf);
      }
      fft8<true>(u);
#pragma unroll
      for (int j = 0; j < 8; ++j) e[j] = u[j];
    }
    __syncthreads();
#pragma unroll
    for (int kb = 0; kb < 16; ++kb) { f32x2 x = buf[ka2 * 136 + 8 * kb + c2]; v[kb] = kb ? cmulc(x, tw[16 * c2 * kb]) : x; }
    fft16<true>(v);
#pragma unroll
    for (int b = 0; b < 16; ++b) buf[ka2 * 136 + 8 * b + c2] = v[b];
    __syncthreads();
#pragma unroll
    for (int ka = 0; ka < 16; ++ka) { f32x2 x = buf[ka * 136 + t]; v[ka] = ka ? cmulc(x, tw[t * ka]) : x; }
    fft16<true>(v);
    if (pass == 1) {
#pragma unroll
      for (int a = 0; a < 16; ++a) keep[a] = v[a];
    } else {
      if (q == 0 && c == 0) {
#pragma unroll
        for (int a = 0; a < 16; ++a) v[a].y = keep[a].x;
      }
      __syncthreads();
#pragma unroll
      for (int a = 0; a < 16; ++a) fin[(128 * a + t) * 4 + c] = v[a];
      __syncthreads();
    }
  }
#pragma unroll
  for (int i = 0; i < 4; ++i) {
    const int n = tid + 512 * i;
    const f32x4 x0 = *(const f32x4*)(fin + n * 4), x1 = *(const f32x4*)(fin + n * 4 + 2);
    u32x4 hi = {pk2(x0[0], x0[1]), pk2(x0[2], x0[3]), pk2(x1[0], x1[1]), pk2(x1[2], x1[3])};
    u32x4 lo = {pk2(x0[0] - __uint_as_float(hi[0] << 16), x0[1] - __uint_as_float(hi[0] & 0xFFFF0000u)),
                pk2(x0[2] - __uint_as_float(hi[1] << 16), x0[3] - __uint_as_float(hi[1] & 0xFFFF0000u)),
                pk2(x1[0] - __uint_as_float(hi[2] << 16), x1[1] - __uint_as_float(hi[2] & 0xFFFF0000u)),
                pk2(x1[2] - __uint_as_float(hi[3] << 16), x1[3] - __uint_as_float(hi[3] & 0xFFFF0000u))};
    const u32 o = (u32)((n >> 4) * 40960 + (q >> 2) * 1024 + ((q & 3) * 16 + (n & 15)) * 16);
    st16c(ws, o, OFF_FY, hi);
    st16c(ws, o + 20480, OFF_FY, lo);
  }
}

__global__ __launch_bounds__(512, 2) void k_f1(Params p) {
  extern __shared__ __attribute__((aligned(16))) unsigned char lds[];
  const int tid = threadIdx.x, l = tid & 63, c16 = l & 15, g = l >> 4;
  const int w = __builtin_amdgcn_readfirstlane(tid >> 6);
  const rsrc_t ws = mk_rsrc(p.ws, WS_BYTES);
  const int type = blockIdx.x % 5, rg = blockIdx.x / 5, tile = type * 8 + w;
  u32x4 wt[2][20];
#pragma unroll
  for (int i = 0; i < 2; ++i)
#pragma unroll
    for (int ks = 0; ks < 20; ++ks) wt[i][ks] = ld16(ws, l * 16, OFF_FW1 + (u32)((tile * 40 + i * 20 + ks) * 1024));
  for (int seg = rg * 4; seg < rg * 4 + 4; ++seg) {
    __syncthreads();
#pragma unroll
    for (int i = 0; i < 5; ++i) *(u32x4*)(lds + (i * 512 + tid) * 16) = ld16(ws, tid * 16, OFF_AXF + (u32)(seg * 40960 + i * 8192));
    __syncthreads();
    f32x4 acc[2][2] = {};
#pragma unroll
    for (int ks = 0; ks < 20; ++ks)
#pragma unroll
      for (int tt = 0; tt < 2; ++tt) {
        u32x4 xb = *(const u32x4*)(lds + (tt * 20 + ks) * 1024 + l * 16);
        acc[0][tt] = mfma(wt[0][ks], xb, acc[0][tt]);
        acc[1][tt] = mfma(wt[1][ks], xb, acc[1][tt]);
      }
#pragma unroll
    for (int tt = 0; tt < 2; ++tt) {
      f32x4 o = acc[0][tt] + acc[1][tt];
      st16c(ws, (u32)(g * 32768 + c16 * 16), OFF_FX + (u32)(tile * 4 * 32768 + (seg * 32 + tt * 16) * 16), __builtin_bit_cast(u32x4, o));
    }
  }
}
__global__ __launch_bounds__(512, 2) void k_f2(Params p) {
  extern __shared__ __attribute__((aligned(16))) unsigned char lds[];
  const rsrc_t ws = mk_rsrc(p.ws, WS_BYTES);
  fft_cols(p, ws, blockIdx.x, lds);
}
__global__ __launch_bounds__(512, 2) void k_f3(Params p) {
  extern __shared__ __attribute__((aligned(16))) unsigned char lds[];
  const int tid = threadIdx.x, l = tid & 63, c16 = l & 15, g = l >> 4;
  const int w = __builtin_amdgcn_readfirstlane(tid >> 6);
  const rsrc_t ws = mk_rsrc(p.ws, WS_BYTES);
  const int type = blockIdx.x % 5, rg = blockIdx.x / 5, tile = type * 8 + w;
  u32x4 wt[2][20];
#pragma unroll
  for (int i = 0; i < 2; ++i)
#pragma unroll
    for (int ks = 0; ks < 20; ++ks) wt[i][ks] = ld16(ws, l * 16, OFF_FW3 + (u32)((tile * 40 + i * 20 + ks) * 1024));
  for (int rt = rg * 8; rt < rg * 8 + 8; ++rt) {
    __syncthreads();
#pragma unroll
    for (int i = 0; i < 5; ++i) *(u32x4*)(lds + (i * 512 + tid) * 16) = ld16c(ws, tid * 16, OFF_FY + (u32)(rt * 40960 + i * 8192));
    __syncthreads();
    f32x4 a0 = {0, 0, 0, 0}, a1 = {0, 0, 0, 0}, a2 = {0, 0, 0, 0};
#pragma unroll
    for (int ks = 0; ks < 20; ++ks) {
      u32x4 yh = *(const u32x4*)(lds + ks * 1024 + l * 16), yl = *(const u32x4*)(lds + (20 + ks) * 1024 + l * 16);
      a0 = mfma(wt[0][ks], yh, a0);
      a1 = mfma(wt[1][ks], yh, a1);
      a2 = mfma(wt[0][ks], yl, a2);
    }
    f32x4 o = a0 + (a1 + a2);
    u32x2 ov = {pk2(o[0], o[1]), pk2(o[2], o[3])};
    *(u32x2*)(p.out + (rt * 16 + c16) * 1280 + tile * 16 + 4 * g) = ov;
  }
}

extern "C" void kernel_launch(void* const* d_in, const int* in_sizes, int n_in, void* d_out, int out_size, void* d_ws, size_t ws_size,
                              hipStream_t stream) {
  Params p;
  p.ax = (const u16*)d_in[0]; p.wr = (const u16*)d_in[1]; p.wi = (const u16*)d_in[2];
  p.x = (const u16*)d_in[3]; p.mem0 = (const u16*)d_in[4]; p.wq = (const u16*)d_in[5]; p.wk = (const u16*)d_in[6];
  p.wv = (const u16*)d_in[7]; p.wo = (const u16*)d_in[8];
  p.out = (u16*)d_out; p.ws = (unsigned char*)d_ws;
  hipFuncSetAttribute((const void*)k_chain, hipFuncAttributeMaxDynamicSharedMemorySize, 96 * 1024);
  hipFuncSetAttribute((const void*)k_out, hipFuncAttributeMaxDynamicSharedMemorySize, 96 * 1024);
  k_prep<<<256, 512, 40 * 1024, stream>>>(p);
  k_bg<<<256, 512, 52 * 1024, stream>>>(p);
  k_chain<<<256, 512, 96 * 1024, stream>>>(p);
  k_out<<<320, 512, 80 * 1024, stream>>>(p);
  hipFuncSetAttribute((const void*)k_f2, hipFuncAttributeMaxDynamicSharedMemorySize, 96 * 1024);
  k_f1<<<80, 512, 40 * 1024, stream>>>(p);
  k_f2<<<80, 512, 88 * 1024, stream>>>(p);
  k_f3<<<80, 512, 40 * 1024, stream>>>(p);
}
```

```cpp
#include <hip/hip_runtime.h>
#include <hip/hip_bf16.h>
#include <stdint.h>
#define BAR4
#define BULK1

typedef unsigned short u16;
typedef unsigned int u32;
typedef unsigned long long u64;
typedef __bf16 bf16x2 __attribute__((ext_vector_type(2)));
typedef __bf16 bf16x8 __attribute__((ext_vector_type(8)));
typedef __bf16 bf16x16 __attribute__((ext_vector_type(16)));
typedef float f32x2 __attribute__((ext_vector_type(2)));
typedef float f32x4 __attribute__((ext_vector_type(4)));
typedef u32 u32x2 __attribute__((ext_vector_type(2)));
typedef u32 u32x4 __attribute__((ext_vector_type(4)));
typedef u32 u32x8 __attribute__((ext_vector_type(8)));
typedef __amdgpu_buffer_rsrc_t rsrc_t;
#define DEV __device__ __forceinline__

constexpr int NSEG = 64;
constexpr u32 MB = 1u << 20;
constexpr u32 OFF_WF = 6 * MB;
constexpr u32 OFF_WOF = 21 * MB;
constexpr u32 OFF_BF = 26 * MB;
constexpr u32 OFF_XF = 51 * MB;
constexpr u32 OFF_AXF = 54 * MB;
constexpr u32 OFF_KX = 57 * MB;
constexpr u32 OFF_QX = 73 * MB;
constexpr u32 OFF_UX = 89 * MB;
constexpr u32 OFF_UM = 169 * MB;
constexpr u32 OFF_PXX = 189 * MB;
constexpr u32 OFF_PXM = 193 * MB;
constexpr u32 OFF_FX = 194 * MB;
constexpr u32 OFF_FY = 200 * MB;
constexpr u32 OFF_FLAGS = 206 * MB;
constexpr u32 OFF_FW1 = 208 * MB;
constexpr u32 OFF_FW3 = 210 * MB;
constexpr u32 OFF_SIDE = 212 * MB;
constexpr u32 SIDE_STEP = 112 * 128;
constexpr u32 OFF_WT = 213 * MB;
constexpr u32 WS_BYTES = 216 * MB;
constexpr u32 STEP_BYTES = 65536;
constexpr u32 SENT = 0xFFFFFFFFu;
constexpr u32 SPIN_CAP = 1u << 16;
constexpr u32 OFF_PQK = OFF_FLAGS + 0xA000;
constexpr u32 OFF_PVU = OFF_FLAGS + 0xB000;
constexpr u32 OFF_PPX = OFF_FLAGS;
constexpr u32 OFF_PUM = OFF_FLAGS + 0x1000;
constexpr u32 OFF_TICKET = OFF_FLAGS + 0xB400;
constexpr u32 OFF_F1C = OFF_FLAGS + 0xB480;
constexpr u32 OFF_F2C = OFF_FLAGS + 0xB800;
constexpr u32 OFF_FWC = OFF_FLAGS + 0xB880;
constexpr u32 OFF_TICKET2 = OFF_FLAGS + 0xB900;
constexpr int LDS_MAIN = 97 * 1024;
#define S0 16
__host__ __device__ constexpr int seg_prod(int s) { return s < S0 ? (s & 1) : 0; }
__host__ __device__ constexpr u32 seg_need(int s) { return s < S0 ? (u32)(s >> 1) + 1 : (u32)(s - S0 / 2) + 1; }
__host__ __device__ constexpr int tile_seg(int par, int i) { return i < S0 / 2 ? 2 * i + par : i + S0 / 2; }

struct Params {
  const u16* ax; const u16* wr; const u16* wi;
  const u16* x; const u16* mem0; const u16* wq; const u16* wk; const u16* wv; const u16* wo;
  u16* out; unsigned char* ws;
};

DEV rsrc_t mk_rsrc(const void* p, u32 bytes) { return __builtin_amdgcn_make_buffer_rsrc((void*)p, 0, bytes, 0x00020000); }
DEV u32x4 ld16(rsrc_t r, u32 vo, u32 so) { return __builtin_amdgcn_raw_buffer_load_b128(r, vo, so, 0); }
DEV u32x4 ld16c(rsrc_t r, u32 vo, u32 so) { return __builtin_amdgcn_raw_buffer_load_b128(r, vo, so, 16); }
DEV u32x2 ld8c(rsrc_t r, u32 vo, u32 so) { return __builtin_amdgcn_raw_buffer_load_b64(r, vo, so, 16); }
DEV u32 ld4c(rsrc_t r, u32 vo, u32 so) { return __builtin_amdgcn_raw_buffer_load_b32(r, vo, so, 16); }
DEV void st16(rsrc_t r, u32 vo, u32 so, u32x4 v) { __builtin_amdgcn_raw_buffer_store_b128(v, r, vo + so, 0, 0); }
DEV void st16c(rsrc_t r, u32 vo, u32 so, u32x4 v) { __builtin_amdgcn_raw_buffer_store_b128(v, r, vo + so, 0, 16); }
DEV void st8c(rsrc_t r, u32 vo, u32 so, u32x2 v) { __builtin_amdgcn_raw_buffer_store_b64(v, r, vo, so, 16); }
DEV void st4c(rsrc_t r, u32 vo, u32 so, u32 v) { __builtin_amdgcn_raw_buffer_store_b32(v, r, vo, so, 16); }
DEV u32 pk2(float a, float b) { bf16x2 v = {(__bf16)a, (__bf16)b}; return __builtin_bit_cast(u32, v); }
DEV u32 pk2s(float a, float b) { u32 v = pk2(a, b); return v < 0xFFFFFFFEu ? v : 0xFFFFFFFEu; }
DEV u32 pk2lo(float a, float b, u32 hi) { return pk2(a - __uint_as_float(hi << 16), b - __uint_as_float(hi & 0xFFFF0000u)); }
DEV f32x4 mfma(u32x4 a, u32x4 b, f32x4 c) {
  return __builtin_amdgcn_mfma_f32_16x16x32_bf16(__builtin_bit_cast(bf16x8, a), __builtin_bit_cast(bf16x8, b), c, 0, 0, 0);
}
DEV f32x4 smfma(u32x4 a, u32x8 b, f32x4 c, int idx) {
  return __builtin_amdgcn_smfmac_f32_16x16x64_bf16(__builtin_bit_cast(bf16x8, a), __builtin_bit_cast(bf16x16, b), c, idx, 0, 0);
}
DEV u32x8 cat(u32x4 a, u32x4 b) { return __builtin_shufflevector(a, b, 0, 1, 2, 3, 4, 5, 6, 7); }
DEV u32 comm_base(const unsigned char* ws) { return (u32)((2 * MB - ((uintptr_t)ws & (2 * MB - 1))) & (2 * MB - 1)); }
DEV u64 stamp_cyc() { u64 t; asm volatile("s_memtime %0\n\ts_waitcnt lgkmcnt(0)" : "=s"(t) :: "memory"); return t; }
DEV u32 umax3(u32 a, u32 b, u32 c) { u32 m = a > b ? a : b; return m > c ? m : c; }
DEV float fmx(float a, float b) { return __builtin_elementwise_maximum(a, b); }
#define SCHED_FENCE() __builtin_amdgcn_sched_barrier(0)
#define VM_DRAIN() asm volatile("s_waitcnt vmcnt(0)" ::: "memory")
#define RAW_BARRIER() do { asm volatile("s_waitcnt lgkmcnt(0)" ::: "memory"); __builtin_amdgcn_s_barrier(); asm volatile("" ::: "memory"); } while (0)

#define STAMP(ev)
#define STAMPV(ev, val)
#define COUNT(ev, val)
#define GSTAMP(slot)

typedef short s16x4 __attribute__((ext_vector_type(4)));
#define LAS __attribute__((address_space(3)))
DEV u32x4 tr_frag(const unsigned char* a0, const unsigned char* a1) {
  const u32x2 x = __builtin_bit_cast(u32x2, __builtin_amdgcn_ds_read_tr16_b64_v4i16((LAS s16x4*)a0));
  const u32x2 y = __builtin_bit_cast(u32x2, __builtin_amdgcn_ds_read_tr16_b64_v4i16((LAS s16x4*)a1));
  return (u32x4){x.x, x.y, y.x, y.y};
}
__global__ __launch_bounds__(512) void k_prep(Params p) {
  extern __shared__ __attribute__((aligned(16))) unsigned char lds[];
  const int b = blockIdx.x, tid = threadIdx.x, l = tid & 63, c16 = l & 15, g = l >> 4;
  const int w = __builtin_amdgcn_readfirstlane(tid >> 6);
  const rsrc_t ws = mk_rsrc(p.ws, WS_BYTES);
  const int h = (b & 7) * 4 + (b >> 6), part = (b >> 3) & 7;
  const rsrc_t rv = mk_rsrc(p.wv, 640 * 8192), ro = mk_rsrc(p.wo, 640 * 8192);
  u32x4 wo_r[3];
#pragma unroll
  for (int i = 0; i < 3; ++i) {
    const int c = tid + 512 * i, e = c / 10, cc = c % 10;
    wo_r[i] = ld16(ro, c < 1280 ? (u32)(e * 1280 + cc * 16) : 0x80000000u, (u32)(h * 163840 + part * 160));
  }
  u32x4 a[3][2][4];
#pragma unroll
  for (int i = 0; i < 3; ++i)
#pragma unroll
    for (int t = 0; t < 2; ++t)
#pragma unroll
      for (int ks = 0; ks < 4; ++ks)
        a[i][t][ks] = ld16(rv, (u32)((8 * (c16 >> 2) + (c16 & 3)) * 8192 + g * 16 + ks * 64), (u32)((32 * (w + 8 * i) + 4 * t) * 8192 + h * 256));
  {
    const int q = b * 512 + tid;
    u32x4 t0[5];
#pragma unroll
    for (int i = 0; i < 5; ++i) {
      const int c = q + 131072 * i;
      t0[i] = *(const u32x4*)((c < 327680 ? p.wq : p.wk - 327680 * 8) + (size_t)c * 8);
    }
#pragma unroll
    for (int i = 0; i < 2; ++i) {
      const int r0 = q + 131072 * i;
      if (r0 < 163840) {
        const int seg = r0 / 2560, r = r0 % 2560, tt = r / 1280, ks = (r % 1280) >> 6, ln = r & 63;
        st16(ws, OFF_XF + (u32)r0 * 16, 0, *(const u32x4*)(p.x + (32 * seg + 16 * tt + (ln & 15)) * 640 + 32 * ks + 8 * (ln >> 4)));
      }
    }
#pragma unroll
    for (int i = 0; i < 5; ++i) asm volatile("" :: "v"(t0[i]));
  }
  {
    const u32 cb = comm_base(p.ws);
    for (int i = 0; i < 2; ++i) {
      u32 o = (u32)(b * 1024 + i * 512 + tid) * 16;
      u32x4 v = {SENT, SENT, SENT, SENT};
      if (o < STEP_BYTES) {
        u32 L = (o >> 14) * 8192 + (o & 8191);
        if (L < 10240) {
          int vv = L >> 7, t = (L >> 4) & 7;
          const u16* m = p.mem0 + t * 640 + 16 * (vv >> 1) + 2 * (vv & 1);
          for (int q = 0; q < 4; ++q) { u32 x = *(const u32*)(m + 4 * q); v[q] = x < 0xFFFFFFFEu ? x : 0xFFFFFFFEu; }
        }
      }
      st16c(ws, cb + o, 0, v);
    }
    if (b < 6) st16c(ws, OFF_FLAGS + (u32)(b * 512 + tid) * 16, 0, (u32x4){0, 0, 0, 0});
    if (b >= 128 && b < 240) st16c(ws, OFF_SIDE + (u32)((b - 128) * 512 + tid) * 16, 0, (u32x4){SENT, SENT, SENT, SENT});
  }
#pragma unroll
  for (int i = 0; i < 3; ++i) {
    const int c = tid + 512 * i, e = c / 10, cc = c % 10;
    if (c < 1280) *(u32x4*)(lds + e * 160 + (e >> 3) * 128 + cc * 16) = wo_r[i];
  }
  __syncthreads();
  u32x4 bf[5][4];
  const unsigned char* rp = lds + (8 * g + ((l >> 2) & 3)) * 160 + g * 128 + (l & 3) * 8;
#pragma unroll
  for (int ct = 0; ct < 5; ++ct)
#pragma unroll
    for (int ks = 0; ks < 4; ++ks) bf[ct][ks] = tr_frag(rp + ks * (32 * 160 + 512) + ct * 32, rp + ks * (32 * 160 + 512) + 4 * 160 + ct * 32);
  const u32 vo = (u32)(((c16 >> 1) & 1) * (16 * 20 * 1024) + (16 * g + 8 * (h & 1) + 2 * (c16 >> 2) + (c16 & 1)) * 16);
#pragma unroll
  for (int i = 0; i < 3; ++i) {
    const int blk = w + 8 * i;
    if (blk < 20) {
#pragma unroll
      for (int ct = 0; ct < 5; ++ct) {
        f32x4 c0 = {0, 0, 0, 0}, c1 = {0, 0, 0, 0};
#pragma unroll
        for (int ks = 0; ks < 4; ++ks) { c0 = mfma(a[i][0][ks], bf[ct][ks], c0); c1 = mfma(a[i][1][ks], bf[ct][ks], c1); }
        u32x4 o = {pk2(c0[0], c0[1]), pk2(c0[2], c0[3]), pk2(c1[0], c1[1]), pk2(c1[2], c1[3])};
        st16(ws, vo, OFF_BF + (u32)(((2 * (5 * part + ct) * 16 + (h >> 1)) * 20 + blk) * 1024), o);
      }
    }
  }
}

DEV float dpp_ror8(float x) { return __uint_as_float(__builtin_amdgcn_update_dpp(0, (int)__float_as_uint(x), 0x128, 0xF, 0xF, true)); }
DEV float red_max_g(float m) {
  auto a = __builtin_amdgcn_permlane16_swap(__float_as_uint(m), __float_as_uint(m), false, false);
  m = fmx(__uint_as_float(a[0]), __uint_as_float(a[1]));
  auto b = __builtin_amdgcn_permlane32_swap(__float_as_uint(m), __float_as_uint(m), false, false);
  return fmx(__uint_as_float(b[0]), __uint_as_float(b[1]));
}
DEV float red_sum_g(float m) {
  auto a = __builtin_amdgcn_permlane16_swap(__float_as_uint(m), __float_as_uint(m), false, false);
  m = __uint_as_float(a[0]) + __uint_as_float(a[1]);
  auto b = __builtin_amdgcn_permlane32_swap(__float_as_uint(m), __float_as_uint(m), false, false);
  return __uint_as_float(b[0]) + __uint_as_float(b[1]);
}
DEV float red_max(float m) { return red_max_g(fmx(m, dpp_ror8(m))); }
DEV float red_sum(float m) { return red_sum_g(m + dpp_ror8(m)); }

#define NPROBE 6
DEV u32 probe(rsrc_t ws, u32 cb, int idx, int page, int l) {
  const u32 vo = (l == 0) ? 0u : 0x80000000u;
  const u32 so = cb + (u32)(page * 32 + 30) * STEP_BYTES + (u32)(2 + (idx >> 6)) * 16384 + (u32)(idx & 63) * 128;
  u32 b0 = ~0u, b1 = ~0u;
#pragma nounroll
  for (int rep = 0; rep < 2 * NPROBE; ++rep) {
    u64 t0 = stamp_cyc();
    st4c(ws, vo, so + (u32)(rep & 1) * 8192, SENT);
    VM_DRAIN();
    u64 t1 = stamp_cyc();
    u32 d = (u32)(t1 - t0);
    if (rep & 1) b1 = d < b1 ? d : b1; else b0 = d < b0 ? d : b0;
  }
  return b1 < b0 ? 1u : 0u;
}

#define D1 140
#define D2 12
#define D3 60
#define D4 12
#define DV 25
#define DX 25
#define DL 5
#define PFD 2
#define DLS 16
#define THR 6
#define THR_S2(x) #x
#define THR_S(x) THR_S2(x)
#define THROTTLE(i) asm volatile("s_waitcnt vmcnt(" THR_S(THR) ")" ::: "memory")
#define PSLEEP 0
#define PFW 1
#define PFQ 4
#define PFS 2
#define PFSTR 128
#define PROT v
#define KQPOS 0
#define DA 12
DEV u32 rtime() { return (u32)__builtin_amdgcn_s_memrealtime(); }
DEV void wait_until(u32 T) {
  u32 r = rtime();
  if ((int)(T - r) < 28) {
#pragma nounroll
    for (int i = 0; i < 64 && (int)(T - r) > 0; ++i) r = rtime();
    return;
  }
#pragma nounroll
  for (int i = 0; i < 4096 && (int)(T - r) > 35; ++i) r = rtime();
  u32 n;
  asm volatile(
    "s_memrealtime s[80:81]\n\ts_nop 2\n\ts_memrealtime s[82:83]\n\ts_nop 2\n\ts_memrealtime s[84:85]\n\ts_nop 2\n\ts_memrealtime s[86:87]\n\ts_nop 2\n\t"
    "s_memrealtime s[88:89]\n\ts_nop 2\n\ts_memrealtime s[90:91]\n\ts_nop 2\n\ts_memrealtime s[92:93]\n\ts_nop 2\n\ts_memrealtime s[94:95]\n\ts_waitcnt lgkmcnt(0)\n\t"
    "s_mov_b32 s81, 0\n\t"
    "s_cmp_eq_u32 s82, s80\n\ts_addc_u32 s81, s81, 0\n\t"
    "s_cmp_eq_u32 s84, s80\n\ts_addc_u32 s81, s81, 0\n\t"
    "s_cmp_eq_u32 s86, s80\n\ts_addc_u32 s81, s81, 0\n\t"
    "s_cmp_eq_u32 s88, s80\n\ts_addc_u32 s81, s81, 0\n\t"
    "s_cmp_eq_u32 s90, s80\n\ts_addc_u32 s81, s81, 0\n\t"
    "s_cmp_eq_u32 s92, s80\n\ts_addc_u32 s81, s81, 0\n\t"
    "s_cmp_eq_u32 s94, s80\n\ts_addc_u32 s81, s81, 0\n\t"
    "s_sub_u32 %0, %1, s80\n\t"
    "s_mul_i32 %0, %0, 3\n\t"
    "s_lshl1_add_u32 %0, s81, %0\n\t"
    "s_sub_i32 %0, %0, 64\n\t"
    "s_max_i32 %0, %0, 0\n\t"
    "s_min_i32 %0, %0, 63\n\t"
    "s_bitcmp1_b32 %0, 0\n\ts_cbranch_scc0 1f\n\ts_nop 5\n\t1:\n\t"
    "s_bitcmp1_b32 %0, 1\n\ts_cbranch_scc0 2f\n\ts_nop 7\n\t2:\n\t"
    "s_bitcmp1_b32 %0, 2\n\ts_cbranch_scc0 3f\n\ts_nop 11\n\t3:\n\t"
    "s_bitcmp1_b32 %0, 3\n\ts_cbranch_scc0 4f\n\ts_nop 15\n\ts_nop 3\n\t4:\n\t"
    "s_bitcmp1_b32 %0, 4\n\ts_cbranch_scc0 5f\n\ts_nop 15\n\ts_nop 15\n\ts_nop 3\n\t5:\n\t"
    "s_bitcmp1_b32 %0, 5\n\ts_cbranch_scc0 6f\n\ts_nop 15\n\ts_nop 15\n\ts_nop 15\n\ts_nop 15\n\ts_nop 3\n\t6:\n\t"
    : "=&s"(n) : "s"(T)
    : "scc", "memory", "s80", "s81", "s82", "s83", "s84", "s85", "s86", "s87", "s88", "s89", "s90", "s91", "s92", "s93", "s94", "s95");
}
template <int CTRL> DEV int dpp_i(int x) { return __builtin_amdgcn_update_dpp(x, x, CTRL, 0xF, 0xF, false); }
DEV int row_max(int d) {
  d = max(d, dpp_i<0x121>(d));
  d = max(d, dpp_i<0x122>(d));
  d = max(d, dpp_i<0x124>(d));
  return max(d, dpp_i<0x128>(d));
}
DEV int wave_max(int d) {
  d = row_max(d);
  return max(max(__builtin_amdgcn_readlane(d, 0), __builtin_amdgcn_readlane(d, 16)), max(__builtin_amdgcn_readlane(d, 32), __builtin_amdgcn_readlane(d, 48)));
}
DEV u32 fine_now() {
  u32 n;
  asm volatile(
    "s_memrealtime s[80:81]\n\ts_nop 2\n\ts_memrealtime s[82:83]\n\ts_nop 2\n\ts_memrealtime s[84:85]\n\ts_nop 2\n\ts_memrealtime s[86:87]\n\ts_nop 2\n\t"
    "s_memrealtime s[88:89]\n\ts_nop 2\n\ts_memrealtime s[90:91]\n\ts_nop 2\n\ts_memrealtime s[92:93]\n\ts_waitcnt lgkmcnt(0)\n\t"
    "s_mov_b32 s81, 0\n\t"
    "s_cmp_eq_u32 s82, s80\n\ts_addc_u32 s81, s81, 0\n\t"
    "s_cmp_eq_u32 s84, s80\n\ts_addc_u32 s81, s81, 0\n\t"
    "s_cmp_eq_u32 s86, s80\n\ts_addc_u32 s81, s81, 0\n\t"
    "s_cmp_eq_u32 s88, s80\n\ts_addc_u32 s81, s81, 0\n\t"
    "s_cmp_eq_u32 s90, s80\n\ts_addc_u32 s81, s81, 0\n\t"
    "s_cmp_eq_u32 s92, s80\n\ts_addc_u32 s81, s81, 0\n\t"
    "s_mul_i32 s81, s81, 11\n\t"
    "s_lshr_b32 s81, s81, 4\n\t"
    "s_add_u32 %0, s80, 4\n\t"
    "s_sub_u32 %0, %0, s81\n\t"
    : "=&s"(n) :
    : "scc", "memory", "s80", "s81", "s82", "s83", "s84", "s85", "s86", "s87", "s88", "s89", "s90", "s91", "s92", "s93");
  return n;
}
struct Side { u32x2 a, b; };
DEV Side load_side(rsrc_t ws, int s, int l) {
  Side t;
  t.a = ld8c(ws, (u32)l * 128, OFF_SIDE + (u32)s * SIDE_STEP);
  t.b = ld8c(ws, (u32)(64 + (l & 15)) * 128, OFF_SIDE + (u32)s * SIDE_STEP);
  return t;
}
DEV void schedule(rsrc_t ws, int s, int l, Side& t, u32& Rm, u32& Rp, bool& dead) {
  if (__builtin_expect(__any(umax3(t.a.x, t.a.y, t.b.x > t.b.y ? t.b.x : t.b.y) == SENT), 0)) {
#pragma nounroll
    for (u32 spins = 0; !dead; ++spins) {
      asm volatile("" ::: "memory");
      t = load_side(ws, s, l);
      if (__all(umax3(t.a.x, t.a.y, t.b.x > t.b.y ? t.b.x : t.b.y) != SENT)) break;
      if (spins > SPIN_CAP) dead = true;
    }
  }
  u32 B = Rm;
  if (__builtin_expect(__any((t.a.x | t.b.x) != 0), 0)) {
    const u32 ref = rtime();
    const int dx = t.a.x ? (int)(t.a.x - ref) : -(1 << 30), dy = t.b.x ? (int)(t.b.x - ref) : -(1 << 30);
    B = ref + (u32)wave_max(max(dx, dy)) + D4 + DX;
  }
  const u32 d = __builtin_amdgcn_readfirstlane(t.a.y);
  Rp = B + ((d >> 10) & 1023u);
  Rm = Rp + ((d >> 20) & 1023u);
}

DEV u32 gather_mem(rsrc_t ws, u32 sbp, int wr, unsigned char* dA, unsigned char* dB, int l, bool& dead) {
  u32x4 a = ld16c(ws, (u32)l * 16, sbp + (u32)wr * 1024);
  u32 b = ld4c(ws, (u32)l * 4, sbp + 16384 + (u32)wr * 256);
  SCHED_FENCE();
  *(u32x4*)dA = a;
  *(u32*)dB = b;
  u32 m = umax3(umax3(a.x, a.y, a.z), a.w, b);
  u32 spins = 0;
  if (__builtin_expect(__any(m == SENT), 0)) {
#pragma nounroll
    while (!dead) {
      ++spins;
      asm volatile("" ::: "memory");
      a = ld16c(ws, (u32)l * 16, sbp + (u32)wr * 1024);
      b = ld4c(ws, (u32)l * 4, sbp + 16384 + (u32)wr * 256);
      m = umax3(umax3(a.x, a.y, a.z), a.w, b);
      if (__all(m != SENT)) break;
      if (spins > SPIN_CAP) dead = true;
    }
    *(u32x4*)dA = a;
    *(u32*)dB = b;
  }
  return spins;
}
DEV u32 wait_prog(rsrc_t ws, u32 off, int n, u32 need, int l, bool& dead, int slp, u32 stride = 4) {
  u32 mn = 0;
#pragma nounroll
  for (u32 spins = 0;; ++spins) {
    asm volatile("" ::: "memory");
    const u32 x = ld4c(ws, (u32)(l < n ? l : 0) * stride, off);
    int d = -(int)x;
    d = row_max(d);
    mn = (u32)(-max(__builtin_amdgcn_readlane(d, 0), __builtin_amdgcn_readlane(d, 16)));
    if (mn >= need || dead) break;
    if (spins > SPIN_CAP) { dead = true; break; }
    if (slp == 2) __builtin_amdgcn_s_sleep(32); else if (slp) __builtin_amdgcn_s_sleep(6); else __builtin_amdgcn_s_sleep(1);
  }
  asm volatile("" ::: "memory");
  return mn;
}

DEV int wrow(int r) { return r * 288 + (r >> 3) * 128; }
DEV void load_qk(const Params& p, int h, int w, int l, unsigned char* lds, u32x4 (&wt)[2][20]) {
  const rsrc_t src = mk_rsrc((w >> 2) ? p.wk : p.wq, 640 * 8192);
  u32x4 st[40];
#pragma unroll
  for (int i = 0; i < 40; ++i)
    st[i] = ld16(src, (u32)((l >> 4) * 8192 + (l & 15) * 16), (u32)((32 * (w & 3) + (i >> 3) * 128 + (i & 7) * 4) * 8192 + h * 256));
  unsigned char* img = lds + (w >> 2) * 38912;
  unsigned char* wp = img + wrow(32 * (w & 3)) + (l >> 4) * 288 + (l & 15) * 16;
  const unsigned char* rp = img + wrow(8 * (l >> 4)) + ((l >> 2) & 3) * 288 + (w & 3) * 64 + (l & 3) * 8;
#pragma unroll
  for (int r = 0; r < 5; ++r) {
#pragma unroll
    for (int i = 0; i < 8; ++i) *(u32x4*)(wp + wrow(4 * i)) = st[r * 8 + i];
    __syncthreads();
#pragma unroll
    for (int kk = 0; kk < 4; ++kk)
#pragma unroll
      for (int i = 0; i < 2; ++i) wt[i][4 * r + kk] = tr_frag(rp + wrow(32 * kk) + i * 32, rp + wrow(32 * kk) + 4 * 288 + i * 32);
    __syncthreads();
  }
}
DEV void load_vu(const Params& p, int h, int w, int l, unsigned char* lds, u32x4 (&wv)[20], u32x4 (&wo)[5][4]) {
  const rsrc_t sv = mk_rsrc(p.wv, 640 * 8192), so = mk_rsrc(p.wo, 640 * 8192);
  u32x4 st[40];
#pragma unroll
  for (int i = 0; i < 20; ++i)
    st[i] = ld16(sv, (u32)((l >> 4) * 8192 + (l & 15) * 16), (u32)((40 * w + (i / 10) * 320 + (i % 10) * 4) * 8192 + h * 256));
#pragma unroll
  for (int i = 0; i < 20; ++i) st[20 + i] = ld16(so, (u32)l * 16, (u32)(h * 163840 + (w * 640 + (i / 10) * 5120 + (i % 10) * 64) * 16));
  unsigned char* wp = lds + wrow(40 * w) + (l >> 4) * 288 + (l & 15) * 16;
  const unsigned char* rp = lds + wrow(8 * (l >> 4)) + ((l >> 2) & 3) * 288 + w * 32 + (l & 3) * 8;
#pragma unroll
  for (int r = 0; r < 2; ++r) {
#pragma unroll
    for (int i = 0; i < 10; ++i) *(u32x4*)(wp + wrow(4 * i)) = st[r * 10 + i];
    __syncthreads();
#pragma unroll
    for (int kk = 0; kk < 10; ++kk) wv[10 * r + kk] = tr_frag(rp + wrow(32 * kk), rp + wrow(32 * kk) + 4 * 288);
    __syncthreads();
  }
  const unsigned char* ro = lds + (4 * (l >> 4) + ((l >> 2) & 3)) * 1312 + w * 160 + (l & 3) * 8;
#pragma unroll
  for (int r = 0; r < 2; ++r) {
#pragma unroll
    for (int i = 0; i < 10; ++i) {
      const int c = (w * 10 + i) * 64 + l;
      *(u32x4*)(lds + (c / 80) * 1312 + (c % 80) * 16) = st[20 + r * 10 + i];
    }
    __syncthreads();
#pragma unroll
    for (int kk = 0; kk < 2; ++kk)
#pragma unroll
      for (int ct = 0; ct < 5; ++ct) wo[ct][2 * r + kk] = tr_frag(ro + 32 * kk * 1312 + ct * 32, ro + (32 * kk + 16) * 1312 + ct * 32);
    __syncthreads();
  }
}

constexpr float CEXP = 0.08838834764831845f * 1.4426950408889634f;
constexpr float NEGS = -1.0e9f / 0.08838834764831845f;
constexpr int QS = 288;

DEV void read_memf(const unsigned char* memf, int l, u32x4 (&a)[10]) {
#pragma unroll
  for (int ks = 0; ks < 10; ++ks) a[ks] = *(const u32x4*)(memf + ks * 1024 + l * 16);
}
DEV void project2(const u32x4 (&a)[10], const u32x8 (&wt)[2][10], int sidx, unsigned char* dst) {
  f32x4 c0 = {0, 0, 0, 0}, c1 = {0, 0, 0, 0};
#pragma unroll
  for (int ks = 0; ks < 10; ++ks) {
    c0 = smfma(a[ks], wt[0][ks], c0, sidx);
    c1 = smfma(a[ks], wt[1][ks], c1, sidx);
  }
  *(__bf16*)(dst) = (__bf16)(c0[0] + c0[1]);
  *(__bf16*)(dst + QS) = (__bf16)(c0[2] + c0[3]);
  *(__bf16*)(dst + 8) = (__bf16)(c1[0] + c1[1]);
  *(__bf16*)(dst + 8 + QS) = (__bf16)(c1[2] + c1[3]);
}

DEV void score_role(const Params& p, rsrc_t ws, u32 cb, int h, unsigned char* lds) {
  const int tid = threadIdx.x, l = tid & 63, c16 = l & 15, g = l >> 4;
  const int w = __builtin_amdgcn_readfirstlane(tid >> 6);
  unsigned char* memf = lds;
  unsigned char* qs = lds + 10240;
  unsigned char* kms = qs + 8 * QS;
  u32* ctl = (u32*)(lds + 15360);
  unsigned char* kq = lds + 16384;
  u32x8 wt[2][10];
  {
    u32x4 wf[2][20];
    load_qk(p, h, w, l, lds, wf);
#pragma unroll
    for (int i = 0; i < 2; ++i)
#pragma unroll
      for (int ks = 0; ks < 10; ++ks) wt[i][ks] = cat(wf[i][2 * ks], wf[i][2 * ks + 1]);
  }
  GSTAMP(1);
  bool dead = false;
  if (tid < 2) ctl[2 + tid] = 0;
  u32 seen0 = 0, seen1 = 0;
  const u32 kq_src = ((w & 2) ? OFF_QX : OFF_KX) + (u32)(h * 8192 + (w & 1) * 4096);
  if (w < 2) { u32 r = probe(ws, cb, h, w, l); if (l == 0) ctl[4 + w] = r; }
  else if (w >= 4) {
    seen0 = wait_prog(ws, OFF_PQK + (u32)h * 128, 1, 1, l, dead, 0);
    seen1 = wait_prog(ws, OFF_PQK + (u32)h * 128 + 4, 1, 1, l, dead, 0);
#pragma unroll
    for (int i = 0; i < 4; ++i) *(u32x4*)(kq + (w - 4) * 4096 + i * 1024 + l * 16) = ld16c(ws, l * 16 + i * 1024, kq_src);
  }
  __syncthreads();
  GSTAMP(2);
  const u32 parmask = __builtin_amdgcn_readfirstlane(ctl[4] | (ctl[5] << 1));
  const int sidx = (l & 1) ? 0xEEEE : 0x4444;
  unsigned char* qk_w = qs + (w >> 2) * 8 * QS + 2 * g * QS + (32 * (w & 3) + 8 * (c16 >> 2) + (c16 & 3)) * 2;
  const unsigned char* km_r = kms + (2 * (c16 >> 2) + (c16 & 1)) * QS + g * 16;
  const int wr = (w + h) & 7;
  unsigned char* gA = memf + wr * 1024 + g * 256 + (l & 7) * 32 + ((l >> 3) & 1) * 16;
  unsigned char* gB = memf + 8192 + wr * 256 + ((l >> 2) & 7) * 32 + (l >> 5) * 16 + (l & 3) * 4;
  if (w == 0) {
    const unsigned char* q_r = qs + (c16 & 7) * QS + g * 16;
    const bool hb = c16 >= 8;
    const u32 xo = (u32)(((h >> 1) & 7) * 1024 + g * 256 + (h & 1) * 128 + (c16 & 7) * 16 + (c16 >> 3) * 8);
    const u32 mo = (u32)((c16 >> 3) * 8192 + h * 128 + (c16 & 7) * 16 + g * 4);
    for (int s = 0; s < NSEG; ++s) {
      const u32 sb = cb + (u32)s * STEP_BYTES;
      const u32 par = ((parmask >> (s >> 5)) & 1) * 8192;
      const unsigned char* kxs = kq + (s & 1) * 16384;
      __syncthreads();
      { u32 sp = gather_mem(ws, sb + par, wr, gA, gB, l, dead); COUNT(5, sp); if (sp && l == 0) ctl[2 + (s & 1)] = (u32)s; }
      __syncthreads();
      { u32x4 a[10]; read_memf(memf, l, a); SCHED_FENCE(); project2(a, wt, sidx, qk_w); }
      u32x4 kx[8];
#pragma unroll
      for (int i = 0; i < 8; ++i) kx[i] = *(const u32x4*)(kxs + i * 1024 + l * 16);
      __syncthreads();
      if (s == NSEG - 1) break;
      u32x4 qf[4], km[4];
#pragma unroll
      for (int ks = 0; ks < 4; ++ks) { qf[ks] = *(const u32x4*)(q_r + ks * 64); km[ks] = *(const u32x4*)(km_r + ks * 64); }
      SCHED_FENCE();
      f32x4 s0 = {0, 0, 0, 0}, s1 = {0, 0, 0, 0}, s2 = {0, 0, 0, 0};
#pragma unroll
      for (int ks = 0; ks < 4; ++ks) {
        s1 = mfma(kx[ks], qf[ks], s1);
        s2 = mfma(kx[4 + ks], qf[ks], s2);
        s0 = mfma(km[ks], qf[ks], s0);
      }
      float v[4];
#pragma unroll
      for (int r = 0; r < 4; ++r) v[r] = hb ? s2[r] : s1[r];
      float m = fmx(fmx(fmx(v[0], v[1]), v[2]), fmx(fmx(v[3], s0[0]), s0[1]));
      m = red_max(m);
      const float mc = -m * CEXP;
#pragma unroll
      for (int r = 0; r < 4; ++r) v[r] = __builtin_amdgcn_exp2f(fmaf(v[r], CEXP, mc));
      const float e0 = __builtin_amdgcn_exp2f(fmaf(s0[0], CEXP, mc)), e1 = __builtin_amdgcn_exp2f(fmaf(s0[1], CEXP, mc));
      float sum = ((v[0] + v[1]) + (v[2] + v[3])) + (hb ? e1 : e0);
      sum = red_sum(sum);
      const float inv = __builtin_amdgcn_rcpf(sum);
      u32x2 px = {pk2s(v[0] * inv, v[1] * inv), pk2s(v[2] * inv, v[3] * inv)};
      const u32 pm = pk2s(e0 * inv, e1 * inv);
      const u32 xs = sb + (u32)(2 + (h >> 4)) * 16384;
      st8c(ws, xo, xs + par, px);
      st4c(ws, mo, sb + 16384 + 2048, pm);
      st8c(ws, xo, xs + (par ^ 8192), px);
      const u32 tnow = fine_now();
      const u32 rm = __builtin_amdgcn_readfirstlane(ctl[s & 1]), bad = __builtin_amdgcn_readfirstlane(ctl[2 + (s & 1)]);
      const u32 d1 = (s >= 2 && bad != (u32)s) ? tnow - rm : 0u;
      st4c(ws, l < 32 ? (u32)l * 4 : 0x80000000u, OFF_SIDE + (u32)s * SIDE_STEP + (u32)(80 + h) * 128, d1 < 1000u ? d1 : 0u);
      STAMPV(7, d1);
      STAMPV(4, tnow);
    }
  } else if (w < 3) {
    const int tx = 16 * (w - 1) + c16;
    for (int s = 0; s < NSEG; ++s) {
      const unsigned char* kxs = kq + (s & 1) * 16384;
      const u32 sb = cb + (u32)s * STEP_BYTES;
      const u32 par = ((parmask >> (s >> 5)) & 1) * 8192;
      __syncthreads();
      { u32 sp = gather_mem(ws, sb + par, wr, gA, gB, l, dead); COUNT(5, sp); if (sp && l == 0) ctl[2 + (s & 1)] = (u32)s; }
      __syncthreads();
      if (w == 1) STAMP(1);
      { u32x4 a[10]; read_memf(memf, l, a); SCHED_FENCE(); project2(a, wt, sidx, qk_w); }
      __syncthreads();
      if (w == 1) STAMP(2);
      if (w == 1) st4c(ws, l == 0 ? 0u : 0x80000000u, OFF_PPX + (u32)h * 128, (u32)s);
      f32x4 s0 = {0, 0, 0, 0}, s1 = {0, 0, 0, 0}, s2 = {0, 0, 0, 0};
#pragma unroll
      for (int ks = 0; ks < 4; ++ks) {
        u32x4 qf = *(const u32x4*)(kxs + 8192 + ((w - 1) * 4 + ks) * 1024 + l * 16);
        s0 = mfma(*(const u32x4*)(km_r + ks * 64), qf, s0);
        s1 = mfma(*(const u32x4*)(kxs + ks * 1024 + l * 16), qf, s1);
        s2 = mfma(*(const u32x4*)(kxs + (4 + ks) * 1024 + l * 16), qf, s2);
      }
      float m = fmx(s0[0], s0[1]);
#pragma unroll
      for (int r = 0; r < 4; ++r) {
        s1[r] = (4 * g + r <= tx) ? s1[r] : NEGS;
        s2[r] = (16 + 4 * g + r <= tx) ? s2[r] : NEGS;
        m = fmx(m, fmx(s1[r], s2[r]));
      }
      m = red_max_g(m);
      const float mc = -m * CEXP;
      const float e0 = __builtin_amdgcn_exp2f(fmaf(s0[0], CEXP, mc)), e1 = __builtin_amdgcn_exp2f(fmaf(s0[1], CEXP, mc));
      float sum = e0 + e1;
#pragma unroll
      for (int r = 0; r < 4; ++r) {
        s1[r] = __builtin_amdgcn_exp2f(fmaf(s1[r], CEXP, mc));
        s2[r] = __builtin_amdgcn_exp2f(fmaf(s2[r], CEXP, mc));
        sum += s1[r] + s2[r];
      }
      sum = red_sum_g(sum);
      const float inv = __builtin_amdgcn_rcpf(sum);
      u32x4 px = {pk2(s1[0] * inv, s1[1] * inv), pk2(s1[2] * inv, s1[3] * inv), pk2(s2[0] * inv, s2[1] * inv), pk2(s2[2] * inv, s2[3] * inv)};
      st16c(ws, l * 16, OFF_PXX + (u32)(((s * 32 + h) * 2 + (w - 1)) * 1024), px);
      st4c(ws, c16 * 16 + g * 4, OFF_PXM + (u32)((((s * 2 + (w - 1)) * 32 + h) * 16) * 16), pk2(e0 * inv, e1 * inv));
      VM_DRAIN();
    }
  } else if (w == 3) {
    u32 Rm = 0, Rp;
    for (int s = 0; s < NSEG; ++s) {
      const u32 sb = cb + (u32)s * STEP_BYTES;
      const u32 par = ((parmask >> (s >> 5)) & 1) * 8192;
      if (s >= 2) {
        if (l == 0) { ctl[s & 1] = Rm; if ((int)(Rm - rtime()) < 28) ctl[2 + (s & 1)] = (u32)s; }
        wait_until(Rm);
      }
      __syncthreads();
      { u32 sp = gather_mem(ws, sb + par, wr, gA, gB, l, dead); COUNT(5, sp); if (sp && l == 0) ctl[2 + (s & 1)] = (u32)s; }
      __syncthreads();
      u32x4 a[10];
      read_memf(memf, l, a);
      Side sd = load_side(ws, s, l);
      SCHED_FENCE();
      project2(a, wt, sidx, qk_w);
      __syncthreads();
      STAMPV(0, Rm);
      if (s >= 1 && s < NSEG - 1) schedule(ws, s, l, sd, Rm, Rp, dead);
    }
  } else {
    for (int s = 0; s < NSEG; ++s) {
      const u32 sb = cb + (u32)s * STEP_BYTES;
      const u32 par = ((parmask >> (s >> 5)) & 1) * 8192;
      const int sn = s + 1 < NSEG ? s + 1 : s;
      __syncthreads();
      { u32 sp = gather_mem(ws, sb + par, wr, gA, gB, l, dead); COUNT(5, sp); if (sp && l == 0) ctl[2 + (s & 1)] = (u32)s; }
      __syncthreads();
      u32x4 a[10];
      read_memf(memf, l, a);
      SCHED_FENCE();
      u32x4 pk[4];
      f32x4 c0 = {0, 0, 0, 0}, c1 = {0, 0, 0, 0};
#pragma unroll
      for (int ks = 0; ks < 10; ++ks) {
        c0 = smfma(a[ks], wt[0][ks], c0, sidx);
        c1 = smfma(a[ks], wt[1][ks], c1, sidx);
        if (ks == KQPOS) {
          SCHED_FENCE();
#pragma unroll
          for (int i = 0; i < 4; ++i) pk[i] = ld16c(ws, l * 16 + i * 1024, kq_src + (u32)sn * (32 * 8192));
          SCHED_FENCE();
        }
      }
      *(__bf16*)(qk_w) = (__bf16)(c0[0] + c0[1]);
      *(__bf16*)(qk_w + QS) = (__bf16)(c0[2] + c0[3]);
      *(__bf16*)(qk_w + 8) = (__bf16)(c1[0] + c1[1]);
      *(__bf16*)(qk_w + 8 + QS) = (__bf16)(c1[2] + c1[3]);
      __syncthreads();
      unsigned char* kn = kq + ((s + 1) & 1) * 16384 + (w - 4) * 4096 + l * 16;
#pragma unroll
      for (int i = 0; i < 4; ++i) *(u32x4*)(kn + i * 1024) = pk[i];
      const int s2 = s + 2;
      if (s2 < NSEG) {
        if (seg_prod(s2)) { if (seen1 < seg_need(s2)) { __builtin_amdgcn_s_sleep(12); seen1 = wait_prog(ws, OFF_PQK + (u32)h * 128 + 4, 1, seg_need(s2), l, dead, 0); } }
        else { if (seen0 < seg_need(s2)) { __builtin_amdgcn_s_sleep(12); seen0 = wait_prog(ws, OFF_PQK + (u32)h * 128, 1, seg_need(s2), l, dead, 0); } }
      }
    }
  }
  __syncthreads();
  if (tid == 0) st4c(ws, 0u, OFF_PPX + (u32)h * 128, (u32)NSEG);
}

DEV u32x4 finish_mem(f32x4 acc) {
  auto x = __builtin_amdgcn_permlane32_swap(__float_as_uint(acc[0]), __float_as_uint(acc[2]), false, false);
  auto y = __builtin_amdgcn_permlane32_swap(__float_as_uint(acc[1]), __float_as_uint(acc[3]), false, false);
  float t0, t1;
  asm("s_nop 1\n\tv_add_f32_dpp %0, %2, %3 row_ror:8 row_mask:0xf bank_mask:0xf\n\tv_add_f32_dpp %1, %4, %5 row_ror:8 row_mask:0xf bank_mask:0xf"
      : "=&v"(t0), "=&v"(t1) : "v"(x[1]), "v"(x[0]), "v"(y[1]), "v"(y[0]));
  const u32 d = pk2s(t0, t1);
  auto s16 = __builtin_amdgcn_permlane16_swap(d, d, false, false);
  auto sa = __builtin_amdgcn_permlane32_swap(s16[0], s16[0], false, false);
  auto sb = __builtin_amdgcn_permlane32_swap(s16[1], s16[1], false, false);
  return (u32x4){sa[0], sa[1], sb[0], sb[1]};
}

DEV u32 second4(u32 a, u32 b, u32 c, u32 d) {
  const u32 h0 = max(a, b), l0 = min(a, b), h1 = max(c, d), l1 = min(c, d);
  return max(min(h0, h1), h0 > h1 ? l0 : l1);
}
DEV void value_role(const Params& p, rsrc_t ws, u32 cb, int v, unsigned char* lds) {
  const int tid = threadIdx.x, l = tid & 63, c16 = l & 15, g = l >> 4;
  const int w = __builtin_amdgcn_readfirstlane(tid >> 6);
  unsigned char* memf = lds;
  unsigned char* um = lds + 10240;
  u32* ctl = (u32*)(lds + 15360);
  unsigned char* w0s = lds + 16384;
  unsigned char* uxs = lds + 57344;
  const int sidx = (l & 1) ? 0xEEEE : 0x4444;
  const u32 um_w = (u32)((4 * w + (c16 >> 3)) * 128 + (c16 & 7) * 16 + g * 4);
  const int wr = (w + v) & 7;
  unsigned char* gA = memf + wr * 1024 + g * 256 + (l & 7) * 32 + ((l >> 3) & 1) * 16;
  unsigned char* gB = memf + 8192 + wr * 256 + ((l >> 2) & 7) * 32 + (l >> 5) * 16 + (l & 3) * 4;
  bool dead = false;
  if (w == 0) {
#pragma unroll
    for (int i = 0; i < 40; ++i) *(u32x4*)(w0s + i * 1024 + l * 16) = ld16(ws, l * 16, OFF_BF + (u32)((v * 16 * 20 + i) * 1024));
    __syncthreads();
    GSTAMP(1);
    __syncthreads();
    GSTAMP(2);
    const u32 parmask = __builtin_amdgcn_readfirstlane(ctl[4] | (ctl[5] << 1));
    const u32 offX = (u32)((c16 >> 3) * 512 + g * 128 + (c16 & 7) * 16);
    const u32 L = (u32)(v * 128 + (c16 & 7) * 16);
    const u32 pub = (c16 < 8 && !(g & 1)) ? (L >> 13) * 16384 + (u32)(g >> 1) * 8192 + (L & 8191) : 0x80000000u;
    u32 Rg = 0, d3 = 0, Rm = 0, dcur = v == 0 ? ((D1 + D2) << 10) | ((D3 + D4) << 20) : 0u;
    u32 a0 = D1, a1 = D1, a2 = D1, a3 = D1, b0 = D3, b1 = D3, b2 = D3, b3 = D3;
    const bool sidel = (g == 1 && c16 < 8);
    for (int s = 0; s < NSEG; ++s) {
      const u32 sb = cb + (u32)s * STEP_BYTES;
      const u32 par = ((parmask >> (s >> 5)) & 1) * 8192;
      if (s >= 1) wait_until(Rg);
      __syncthreads();
      Side sd = load_side(ws, s, l);
      u32 d1s = 0;
      if (v == 0 && s >= 1) d1s = ld4c(ws, (u32)(80 + (l & 31)) * 128, OFF_SIDE + (u32)(s - 1) * SIDE_STEP);
      { u32 sp = gather_mem(ws, sb + par, wr, gA, gB, l, dead); COUNT(5, sp); }
      u32x4 uf[20];
#pragma unroll
      for (int i = 0; i < 16; ++i) uf[i] = *(const u32x4*)(uxs + (((i >> 2) + PROT) & 3) * 4096 + (i & 3) * 1024 + offX);
      __syncthreads();
      STAMP(0);
      if (s == NSEG - 1) {
        __syncthreads();
        __syncthreads();
        break;
      }
      u32 so[5];
#pragma unroll
      for (int i = 0; i < 4; ++i) { const u32 qd = (u32)(i + PROT) & 3; so[i] = sb + par + (2 + (qd >> 1)) * 16384 + (qd & 1) * 4096; }
      so[4] = sb + par + 16384 + 2048;
      asm volatile("" : "+s"(so[0]), "+s"(so[1]), "+s"(so[2]), "+s"(so[3]), "+s"(so[4]));
      const u32 pubo = sidel ? OFF_SIDE + (u32)(s + 1) * SIDE_STEP + (u32)(v * 128 + c16 * 16) : pub + sb + STEP_BYTES;
      u32 Rp = 0;
      if (s >= 1) schedule(ws, s, l, sd, Rm, Rp, dead);
      const u32 sy = d3 | dcur;
      const int lead = (int)(Rp - rtime());
      bool ontime = s >= 1 && lead > 0, pfail = false;
      STAMPV(1, Rp);
      STAMP(2);
      SCHED_FENCE();
      if (s >= 1) wait_until(Rp);
      SCHED_FENCE();
      u32x4 pf[20];
#pragma unroll
      for (int i = 0; i < 20; ++i) { pf[i] = ld16c(ws, (u32)l * 16 + (i & 3) * 1024, so[i >> 2]); SCHED_FENCE(); }
      __syncthreads();
#pragma unroll
      for (int q = 0; q < 4; ++q) uf[16 + q] = *(const u32x4*)(um + q * 1024 + l * 16);
      SCHED_FENCE();
      f32x4 acc = {0, 0, 0, 0};
#pragma unroll
      for (int i = 0; i < 20; ++i) { acc = mfma(uf[i], pf[i], acc); SCHED_FENCE(); }
      const u32 tl = rtime();
      u32x4 o = finish_mem(acc);
      if (__builtin_expect(__any(acc[0] != acc[0]), 0)) {
        u32 spins = 0;
#pragma nounroll
        while (!dead) {
          u32 m = 0;
#pragma unroll
          for (int i = 0; i < 20; ++i) m = umax3(umax3(m, pf[i].x, pf[i].y), pf[i].z, pf[i].w);
          if (__all(m != SENT)) break;
          ontime = false; pfail = true;
          if (++spins > SPIN_CAP) dead = true;
          asm volatile("" ::: "memory");
#pragma unroll
          for (int i = 0; i < 20; ++i) pf[i] = ld16c(ws, (u32)l * 16 + (i & 3) * 1024, so[i >> 2]);
        }
        STAMPV(4, spins);
        acc = (f32x4){0, 0, 0, 0};
#pragma unroll
        for (int i = 0; i < 20; ++i) acc = mfma(uf[i], pf[i], acc);
        o = finish_mem(acc);
      }
      u32 sx = 0;
      if ((int)(tl + DLS - Rm) > 0) sx = (tl + DL + 4) | 1u;
      if (__builtin_expect(!ontime, 0)) sx = (rtime() + DL) | 1u;
      if (sx == SENT) sx = 1u;
      o.x = sidel ? sx : o.x;
      o.y = sidel ? sy : o.y;
      st16c(ws, pubo, 0u, o);
      const u32 tp = fine_now();
      __syncthreads();
      d3 = (ontime && lead >= 28) ? tp - Rp : 0u;
      d3 = d3 < 1000u ? d3 : 0u;
      Rg = sx == 0 ? Rm + DV : tp + D4 + DV;
      STAMPV(7, d3);
      if (v == 0) {
        const u32 ma = (u32)wave_max(d1s == SENT ? 0 : (int)d1s), mb = s >= 1 ? (u32)wave_max((int)max(sd.a.y & 1023u, sd.b.y & 1023u)) : 0u;
        if (ma) { a3 = a2; a2 = a1; a1 = a0; a0 = ma; }
        if (pfail && s >= 2) { a0 = a1 = min(second4(a0, a1, a2, a3) + 8, 300u); }
        if (mb) { b3 = b2; b2 = b1; b1 = b0; b0 = mb; }
        dcur = (min(second4(a0, a1, a2, a3) + D2, 1000u) << 10) | (min(second4(b0, b1, b2, b3) + D4, 1000u) << 20);
      }
      STAMP(3);
      STAMPV(6, ((sy >> 10) & 1023u) * 1000 + ((sy >> 20) & 1023u));
    }
  } else {
    u32x8 wb[2][10];
#pragma unroll
    for (int t = 0; t < 2; ++t)
#pragma unroll
      for (int ks = 0; ks < 10; ++ks) {
        u32 o = OFF_BF + (u32)(((v * 16 + 2 * w + t) * 20 + 2 * ks) * 1024);
        wb[t][ks] = cat(ld16(ws, l * 16, o), ld16(ws, l * 16 + 1024, o));
      }
    __builtin_amdgcn_s_waitcnt(0x0F70);
    __syncthreads();
    if (w < 3) { u32 r = probe(ws, cb, 32 + v, w - 1, l); if (l == 0) ctl[3 + w] = r; }
    const bool pfw = w == 3 || w >= 5;
    const u32 uxo = (u32)((w == 3 ? 0 : w - 4) * 4096 + l * 16);
    u32 seen0 = 0, seen1 = 0;
    if (pfw) {
      seen0 = wait_prog(ws, OFF_PVU, 32, 1, l, dead, 0);
      seen1 = wait_prog(ws, OFF_PVU + 128, 32, 1, l, dead, 0);
#pragma unroll
      for (int i = 0; i < 4; ++i) *(u32x4*)(uxs + uxo + i * 1024) = ld16c(ws, uxo + i * 1024, OFF_UX + (u32)(v * 16384));
    }
    __syncthreads();
    const u32 parmask = __builtin_amdgcn_readfirstlane(ctl[4] | (ctl[5] << 1));
    if (w == 4) {
      for (int s = 0; s < NSEG; ++s) {
        const u32 sb = cb + (u32)s * STEP_BYTES;
        const u32 par = ((parmask >> (s >> 5)) & 1) * 8192;
        u32x8 sb0[2], sb1[2];
        sb0[0] = cat(*(const u32x4*)(w0s + l * 16), *(const u32x4*)(w0s + 1024 + l * 16));
        sb1[0] = cat(*(const u32x4*)(w0s + 20480 + l * 16), *(const u32x4*)(w0s + 21504 + l * 16));
        __syncthreads();
        { u32 sp = gather_mem(ws, sb + par, wr, gA, gB, l, dead); COUNT(5, sp); }
        __syncthreads();
        u32x4 a[3];
        a[0] = *(const u32x4*)(memf + l * 16);
        a[1] = *(const u32x4*)(memf + 1024 + l * 16);
        f32x4 a0 = {0, 0, 0, 0}, a1 = {0, 0, 0, 0}, a2 = {0, 0, 0, 0}, a3 = {0, 0, 0, 0};
#pragma unroll
        for (int ks = 0; ks < 10; ++ks) {
          if (ks + 2 < 10) a[(ks + 2) % 3] = *(const u32x4*)(memf + (ks + 2) * 1024 + l * 16);
          if (ks + 1 < 10) {
            const int kn = ks + 1;
            sb0[kn & 1] = cat(*(const u32x4*)(w0s + (2 * kn) * 1024 + l * 16), *(const u32x4*)(w0s + (2 * kn + 1) * 1024 + l * 16));
            sb1[kn & 1] = cat(*(const u32x4*)(w0s + (20 + 2 * kn) * 1024 + l * 16), *(const u32x4*)(w0s + (21 + 2 * kn) * 1024 + l * 16));
          }
          SCHED_FENCE();
          a0 = smfma(a[ks % 3], wb[0][ks], a0, sidx);
          a1 = smfma(a[ks % 3], wb[1][ks], a1, sidx);
          a2 = smfma(a[ks % 3], sb0[ks & 1], a2, sidx);
          a3 = smfma(a[ks % 3], sb1[ks & 1], a3, sidx);
          SCHED_FENCE();
        }
        *(u32*)(um + um_w) = pk2(a0[0] + a0[1], a0[2] + a0[3]);
        *(u32*)(um + um_w + 256) = pk2(a1[0] + a1[1], a1[2] + a1[3]);
        *(u32*)(um + um_w - 2048) = pk2(a2[0] + a2[1], a2[2] + a2[3]);
        *(u32*)(um + um_w - 2048 + 256) = pk2(a3[0] + a3[1], a3[2] + a3[3]);
        __syncthreads();
        __syncthreads();
      }
    } else {
      for (int s = 0; s < NSEG; ++s) {
        const u32 sb = cb + (u32)s * STEP_BYTES;
        const u32 par = ((parmask >> (s >> 5)) & 1) * 8192;
        __syncthreads();
        { u32 sp = gather_mem(ws, sb + par, wr, gA, gB, l, dead); COUNT(5, sp); }
        __syncthreads();
        u32x4 a[10];
        read_memf(memf, l, a);
        u32x4 pu[4];
        if (pfw && s + 1 < NSEG) {
#pragma unroll
          for (int i = 0; i < 4; ++i) pu[i] = ld16c(ws, uxo + i * 1024, OFF_UX + (u32)(((s + 1) * 80 + v) * 16384));
        }
        SCHED_FENCE();
        f32x4 a0 = {0, 0, 0, 0}, a1 = {0, 0, 0, 0};
#pragma unroll
        for (int ks = 0; ks < 10; ++ks) {
          a0 = smfma(a[ks], wb[0][ks], a0, sidx);
          a1 = smfma(a[ks], wb[1][ks], a1, sidx);
        }
        *(u32*)(um + um_w) = pk2(a0[0] + a0[1], a0[2] + a0[3]);
        *(u32*)(um + um_w + 256) = pk2(a1[0] + a1[1], a1[2] + a1[3]);
        if (w == 1) st4c(ws, l == 0 ? 0u : 0x80000000u, OFF_PUM + (u32)v * 128, (u32)s);
        __syncthreads();
        __syncthreads();
        if (pfw && s + 1 < NSEG) {
#pragma unroll
          for (int i = 0; i < 4; ++i) *(u32x4*)(uxs + uxo + i * 1024) = pu[i];
          const int sn = s + 2;
          if (sn < NSEG) {
            if (seg_prod(sn)) { if (seen1 < seg_need(sn)) seen1 = wait_prog(ws, OFF_PVU + 128, 32, seg_need(sn), l, dead, 0); }
            else { if (seen0 < seg_need(sn)) seen0 = wait_prog(ws, OFF_PVU, 32, seg_need(sn), l, dead, 0); }
          }
        }
        if (w == 1 || w == 2) {
#pragma unroll
          for (int i = 0; i < 2; ++i) {
            u32 o = (u32)(((w - 1) * 2 + i) * 1024 + l * 16);
            st16c(ws, o, OFF_UM + (u32)((s * 80 + v) * 4096), *(const u32x4*)(um + o));
          }
          VM_DRAIN();
        }
      }
    }
  }
  __syncthreads();
  if (tid == 0) st4c(ws, 0u, OFF_PUM + (u32)v * 128, (u32)NSEG);
}

DEV void glds16(const void* gsrc, u32 lds_dst) {
  u32 keep;
  asm volatile("s_mov_b32 %0, m0\n\ts_mov_b32 m0, %2\n\ts_nop 0\n\tglobal_load_lds_dwordx4 %1, off\n\ts_mov_b32 m0, %0"
               : "=&s"(keep) : "v"(gsrc), "s"(lds_dst) : "memory");
}
template <int NST, bool ROWS, class Off, class Body, class Pub>
DEV void stream_tiles(const unsigned char* src, Off off, int n, int nbody, unsigned char* lds, Body body, Pub pub) {
  const int l = threadIdx.x & 63, w = __builtin_amdgcn_readfirstlane(threadIdx.x >> 6);
  const u32 lbase = (u32)(uintptr_t)lds + (u32)w * 5120;
  constexpr int QS_ = ROWS ? 64 : 1024;
  const unsigned char* g0 = ROWS ? src + (16 * (w >> 2) + (l & 15)) * 1280 + (w & 3) * 320 + (l >> 4) * 16 : src + w * 5120 + l * 16;
#pragma unroll
  for (int q = 0; q < 5; ++q) glds16(g0 + off(0) + q * QS_, lbase + q * 1024);
#pragma nounroll
  for (int i = 0; i < nbody; ++i) {
    if (i <= 3) VM_DRAIN(); else asm volatile("s_waitcnt vmcnt(%0)" :: "n"(NST) : "memory");
    RAW_BARRIER();
    if (i >= 1) pub(i <= 3 ? i : i - 1);
    const unsigned char* g = g0 + off(i + 1 < n ? i + 1 : n - 1);
    const u32 lb = lbase + (u32)((i + 1) & 1) * 40960;
    body(i, lds + (i & 1) * 40960, [&](int q) { glds16(g + q * QS_, lb + q * 1024); });
  }
  VM_DRAIN();
  RAW_BARRIER();
  pub(nbody);
}
template <int W, class Dma, class Use>
DEV void sweep40(const unsigned char* t, Dma dma, Use use) {
  u32x4 xb[W];
#pragma unroll
  for (int i = 0; i < W; ++i) xb[i] = *(const u32x4*)(t + ((i & 1) * 20 + (i >> 1)) * 1024);
  SCHED_FENCE();
#pragma unroll
  for (int i = 0; i < 40; ++i) {
    use(i, xb[i % W]);
    if (i + W < 40) xb[i % W] = *(const u32x4*)(t + (((i + W) & 1) * 20 + ((i + W) >> 1)) * 1024);
    if (i % 2 == 1 && i < 10) dma(i >> 1);
    SCHED_FENCE();
  }
}

DEV void qk_role(const Params& p, rsrc_t ws, int h, int par, unsigned char* lds) {
  const int tid = threadIdx.x, l = tid & 63;
  const int w = __builtin_amdgcn_readfirstlane(tid >> 6);
  const int mat = w >> 2;
  u32x4 wt[2][20];
  load_qk(p, h, w, l, lds, wt);
  GSTAMP(1);
  const u32 dst = (mat ? OFF_KX : OFF_QX) + (u32)((h * 8 + (w & 3)) * 1024);
  const int n = par ? S0 / 2 : NSEG - S0 / 2;
  stream_tiles<2, false>(p.ws + OFF_XF, [&](int i) { return tile_seg(par, i) * 40960; }, n, n, lds,
    [&](int i, const unsigned char* t, auto dma) {
      f32x4 acc[2][2] = {};
      sweep40<8>(t + l * 16, dma, [&](int f, u32x4 xb) {
        acc[0][f & 1] = mfma(wt[0][f >> 1], xb, acc[0][f & 1]);
        acc[1][f & 1] = mfma(wt[1][f >> 1], xb, acc[1][f & 1]);
      });
#pragma unroll
      for (int tt = 0; tt < 2; ++tt) {
        u32x4 o = {pk2(acc[0][tt][0], acc[0][tt][1]), pk2(acc[0][tt][2], acc[0][tt][3]), pk2(acc[1][tt][0], acc[1][tt][1]), pk2(acc[1][tt][2], acc[1][tt][3])};
        st16c(ws, l * 16 + tt * 4096, dst + (u32)tile_seg(par, i) * (32 * 8192), o);
      }
    },
    [&](int k) { if (w == 0) st4c(ws, l == 0 ? 0u : 0x80000000u, OFF_PQK + (u32)(h * 128 + par * 4), (u32)k); });
}

DEV void vu_role(const Params& p, rsrc_t ws, int h, int par, unsigned char* lds) {
  const int tid = threadIdx.x, l = tid & 63, c16 = l & 15, g = l >> 4;
  const int w = __builtin_amdgcn_readfirstlane(tid >> 6);
  u32x4 wv[20], wo[5][4];
  load_vu(p, h, w, l, lds, wv, wo);
  GSTAMP(1);
  unsigned char* vs = lds + 81920;
  const u32 uoff = (u32)(((c16 >> 1) & 1) * 16384 + h * 512 + g * 128 + (2 * (c16 >> 2) + (c16 & 1)) * 16);
  const int n = par ? S0 / 2 : NSEG - S0 / 2;
  stream_tiles<5, false>(p.ws + OFF_XF, [&](int i) { return tile_seg(par, i) * 40960; }, n, n + 1, lds,
    [&](int i, const unsigned char* t, auto dma) {
      u32x4 vf[2][4];
#pragma unroll
      for (int tt = 0; tt < 2; ++tt)
#pragma unroll
        for (int ks = 0; ks < 4; ++ks) vf[tt][ks] = *(const u32x4*)(vs + ((i + 1) & 1) * 8192 + ((tt * 4 + ks) * 64 + l) * 16);
      const u32 uo = i ? uoff + OFF_UX + (u32)((tile_seg(par, i - 1) * 80 + 10 * w) * 16384) : 0x80000000u;
      f32x4 av[2] = {}, cu[2] = {};
      u32x4 ou[2];
      sweep40<6>(t + l * 16, dma, [&](int f, u32x4 xb) {
        av[f & 1] = mfma(wv[f >> 1], xb, av[f & 1]);
        cu[f & 1] = mfma(vf[f & 1][(f >> 1) & 3], wo[f >> 3][(f >> 1) & 3], cu[f & 1]);
        if ((f & 7) == 7) {
          const int ct = f >> 3;
          u32x4 o = {pk2(cu[0][0], cu[0][1]), pk2(cu[0][2], cu[0][3]), pk2(cu[1][0], cu[1][1]), pk2(cu[1][2], cu[1][3])};
          cu[0] = (f32x4){0, 0, 0, 0}; cu[1] = (f32x4){0, 0, 0, 0};
          if (ct == 0) ou[0] = o;
          else { if (ct == 1) st16c(ws, uo, 0u, ou[0]); st16c(ws, uo, (u32)ct * 32768, o); }
        }
      });
#pragma unroll
      for (int tt = 0; tt < 2; ++tt) {
        u32x2 o = {pk2(av[tt][0], av[tt][1]), pk2(av[tt][2], av[tt][3])};
        *(u32x2*)(vs + (i & 1) * 8192 + ((tt * 4 + (w >> 1)) * 64 + l) * 16 + (w & 1) * 8) = o;
      }
    },
    [&](int k) { if (w == 0 && k >= 2) st4c(ws, l == 0 ? 0u : 0x80000000u, OFF_PVU + (u32)(par * 128 + h * 4), (u32)(k - 1)); });
}

DEV f32x2 cmul(f32x2 a, f32x2 b) { return (f32x2){a.x * b.x - a.y * b.y, a.x * b.y + a.y * b.x}; }
DEV f32x2 cmulc(f32x2 a, f32x2 b) { return (f32x2){a.x * b.x + a.y * b.y, a.y * b.x - a.x * b.y}; }
template <bool INV> DEV f32x2 rot90(f32x2 a) { return INV ? (f32x2){-a.y, a.x} : (f32x2){a.y, -a.x}; }
template <bool INV> DEV void fft4(f32x2& a, f32x2& b, f32x2& c, f32x2& d) {
  f32x2 s0 = a + c, d0 = a - c, s1 = b + d, d1 = rot90<INV>(b - d);
  a = s0 + s1; b = d0 + d1; c = s0 - s1; d = d0 - d1;
}
template <bool INV> DEV f32x2 twc(f32x2 a, float c, float s) {
  return INV ? (f32x2){a.x * c - a.y * s, a.y * c + a.x * s} : (f32x2){a.x * c + a.y * s, a.y * c - a.x * s};
}
template <bool INV> DEV void fft16(f32x2 (&v)[16]) {
  constexpr float C1 = 0.92387953251128674f, S1 = 0.38268343236508977f, R = 0.70710678118654752f;
#pragma unroll
  for (int i = 0; i < 4; ++i) fft4<INV>(v[i], v[i + 4], v[i + 8], v[i + 12]);
  v[5] = twc<INV>(v[5], C1, S1);  v[9] = twc<INV>(v[9], R, R);     v[13] = twc<INV>(v[13], S1, C1);
  v[6] = twc<INV>(v[6], R, R);    v[10] = rot90<INV>(v[10]);       v[14] = twc<INV>(v[14], -R, R);
  v[7] = twc<INV>(v[7], S1, C1);  v[11] = twc<INV>(v[11], -R, R);  v[15] = twc<INV>(v[15], -C1, -S1);
#pragma unroll
  for (int k1 = 0; k1 < 4; ++k1) fft4<INV>(v[4 * k1], v[4 * k1 + 1], v[4 * k1 + 2], v[4 * k1 + 3]);
  f32x2 t;
#define SWP(a, b) t = v[a]; v[a] = v[b]; v[b] = t;
  SWP(1, 4) SWP(2, 8) SWP(3, 12) SWP(6, 9) SWP(7, 13) SWP(11, 14)
#undef SWP
}
template <bool INV> DEV void fft8(f32x2 (&v)[8]) {
  constexpr float R = 0.70710678118654752f;
  fft4<INV>(v[0], v[2], v[4], v[6]);
  fft4<INV>(v[1], v[3], v[5], v[7]);
  f32x2 o1 = twc<INV>(v[3], R, R), o2 = rot90<INV>(v[5]), o3 = twc<INV>(v[7], -R, R), o0 = v[1];
  f32x2 e0 = v[0], e1 = v[2], e2 = v[4], e3 = v[6];
  v[0] = e0 + o0; v[4] = e0 - o0; v[1] = e1 + o1; v[5] = e1 - o1; v[2] = e2 + o2; v[6] = e2 - o2; v[3] = e3 + o3; v[7] = e3 - o3;
}

DEV void fft_cols(const Params& p, rsrc_t ws, int q, unsigned char* lds) {
  const int tid = threadIdx.x, c = tid >> 7, t = tid & 127;
  f32x2* tw = (f32x2*)lds;
  f32x2* buf = (f32x2*)(lds + 16384) + c * (16 * 136);
  f32x2* fin = (f32x2*)(lds + 16384);
  __syncthreads();
  for (int m = tid; m < 2048; m += 512) { float sn, cs; sincospif((float)m * (1.0f / 1024.0f), &sn, &cs); tw[m] = (f32x2){cs, -sn}; }
  f32x2 keep[16];
  const int npass = (q == 0) ? 2 : 1;
  for (int pass = npass - 1; pass >= 0; --pass) {
    f32x2 v[16];
#pragma unroll
    for (int a = 0; a < 16; ++a)
      v[a] = __builtin_bit_cast(f32x2, ld8c(ws, (u32)(t * 16 + (c & 1) * 8 + (c >> 1) * 32768), OFF_FX + (u32)(2 * q * 32768 + a * 2048)));
    if (q == 0 && c == 0) {
#pragma unroll
      for (int a = 0; a < 16; ++a) v[a] = (f32x2){pass ? v[a].y : v[a].x, 0.f};
    }
    u32 wreg[16];
#pragma unroll
    for (int i = 0; i < 2; ++i)
#pragma unroll
      for (int kc = 0; kc < 8; ++kc) {
        const int pp = t + 128 * i, k = (pp >> 4) + 16 * (pp & 15) + 256 * kc;
        wreg[i * 8 + kc] = ld4c(ws, (u32)(k * 16 + c * 4), OFF_WT + (u32)(pass ? 80 : q) * 32768);
      }
    __syncthreads();
    fft16<false>(v);
#pragma unroll
    for (int ka = 0; ka < 16; ++ka) buf[ka * 136 + t] = ka ? cmul(v[ka], tw[t * ka]) : v[ka];
    __syncthreads();
    const int ka2 = t >> 3, c2 = t & 7;
#pragma unroll
    for (int b = 0; b < 16; ++b) v[b] = buf[ka2 * 136 + 8 * b + c2];
    fft16<false>(v);
#pragma unroll
    for (int kb = 0; kb < 16; ++kb) buf[ka2 * 136 + 8 * kb + c2] = kb ? cmul(v[kb], tw[16 * c2 * kb]) : v[kb];
    __syncthreads();
#pragma unroll
    for (int i = 0; i < 2; ++i) {
      const int pp = t + 128 * i;
      f32x2* e = buf + (pp >> 4) * 136 + 8 * (pp & 15);
      f32x2 u[8];
#pragma unroll
      for (int j = 0; j < 8; ++j) u[j] = e[j];
      fft8<false>(u);
#pragma unroll
      for (int kc = 0; kc < 8; ++kc) {
        const u32 wv = wreg[i * 8 + kc];
        const f32x2 wf = {__uint_as_float(wv << 16) * (1.0f / (2048.0f * 640.0f)), __uint_as_float(wv & 0xFFFF0000u) * (1.0f / (2048.0f * 640.0f))};
        u[kc] = cmul(u[kc], wf);
      }
      fft8<true>(u);
#pragma unroll
      for (int j = 0; j < 8; ++j) e[j] = u[j];
    }
    __syncthreads();
#pragma unroll
    for (int kb = 0; kb < 16; ++kb) { f32x2 x = buf[ka2 * 136 + 8 * kb + c2]; v[kb] = kb ? cmulc(x, tw[16 * c2 * kb]) : x; }
    fft16<true>(v);
#pragma unroll
    for (int b = 0; b < 16; ++b) buf[ka2 * 136 + 8 * b + c2] = v[b];
    __syncthreads();
#pragma unroll
    for (int ka = 0; ka < 16; ++ka) { f32x2 x = buf[ka * 136 + t]; v[ka] = ka ? cmulc(x, tw[t * ka]) : x; }
    fft16<true>(v);
    if (pass == 1) {
#pragma unroll
      for (int a = 0; a < 16; ++a) keep[a] = v[a];
    } else {
      if (q == 0 && c == 0) {
#pragma unroll
        for (int a = 0; a < 16; ++a) v[a].y = keep[a].x;
      }
      __syncthreads();
#pragma unroll
      for (int a = 0; a < 16; ++a) fin[(128 * a + t) * 4 + c] = v[a];
      __syncthreads();
    }
  }
#pragma unroll
  for (int i = 0; i < 4; ++i) {
    const int n = tid + 512 * i;
    const f32x4 x0 = *(const f32x4*)(fin + n * 4), x1 = *(const f32x4*)(fin + n * 4 + 2);
    u32x4 hi = {pk2(x0[0], x0[1]), pk2(x0[2], x0[3]), pk2(x1[0], x1[1]), pk2(x1[2], x1[3])};
    u32x4 lo = {pk2(x0[0] - __uint_as_float(hi[0] << 16), x0[1] - __uint_as_float(hi[0] & 0xFFFF0000u)),
                pk2(x0[2] - __uint_as_float(hi[1] << 16), x0[3] - __uint_as_float(hi[1] & 0xFFFF0000u)),
                pk2(x1[0] - __uint_as_float(hi[2] << 16), x1[1] - __uint_as_float(hi[2] & 0xFFFF0000u)),
                pk2(x1[2] - __uint_as_float(hi[3] << 16), x1[3] - __uint_as_float(hi[3] & 0xFFFF0000u))};
    const u32 o = (u32)((n >> 4) * 40960 + (q >> 2) * 1024 + ((q & 3) * 16 + (n & 15)) * 16);
    st16c(ws, o, OFF_FY, hi);
    st16c(ws, o + 20480, OFF_FY, lo);
  }
}

DEV void fw_task(const Params& p, rsrc_t ws, int k) {
  for (int q = k * 2560 + threadIdx.x; q < (k + 1) * 2560; q += 512) {
    const int which = q / 51200, r = q % 51200, tile = r / 1280, ks = (r % 1280) >> 6, ln = r & 63;
    u32x4 hi, lo;
    float val[8];
#pragma unroll
    for (int j = 0; j < 8; ++j) {
      const int aa = 16 * tile + (ln & 15), bb = 32 * ks + 8 * (ln >> 4) + j;
      const int kp = which ? bb : aa, d = which ? aa : bb;
      const int kk = (kp == 1) ? 320 : (kp >> 1);
      float sn, cs;
      sincospif((float)((kk * d) % 640) * (1.0f / 320.0f), &sn, &cs);
      float x = (kp & 1) && kp != 1 ? -sn : cs;
      if (which && kp > 1) x *= 2.0f;
      val[j] = x;
    }
#pragma unroll
    for (int j = 0; j < 4; ++j) { hi[j] = pk2(val[2 * j], val[2 * j + 1]); lo[j] = pk2lo(val[2 * j], val[2 * j + 1], hi[j]); }
    const u32 o = (which ? OFF_FW3 : OFF_FW1) + (u32)((tile * 40 + ks) * 1024 + ln * 16);
    st16c(ws, o, 0, hi);
    st16c(ws, o + 20480, 0, lo);
  }
  VM_DRAIN();
  __syncthreads();
  if (threadIdx.x == 0) __hip_atomic_fetch_add((u32*)(p.ws + OFF_FWC), 1u, __ATOMIC_RELAXED, __HIP_MEMORY_SCOPE_AGENT);
}
DEV void wt_task(const Params& p, rsrc_t ws, int k, unsigned char* lds) {
  const int tid = threadIdx.x;
  const rsrc_t rr = mk_rsrc(p.wr, 2048 * 642), ri = mk_rsrc(p.wi, 2048 * 642);
  for (int blk = 0; blk < 2; ++blk) {
    const int row0 = k * 128 + blk * 64;
    __syncthreads();
#pragma unroll
    for (int i = 0; i < 6; ++i) {
      const int c = tid + 512 * i;
      if (c < 2568) {
        *(u32x4*)(lds + c * 16) = ld16(rr, (u32)c * 16, (u32)row0 * 642);
        *(u32x4*)(lds + 41088 + c * 16) = ld16(ri, (u32)c * 16, (u32)row0 * 642);
      }
    }
    __syncthreads();
    const int r = tid & 63;
    const u16* a = (const u16*)lds + r * 321;
    for (int q = tid >> 6; q < 81; q += 8) {
      u32x4 o;
#pragma unroll
      for (int j = 0; j < 4; ++j) { const int col = q < 80 ? 4 * q + j : 320; o[j] = (u32)a[col] | ((u32)a[20544 + col] << 16); }
      st16c(ws, (u32)(row0 + r) * 16, OFF_WT + (u32)q * 32768, o);
    }
  }
  VM_DRAIN();
  __syncthreads();
  if (tid == 0) __hip_atomic_fetch_add((u32*)(p.ws + OFF_FWC), 1u, __ATOMIC_RELAXED, __HIP_MEMORY_SCOPE_AGENT);
}
DEV void wait_fw(rsrc_t ws) {
  if (threadIdx.x < 64) { bool dead = false; wait_prog(ws, OFF_FWC, 1, 56, threadIdx.x, dead, 1); }
  __syncthreads();
}
DEV void f1_task(const Params& p, rsrc_t ws, int type, int rg, unsigned char* lds) {
  const int tid = threadIdx.x, l = tid & 63, c16 = l & 15, g = l >> 4;
  const int w = __builtin_amdgcn_readfirstlane(tid >> 6);
  const int tile = type * 8 + w;
  wait_fw(ws);
  u32x4 wt[2][20];
#pragma unroll
  for (int i = 0; i < 2; ++i)
#pragma unroll
    for (int ks = 0; ks < 20; ++ks) { wt[i][ks] = ld16c(ws, l * 16, OFF_FW1 + (u32)((tile * 40 + i * 20 + ks) * 1024)); THROTTLE(ks); }
  __builtin_amdgcn_s_waitcnt(0x0F70);
  stream_tiles<2, true>((const unsigned char*)p.ax + rg * 4 * 40960, [](int i) { return i * 40960; }, 4, 4, lds,
    [&](int i, const unsigned char* t, auto dma) {
      f32x4 acc[2][2] = {};
      sweep40<8>(t + l * 16, dma, [&](int f, u32x4 xb) {
        acc[0][f & 1] = mfma(wt[0][f >> 1], xb, acc[0][f & 1]);
        acc[1][f & 1] = mfma(wt[1][f >> 1], xb, acc[1][f & 1]);
      });
#pragma unroll
      for (int tt = 0; tt < 2; ++tt) {
        f32x4 o = acc[0][tt] + acc[1][tt];
        st16c(ws, (u32)(g * 32768 + c16 * 16 + tt * 256), OFF_FX + (u32)(tile * 4 * 32768 + (rg * 4 + i) * 512), __builtin_bit_cast(u32x4, o));
      }
    },
    [&](int k) { if (k == 4 && tid == 0) __hip_atomic_fetch_add((u32*)(p.ws + OFF_F1C + type * 128), 1u, __ATOMIC_RELAXED, __HIP_MEMORY_SCOPE_AGENT); });
}
DEV void f3_task(const Params& p, rsrc_t ws, int type, int rg, unsigned char* lds) {
  const int tid = threadIdx.x, l = tid & 63, c16 = l & 15, g = l >> 4;
  const int w = __builtin_amdgcn_readfirstlane(tid >> 6);
  const int tile = type * 8 + w;
  wait_fw(ws);
  u32x4 wt[2][20];
#pragma unroll
  for (int i = 0; i < 2; ++i)
#pragma unroll
    for (int ks = 0; ks < 20; ++ks) { wt[i][ks] = ld16c(ws, l * 16, OFF_FW3 + (u32)((tile * 40 + i * 20 + ks) * 1024)); THROTTLE(ks); }
  __builtin_amdgcn_s_waitcnt(0x0F70);
  if (w == 0) {
    bool dead = false;
    wait_prog(ws, OFF_F2C, 1, 80, l, dead, 1);
    __builtin_amdgcn_fence(__ATOMIC_ACQUIRE, "agent");
    VM_DRAIN();
  }
  __syncthreads();
  u16* outp = p.out + (rg * 128 + c16) * 1280 + tile * 16 + 4 * g;
  stream_tiles<1, false>(p.ws + OFF_FY + rg * 8 * 40960, [](int i) { return i * 40960; }, 8, 8, lds,
    [&](int i, const unsigned char* t, auto dma) {
      f32x4 a0 = {0, 0, 0, 0}, a1 = {0, 0, 0, 0}, a2 = {0, 0, 0, 0};
      sweep40<10>(t + l * 16, dma, [&](int f, u32x4 y) {
        if (f & 1) a2 = mfma(wt[0][f >> 1], y, a2);
        else { a0 = mfma(wt[0][f >> 1], y, a0); a1 = mfma(wt[1][f >> 1], y, a1); }
      });
      f32x4 o = a0 + (a1 + a2);
      u32x2 ov = {pk2(o[0], o[1]), pk2(o[2], o[3])};
      *(u32x2*)(outp + i * 16 * 1280) = ov;
    },
    [&](int) {});
}

DEV void out_task(const Params& p, rsrc_t ws, int s, int cg, unsigned char* lds) {
  const int tid = threadIdx.x, l = tid & 63, c16 = l & 15, g = l >> 4;
  const int w = __builtin_amdgcn_readfirstlane(tid >> 6);
  const int G = 8 * cg + w;
  const int pp = (c16 >> 1) & 1, j = 2 * (c16 >> 2) + (c16 & 1);
  const u32 ox = (u32)(pp * 16384 + g * 128 + j * 16), om = (u32)(pp * 4096 + g * 128 + j * 16);
  bool dead = false;
  if (w == 0) wait_prog(ws, OFF_PVU + (u32)seg_prod(s) * 128, 32, seg_need(s), l, dead, 1);
  __syncthreads();
  u32x4 ua[32];
#pragma unroll
  for (int h = 0; h < 32; ++h) { ua[h] = ld16c(ws, ox + (h & 7) * 512, OFF_UX + (u32)((s * 80 + 2 * G) * 16384 + (h >> 3) * 4096)); THROTTLE(h); }
  SCHED_FENCE();
  if (w < 2) {
    wait_prog(ws, OFF_PPX + (u32)(cg + 5 * (s & 3)) * 128, 1, (u32)s, l, dead, 2);
    if (w == 0) wait_prog(ws, OFF_PPX, 32, (u32)s + 1, l, dead, 1, 128);
    else wait_prog(ws, OFF_PUM + (u32)cg * 2048, 16, (u32)s + 1, l, dead, 1, 128);
  }
  __syncthreads();
  u32x4 pr[10], ub[8];
#pragma unroll
  for (int i = 0; i < 8; ++i) pr[i] = ld16c(ws, tid * 16, OFF_PXX + (u32)(s * 65536 + i * 8192));
#pragma unroll
  for (int i = 0; i < 2; ++i) pr[8 + i] = ld16c(ws, tid * 16, OFF_PXM + (u32)(s * 16384 + i * 8192));
#pragma unroll
  for (int q = 0; q < 8; ++q) ub[q] = ld16c(ws, om + q * 512, OFF_UM + (u32)((s * 80 + 2 * G) * 4096));
#pragma unroll
  for (int i = 0; i < 10; ++i) *(u32x4*)(lds + (i * 512 + tid) * 16) = pr[i];
  __syncthreads();
  f32x4 acc[2] = {};
  {
    const unsigned char* px = lds + l * 16;
    const unsigned char* pm = lds + 65536 + (g * 16 + c16) * 16;
    auto rd = [&](int i) { return i < 64 ? *(const u32x4*)(px + i * 1024) : *(const u32x4*)(pm + (i & 1) * 8192 + ((i - 64) >> 1) * 1024); };
    u32x4 pb[12];
#pragma unroll
    for (int i = 0; i < 12; ++i) pb[i] = rd(i);
    SCHED_FENCE();
#pragma unroll
    for (int i = 0; i < 80; ++i) {
      acc[i & 1] = mfma(i < 64 ? ua[i >> 1] : ub[(i - 64) >> 1], pb[i % 12], acc[i & 1]);
      if (i + 12 < 80) pb[i % 12] = rd(i + 12);
      SCHED_FENCE();
    }
  }
#pragma unroll
  for (int tt = 0; tt < 2; ++tt) {
    u32x2 o = {pk2(acc[tt][0], acc[tt][1]), pk2(acc[tt][2], acc[tt][3])};
    *(u32x2*)(p.out + (32 * s + 16 * tt + c16) * 1280 + 640 + 16 * G + 4 * g) = o;
  }
}

DEV void prefetch_role(rsrc_t ws, int x, int k) {
  const int l = threadIdx.x & 63;
  const int w = __builtin_amdgcn_readfirstlane(threadIdx.x >> 6);
  if (w >= PFW) return;
  bool dead = false;
  u32 seen0 = 0, seen1 = 0;
  for (int s = 1; s < NSEG; ++s) {
    const u32 prog = wait_prog(ws, OFF_PUM + (u32)(x * 16 + k) * 128, 1, (u32)(s > PFD ? s - PFD : 0), l, dead, 1);
    if ((u32)s <= prog + 1) { s = (int)prog + 1; continue; }
    const u32 need = seg_need(s);
    if (seg_prod(s)) { if (seen1 < need) seen1 = wait_prog(ws, OFF_PVU + 128, 32, need, l, dead, 1); }
    else { if (seen0 < need) seen0 = wait_prog(ws, OFF_PVU, 32, need, l, dead, 1); }
#pragma nounroll
    for (int i = w; i < 14 * (8192 / (64 * PFSTR)); i += PFW) {
      const int u = k * 14 + i / (8192 / (64 * PFSTR));
      u32 o;
      if (x == 0) o = ((u & 1) ? OFF_QX : OFF_KX) + (u32)((s * 32 + (u >> 1)) * 8192);
      else if (x == 3 && u >= 48) o = ((u & 1) ? OFF_QX : OFF_KX) + (u32)((s * 32 + 28 + ((u - 48) >> 1)) * 8192);
      else o = OFF_UX + (u32)((s * 80 + (x - 1) * 28) * 16384 + u * 8192);
      asm volatile("buffer_load_dword v255, %0, %1, %2 offen sc1\n\ts_waitcnt vmcnt(%3)"
                   :: "v"(l * PFSTR), "s"(ws), "s"(o + (u32)(i % (8192 / (64 * PFSTR))) * (64 * PFSTR)), "n"(PFQ) : "memory", "v255");
      __builtin_amdgcn_s_sleep(PFS);
    }
  }
  VM_DRAIN();
}

constexpr int T_SET = 56, T_F1 = 80, T_F2 = 80, T_F3 = 80, T_OUT = 320;
DEV int next_ticket(const Params& p, unsigned char* lds, u32 off) {
  u32* tk = (u32*)(lds + LDS_MAIN - 16);
  __syncthreads();
  if (threadIdx.x == 0) *tk = __hip_atomic_fetch_add((u32*)(p.ws + off), 1u, __ATOMIC_RELAXED, __HIP_MEMORY_SCOPE_AGENT);
  __syncthreads();
  return __builtin_amdgcn_readfirstlane(*tk);
}

__global__ __launch_bounds__(512, 2) void k_main(Params p, int mode) {
  extern __shared__ __attribute__((aligned(16))) unsigned char lds[];
  const rsrc_t ws = mk_rsrc(p.ws, WS_BYTES);
  const u32 cb = comm_base(p.ws);
  const int b = blockIdx.x, x = b & 7, slot = b >> 3;
  if (mode & 1) GSTAMP(0);
  const int cidx = (x >= 4 || slot >= 28) ? 1000 : (x == 0 ? slot : ((x == 3 && slot >= 24) ? 4 + slot : 32 + (x - 1) * 28 + slot));
  if (cidx < 112) {
    if (!(mode & 1)) return;
    if (cidx < 32) score_role(p, ws, cb, cidx, lds);
    else value_role(p, ws, cb, cidx - 32, lds);
    GSTAMP(3);
    return;
  }
  if (x < 4) {
    if (mode & 1) {
      prefetch_role(ws, x, slot - 28);
    }
    return;
  }
  if (x >= 4 && (mode & 2)) {
    const int h = (x - 4) * 8 + (slot >> 2);
    if (slot & 2) vu_role(p, ws, h, slot & 1, lds);
    else qk_role(p, ws, h, slot & 1, lds);
    GSTAMP(2);
  }
  if (!(mode & 4)) return;
  const bool outonly = (slot & 3) == 3 && (slot & 4);
  if (!outonly) {
    int t = next_ticket(p, lds, OFF_TICKET) - T_SET;
    while (t < 0) { const int k = t + T_SET; if (k < 40) fw_task(p, ws, k); else wt_task(p, ws, k - 40, lds); t = next_ticket(p, lds, OFF_TICKET) - T_SET; }
    while (t < T_F1) { f1_task(p, ws, t % 5, t / 5, lds); t = next_ticket(p, lds, OFF_TICKET) - T_SET; }
    GSTAMP(4);
    while (t < T_F1 + T_F2) {
      const int q = t - T_F1;
      if (threadIdx.x < 64) { bool dead = false; wait_prog(ws, OFF_F1C + (u32)(q >> 4) * 128, 1, 16, threadIdx.x, dead, 1); }
      wait_fw(ws);
      fft_cols(p, ws, q, lds);
      VM_DRAIN();
      __syncthreads();
      if (threadIdx.x == 0) __hip_atomic_fetch_add((u32*)(p.ws + OFF_F2C), 1u, __ATOMIC_RELAXED, __HIP_MEMORY_SCOPE_AGENT);
      t = next_ticket(p, lds, OFF_TICKET) - T_SET;
    }
    GSTAMP(5);
    while (t < T_F1 + T_F2 + T_F3) { const int q = t - T_F1 - T_F2; f3_task(p, ws, q % 5, q / 5, lds); t = next_ticket(p, lds, OFF_TICKET) - T_SET; }
  }
  GSTAMP(6);
  for (int t = next_ticket(p, lds, OFF_TICKET2); t < T_OUT; t = next_ticket(p, lds, OFF_TICKET2)) out_task(p, ws, t / 5, t % 5, lds);
  GSTAMP(3);
}

extern "C" void kernel_launch(void* const* d_in, const int* in_sizes, int n_in, void* d_out, int out_size, void* d_ws, size_t ws_size,
                              hipStream_t stream) {
  Params p;
  p.ax = (const u16*)d_in[0]; p.wr = (const u16*)d_in[1]; p.wi = (const u16*)d_in[2];
  p.x = (const u16*)d_in[3]; p.mem0 = (const u16*)d_in[4]; p.wq = (const u16*)d_in[5]; p.wk = (const u16*)d_in[6];
  p.wv = (const u16*)d_in[7]; p.wo = (const u16*)d_in[8];
  p.out = (u16*)d_out; p.ws = (unsigned char*)d_ws;
  (void)hipFuncSetAttribute((const void*)k_prep, hipFuncAttributeMaxDynamicSharedMemorySize, 24 * 1024);
  (void)hipFuncSetAttribute((const void*)k_main, hipFuncAttributeMaxDynamicSharedMemorySize, LDS_MAIN);
  k_prep<<<256, 512, 24 * 1024, stream>>>(p);
  k_main<<<256, 512, LDS_MAIN, stream>>>(p, 7);
}
```

```cpp
#include <hip/hip_runtime.h>
#include <hip/hip_bf16.h>
#include <stdint.h>
#define BAR4
#define BULK1

typedef unsigned short u16;
typedef unsigned int u32;
typedef unsigned long long u64;
typedef __bf16 bf16x2 __attribute__((ext_vector_type(2)));
typedef __bf16 bf16x8 __attribute__((ext_vector_type(8)));
typedef __bf16 bf16x16 __attribute__((ext_vector_type(16)));
typedef float f32x2 __attribute__((ext_vector_type(2)));
typedef float f32x4 __attribute__((ext_vector_type(4)));
typedef u32 u32x2 __attribute__((ext_vector_type(2)));
typedef u32 u32x4 __attribute__((ext_vector_type(4)));
typedef u32 u32x8 __attribute__((ext_vector_type(8)));
typedef __amdgpu_buffer_rsrc_t rsrc_t;
#define DEV __device__ __forceinline__

constexpr int NSEG = 64;
constexpr u32 MB = 1u << 20;
constexpr u32 OFF_WF = 6 * MB;
constexpr u32 OFF_WOF = 21 * MB;
constexpr u32 OFF_BF = 26 * MB;
constexpr u32 OFF_XF = 51 * MB;
constexpr u32 OFF_AXF = 54 * MB;
constexpr u32 OFF_KX = 57 * MB;
constexpr u32 OFF_QX = 73 * MB;
constexpr u32 OFF_UX = 89 * MB;
constexpr u32 OFF_UM = 169 * MB;
constexpr u32 OFF_PXX = 189 * MB;
constexpr u32 OFF_PXM = 193 * MB;
constexpr u32 OFF_FX = 194 * MB;
constexpr u32 OFF_FY = 200 * MB;
constexpr u32 OFF_FLAGS = 206 * MB;
constexpr u32 OFF_FW1 = 208 * MB;
constexpr u32 OFF_FW3 = 210 * MB;
constexpr u32 OFF_SIDE = 212 * MB;
constexpr u32 SIDE_STEP = 112 * 128;
constexpr u32 OFF_WT = 213 * MB;
constexpr u32 WS_BYTES = 216 * MB;
constexpr u32 STEP_BYTES = 65536;
constexpr u32 SENT = 0xFFFFFFFFu;
constexpr u32 SPIN_CAP = 1u << 16;
constexpr u32 OFF_PQK = OFF_FLAGS + 0xA000;
constexpr u32 OFF_PVU = OFF_FLAGS + 0xB000;
constexpr u32 OFF_PPX = OFF_FLAGS;
constexpr u32 OFF_PUM = OFF_FLAGS + 0x1000;
constexpr u32 OFF_TICKET = OFF_FLAGS + 0xB400;
constexpr u32 OFF_F1C = OFF_FLAGS + 0xB480;
constexpr u32 OFF_F2C = OFF_FLAGS + 0xB800;
constexpr u32 OFF_FWC = OFF_FLAGS + 0xB880;
constexpr u32 OFF_TICKET2 = OFF_FLAGS + 0xB900;
constexpr u32 OFF_VOTE = OFF_FLAGS + 0xBA00;
constexpr int LDS_MAIN = 97 * 1024;
#define S0 16
__host__ __device__ constexpr int seg_prod(int s) { return s < S0 ? (s & 1) : 0; }
__host__ __device__ constexpr u32 seg_need(int s) { return s < S0 ? (u32)(s >> 1) + 1 : (u32)(s - S0 / 2) + 1; }
__host__ __device__ constexpr int tile_seg(int par, int i) { return i < S0 / 2 ? 2 * i + par : i + S0 / 2; }

struct Params {
  const u16* ax; const u16* wr; const u16* wi;
  const u16* x; const u16* mem0; const u16* wq; const u16* wk; const u16* wv; const u16* wo;
  u16* out; unsigned char* ws;
};

DEV rsrc_t mk_rsrc(const void* p, u32 bytes) { return __builtin_amdgcn_make_buffer_rsrc((void*)p, 0, bytes, 0x00020000); }
DEV u32x4 ld16(rsrc_t r, u32 vo, u32 so) { return __builtin_amdgcn_raw_buffer_load_b128(r, vo, so, 0); }
DEV u32x4 ld16c(rsrc_t r, u32 vo, u32 so) { return __builtin_amdgcn_raw_buffer_load_b128(r, vo, so, 16); }
DEV u32x2 ld8c(rsrc_t r, u32 vo, u32 so) { return __builtin_amdgcn_raw_buffer_load_b64(r, vo, so, 16); }
DEV u32 ld4c(rsrc_t r, u32 vo, u32 so) { return __builtin_amdgcn_raw_buffer_load_b32(r, vo, so, 16); }
DEV void st16(rsrc_t r, u32 vo, u32 so, u32x4 v) { __builtin_amdgcn_raw_buffer_store_b128(v, r, vo + so, 0, 0); }
DEV void st16c(rsrc_t r, u32 vo, u32 so, u32x4 v) { __builtin_amdgcn_raw_buffer_store_b128(v, r, vo + so, 0, 16); }
DEV void st8c(rsrc_t r, u32 vo, u32 so, u32x2 v) { __builtin_amdgcn_raw_buffer_store_b64(v, r, vo, so, 16); }
DEV void st4c(rsrc_t r, u32 vo, u32 so, u32 v) { __builtin_amdgcn_raw_buffer_store_b32(v, r, vo, so, 16); }
DEV u32 pk2(float a, float b) { bf16x2 v = {(__bf16)a, (__bf16)b}; return __builtin_bit_cast(u32, v); }
DEV u32 pk2s(float a, float b) { u32 v = pk2(a, b); return v < 0xFFFFFFFEu ? v : 0xFFFFFFFEu; }
DEV u32 pk2lo(float a, float b, u32 hi) { return pk2(a - __uint_as_float(hi << 16), b - __uint_as_float(hi & 0xFFFF0000u)); }
DEV f32x4 mfma(u32x4 a, u32x4 b, f32x4 c) {
  return __builtin_amdgcn_mfma_f32_16x16x32_bf16(__builtin_bit_cast(bf16x8, a), __builtin_bit_cast(bf16x8, b), c, 0, 0, 0);
}
DEV f32x4 smfma(u32x4 a, u32x8 b, f32x4 c, int idx) {
  return __builtin_amdgcn_smfmac_f32_16x16x64_bf16(__builtin_bit_cast(bf16x8, a), __builtin_bit_cast(bf16x16, b), c, idx, 0, 0);
}
DEV u32x8 cat(u32x4 a, u32x4 b) { return __builtin_shufflevector(a, b, 0, 1, 2, 3, 4, 5, 6, 7); }
DEV u32 comm_base(const unsigned char* ws) { return (u32)((2 * MB - ((uintptr_t)ws & (2 * MB - 1))) & (2 * MB - 1)); }
DEV u64 stamp_cyc() { u64 t; asm volatile("s_memtime %0\n\ts_waitcnt lgkmcnt(0)" : "=s"(t) :: "memory"); return t; }
DEV u32 umax3(u32 a, u32 b, u32 c) { u32 m = a > b ? a : b; return m > c ? m : c; }
DEV float fmx(float a, float b) { return __builtin_elementwise_maximum(a, b); }
#define SCHED_FENCE() __builtin_amdgcn_sched_barrier(0)
#define VM_DRAIN() asm volatile("s_waitcnt vmcnt(0)" ::: "memory")
#define RAW_BARRIER() do { asm volatile("s_waitcnt lgkmcnt(0)" ::: "memory"); __builtin_amdgcn_s_barrier(); asm volatile("" ::: "memory"); } while (0)

#define STAMP(ev)
#define STAMPV(ev, val)
#define COUNT(ev, val)
#define GSTAMP(slot)

typedef short s16x4 __attribute__((ext_vector_type(4)));
#define LAS __attribute__((address_space(3)))
DEV u32x4 tr_frag(const unsigned char* a0, const unsigned char* a1) {
  const u32x2 x = __builtin_bit_cast(u32x2, __builtin_amdgcn_ds_read_tr16_b64_v4i16((LAS s16x4*)a0));
  const u32x2 y = __builtin_bit_cast(u32x2, __builtin_amdgcn_ds_read_tr16_b64_v4i16((LAS s16x4*)a1));
  return (u32x4){x.x, x.y, y.x, y.y};
}
#define BST st16c
__global__ __launch_bounds__(512) void k_prep(Params p) {
  extern __shared__ __attribute__((aligned(16))) unsigned char lds[];
  const int b = blockIdx.x, tid = threadIdx.x, l = tid & 63, c16 = l & 15, g = l >> 4;
  const int w = __builtin_amdgcn_readfirstlane(tid >> 6);
  const rsrc_t ws = mk_rsrc(p.ws, WS_BYTES);
  const int h = (b & 7) * 4 + (b >> 6), dh = (b >> 3) & 1, cq = (b >> 4) & 3;
  const rsrc_t rv = mk_rsrc(p.wv, 640 * 8192), ro = mk_rsrc(p.wo, 640 * 8192);
  GSTAMP(8);
  u32x4 wo_r[5];
#pragma unroll
  for (int i = 0; i < 5; ++i) {
    const int c = tid + 512 * i, e = c / 20, cc = c % 20;
    wo_r[i] = ld16(ro, (u32)(e * 1280 + cc * 16), (u32)(h * 163840 + cq * 320));
  }
  u32x4 a[3][2][4];
#pragma unroll
  for (int i = 0; i < 3; ++i) {
    const int bl = (w & 3) + 4 * i;
    const u32 so = bl < 10 ? (u32)((32 * (10 * dh + bl)) * 8192 + h * 256) : 0x80000000u;
#pragma unroll
    for (int t = 0; t < 2; ++t)
#pragma unroll
      for (int ks = 0; ks < 4; ++ks)
        a[i][t][ks] = ld16(rv, (u32)((8 * (c16 >> 2) + (c16 & 3) + 4 * t) * 8192 + g * 16 + ks * 64), so);
  }
  const int q = b * 512 + tid;
  u32x4 xr[2], t0[5];
#pragma unroll
  for (int i = 0; i < 2; ++i) {
    const int r0 = q + 131072 * i < 163840 ? q + 131072 * i : q;
    const int seg = r0 / 2560, r = r0 % 2560, tt = r / 1280, ks = (r % 1280) >> 6, ln = r & 63;
    xr[i] = *(const u32x4*)(p.x + (32 * seg + 16 * tt + (ln & 15)) * 640 + 32 * ks + 8 * (ln >> 4));
  }
#pragma unroll
  for (int i = 0; i < 5; ++i) {
    const int c = q + 131072 * i;
    t0[i] = *(const u32x4*)((c < 327680 ? p.wq : p.wk - 327680 * 8) + (size_t)c * 8);
  }
  SCHED_FENCE();
  {
    const u32 cb = comm_base(p.ws);
#pragma unroll
    for (int i = 0; i < 2; ++i) {
      const u32 o = (u32)(b * 1024 + i * 512 + tid) * 16;
      const u32 L = (o >> 14) * 8192 + (o & 8191);
      if (o >= STEP_BYTES || L >= 10240) st16c(ws, cb + o, 0, (u32x4){SENT, SENT, SENT, SENT});
    }
    if (b < 6) st16c(ws, OFF_FLAGS + (u32)(b * 512 + tid) * 16, 0, (u32x4){0, 0, 0, 0});
    if (b >= 128 && b < 240) st16c(ws, OFF_SIDE + (u32)((b - 128) * 512 + tid) * 16, 0, (u32x4){SENT, SENT, SENT, SENT});
  }
  GSTAMP(9);
#pragma unroll
  for (int i = 0; i < 5; ++i) {
    const int c = tid + 512 * i, e = c / 20, cc = c % 20;
    *(u32x4*)(lds + e * 352 + (e >> 3) * 128 + cc * 16) = wo_r[i];
  }
  __syncthreads();
  GSTAMP(10);
  u32x4 bf[5][4];
  const unsigned char* rp = lds + (8 * g + ((l >> 2) & 3)) * 352 + g * 128 + (w >> 2) * 160 + (l & 3) * 8;
#pragma unroll
  for (int ct = 0; ct < 5; ++ct)
#pragma unroll
    for (int ks = 0; ks < 4; ++ks) bf[ct][ks] = tr_frag(rp + ks * (32 * 352 + 512) + ct * 32, rp + ks * (32 * 352 + 512) + 4 * 352 + ct * 32);
  const u32 vo = (u32)(((c16 >> 1) & 1) * (16 * 20 * 1024) + (16 * g + 8 * (h & 1) + 2 * (c16 >> 2) + (c16 & 1)) * 16);
#pragma unroll
  for (int i = 0; i < 3; ++i) {
    const int bl = (w & 3) + 4 * i;
    if (bl < 10) {
#pragma unroll
      for (int ct = 0; ct < 5; ++ct) {
        f32x4 c0 = {0, 0, 0, 0}, c1 = {0, 0, 0, 0};
#pragma unroll
        for (int ks = 0; ks < 4; ++ks) { c0 = mfma(a[i][0][ks], bf[ct][ks], c0); c1 = mfma(a[i][1][ks], bf[ct][ks], c1); }
        u32x4 o = {pk2(c0[0], c0[1]), pk2(c0[2], c0[3]), pk2(c1[0], c1[1]), pk2(c1[2], c1[3])};
        BST(ws, vo, OFF_BF + (u32)(((2 * (10 * cq + 5 * (w >> 2) + ct) * 16 + (h >> 1)) * 20 + 10 * dh + bl) * 1024), o);
      }
    }
  }
  GSTAMP(11);
  BST(ws, OFF_XF + (u32)q * 16, 0, xr[0]);
  if (q + 131072 < 163840) BST(ws, OFF_XF + (u32)(q + 131072) * 16, 0, xr[1]);
  if (b < 4) {
    const u32 cb = comm_base(p.ws);
    for (int i = 0; i < 2; ++i) {
      const u32 o = (u32)(b * 1024 + i * 512 + tid) * 16;
      const u32 L = (o >> 14) * 8192 + (o & 8191);
      if (L < 10240) {
        const int vv = L >> 7, t = (L >> 4) & 7;
        const u16* m = p.mem0 + t * 640 + 16 * (vv >> 1) + 2 * (vv & 1);
        u32x4 v;
        for (int k = 0; k < 4; ++k) { u32 x = *(const u32*)(m + 4 * k); v[k] = x < 0xFFFFFFFEu ? x : 0xFFFFFFFEu; }
        st16c(ws, cb + o, 0, v);
      }
    }
  }
#pragma unroll
  for (int i = 0; i < 5; ++i) asm volatile("" :: "v"(t0[i]));
}

DEV float dpp_ror8(float x) { return __uint_as_float(__builtin_amdgcn_update_dpp(0, (int)__float_as_uint(x), 0x128, 0xF, 0xF, true)); }
DEV float red_max_g(float m) {
  auto a = __builtin_amdgcn_permlane16_swap(__float_as_uint(m), __float_as_uint(m), false, false);
  m = fmx(__uint_as_float(a[0]), __uint_as_float(a[1]));
  auto b = __builtin_amdgcn_permlane32_swap(__float_as_uint(m), __float_as_uint(m), false, false);
  return fmx(__uint_as_float(b[0]), __uint_as_float(b[1]));
}
DEV float red_sum_g(float m) {
  auto a = __builtin_amdgcn_permlane16_swap(__float_as_uint(m), __float_as_uint(m), false, false);
  m = __uint_as_float(a[0]) + __uint_as_float(a[1]);
  auto b = __builtin_amdgcn_permlane32_swap(__float_as_uint(m), __float_as_uint(m), false, false);
  return __uint_as_float(b[0]) + __uint_as_float(b[1]);
}
DEV float red_max(float m) { return red_max_g(fmx(m, dpp_ror8(m))); }
DEV float red_sum(float m) { return red_sum_g(m + dpp_ror8(m)); }

#define NPROBE 3
DEV u32 probe(rsrc_t ws, u32 cb, int idx, int page, int l) {
  const u32 vo = (l == 0) ? 0u : 0x80000000u;
  const u32 so = cb + (u32)(page * 32 + 30) * STEP_BYTES + (u32)(2 + (idx >> 6)) * 16384 + (u32)(idx & 63) * 128;
  u32 b0 = ~0u, b1 = ~0u;
#pragma nounroll
  for (int rep = 0; rep < 2 * NPROBE; ++rep) {
    u64 t0 = stamp_cyc();
    st4c(ws, vo, so + (u32)(rep & 1) * 8192, SENT);
    VM_DRAIN();
    u64 t1 = stamp_cyc();
    u32 d = (u32)(t1 - t0);
    if (rep & 1) b1 = d < b1 ? d : b1; else b0 = d < b0 ? d : b0;
  }
  return b1 < b0 ? 1u : 0u;
}

DEV u32 xcc_group() { return ((u32)__builtin_amdgcn_s_getreg(20 | (3 << 11)) >> 2) & 3u; }
DEV void vote(const Params& p, int page, u32 r, int l) {
  if (l == 0) __hip_atomic_fetch_add((u32*)(p.ws + OFF_VOTE + (xcc_group() * 2 + page) * 64), r + 0x10000u, __ATOMIC_RELAXED, __HIP_MEMORY_SCOPE_AGENT);
}
DEV u32 vote_result(rsrc_t ws, int l) {
  const u32 x = ld4c(ws, (u32)(l & 1) * 64, OFF_VOTE + xcc_group() * 128);
  const u32 m = (2 * (x & 0xFFFFu) > (x >> 16)) ? 1u : 0u;
  return (u32)__builtin_amdgcn_readlane(m, 0) | ((u32)__builtin_amdgcn_readlane(m, 1) << 1);
}
#define D1 140
#define D2 12
#define D3 60
#define D4 12
#define DV 15
#define DX 25
#define DL 5
#define PFD 2
#define DLS 16
#define THR 6
#define THR_S2(x) #x
#define THR_S(x) THR_S2(x)
#define THROTTLE(i) asm volatile("s_waitcnt vmcnt(" THR_S(THR) ")" ::: "memory")
#define UMPOS 0
#define PSLEEP 0
#define PFW 1
#define PFQ 4
#define PFS 2
#define PFSTR 128
#define PROT v
#define KQPOS 0
#define DA 12
DEV u32 rtime() { return (u32)__builtin_amdgcn_s_memrealtime(); }
DEV void wait_until(u32 T) {
  u32 r = rtime();
  if ((int)(T - r) < 28) {
#pragma nounroll
    for (int i = 0; i < 64 && (int)(T - r) > 0; ++i) r = rtime();
    return;
  }
#pragma nounroll
  for (int i = 0; i < 4096 && (int)(T - r) > 35; ++i) r = rtime();
  u32 n;
  asm volatile(
    "s_memrealtime s[80:81]\n\ts_nop 2\n\ts_memrealtime s[82:83]\n\ts_nop 2\n\ts_memrealtime s[84:85]\n\ts_nop 2\n\ts_memrealtime s[86:87]\n\ts_nop 2\n\t"
    "s_memrealtime s[88:89]\n\ts_nop 2\n\ts_memrealtime s[90:91]\n\ts_nop 2\n\ts_memrealtime s[92:93]\n\ts_nop 2\n\ts_memrealtime s[94:95]\n\ts_waitcnt lgkmcnt(0)\n\t"
    "s_mov_b32 s81, 0\n\t"
    "s_cmp_eq_u32 s82, s80\n\ts_addc_u32 s81, s81, 0\n\t"
    "s_cmp_eq_u32 s84, s80\n\ts_addc_u32 s81, s81, 0\n\t"
    "s_cmp_eq_u32 s86, s80\n\ts_addc_u32 s81, s81, 0\n\t"
    "s_cmp_eq_u32 s88, s80\n\ts_addc_u32 s81, s81, 0\n\t"
    "s_cmp_eq_u32 s90, s80\n\ts_addc_u32 s81, s81, 0\n\t"
    "s_cmp_eq_u32 s92, s80\n\ts_addc_u32 s81, s81, 0\n\t"
    "s_cmp_eq_u32 s94, s80\n\ts_addc_u32 s81, s81, 0\n\t"
    "s_sub_u32 %0, %1, s80\n\t"
    "s_mul_i32 %0, %0, 3\n\t"
    "s_lshl1_add_u32 %0, s81, %0\n\t"
    "s_sub_i32 %0, %0, 64\n\t"
    "s_max_i32 %0, %0, 0\n\t"
    "s_min_i32 %0, %0, 63\n\t"
    "s_bitcmp1_b32 %0, 0\n\ts_cbranch_scc0 1f\n\ts_nop 5\n\t1:\n\t"
    "s_bitcmp1_b32 %0, 1\n\ts_cbranch_scc0 2f\n\ts_nop 7\n\t2:\n\t"
    "s_bitcmp1_b32 %0, 2\n\ts_cbranch_scc0 3f\n\ts_nop 11\n\t3:\n\t"
    "s_bitcmp1_b32 %0, 3\n\ts_cbranch_scc0 4f\n\ts_nop 15\n\ts_nop 3\n\t4:\n\t"
    "s_bitcmp1_b32 %0, 4\n\ts_cbranch_scc0 5f\n\ts_nop 15\n\ts_nop 15\n\ts_nop 3\n\t5:\n\t"
    "s_bitcmp1_b32 %0, 5\n\ts_cbranch_scc0 6f\n\ts_nop 15\n\ts_nop 15\n\ts_nop 15\n\ts_nop 15\n\ts_nop 3\n\t6:\n\t"
    : "=&s"(n) : "s"(T)
    : "scc", "memory", "s80", "s81", "s82", "s83", "s84", "s85", "s86", "s87", "s88", "s89", "s90", "s91", "s92", "s93", "s94", "s95");
}
template <int CTRL> DEV int dpp_i(int x) { return __builtin_amdgcn_update_dpp(x, x, CTRL, 0xF, 0xF, false); }
DEV int row_max(int d) {
  d = max(d, dpp_i<0x121>(d));
  d = max(d, dpp_i<0x122>(d));
  d = max(d, dpp_i<0x124>(d));
  return max(d, dpp_i<0x128>(d));
}
DEV int wave_max(int d) {
  d = row_max(d);
  return max(max(__builtin_amdgcn_readlane(d, 0), __builtin_amdgcn_readlane(d, 16)), max(__builtin_amdgcn_readlane(d, 32), __builtin_amdgcn_readlane(d, 48)));
}
DEV u32 fine_now() {
  u32 n;
  asm volatile(
    "s_memrealtime s[80:81]\n\ts_nop 2\n\ts_memrealtime s[82:83]\n\ts_nop 2\n\ts_memrealtime s[84:85]\n\ts_nop 2\n\ts_memrealtime s[86:87]\n\ts_nop 2\n\t"
    "s_memrealtime s[88:89]\n\ts_nop 2\n\ts_memrealtime s[90:91]\n\ts_nop 2\n\ts_memrealtime s[92:93]\n\ts_waitcnt lgkmcnt(0)\n\t"
    "s_mov_b32 s81, 0\n\t"
    "s_cmp_eq_u32 s82, s80\n\ts_addc_u32 s81, s81, 0\n\t"
    "s_cmp_eq_u32 s84, s80\n\ts_addc_u32 s81, s81, 0\n\t"
    "s_cmp_eq_u32 s86, s80\n\ts_addc_u32 s81, s81, 0\n\t"
    "s_cmp_eq_u32 s88, s80\n\ts_addc_u32 s81, s81, 0\n\t"
    "s_cmp_eq_u32 s90, s80\n\ts_addc_u32 s81, s81, 0\n\t"
    "s_cmp_eq_u32 s92, s80\n\ts_addc_u32 s81, s81, 0\n\t"
    "s_mul_i32 s81, s81, 11\n\t"
    "s_lshr_b32 s81, s81, 4\n\t"
    "s_add_u32 %0, s80, 4\n\t"
    "s_sub_u32 %0, %0, s81\n\t"
    : "=&s"(n) :
    : "scc", "memory", "s80", "s81", "s82", "s83", "s84", "s85", "s86", "s87", "s88", "s89", "s90", "s91", "s92", "s93");
  return n;
}
struct Side { u32x2 a, b; };
DEV Side load_side(rsrc_t ws, int s, int l) {
  Side t;
  t.a = ld8c(ws, (u32)l * 128, OFF_SIDE + (u32)s * SIDE_STEP);
  t.b = ld8c(ws, (u32)(64 + (l & 15)) * 128, OFF_SIDE + (u32)s * SIDE_STEP);
  return t;
}
DEV void schedule(rsrc_t ws, int s, int l, Side& t, u32& Rm, u32& Rp, bool& dead) {
  if (__builtin_expect(__any(umax3(t.a.x, t.a.y, t.b.x > t.b.y ? t.b.x : t.b.y) == SENT), 0)) {
#pragma nounroll
    for (u32 spins = 0; !dead; ++spins) {
      asm volatile("" ::: "memory");
      t = load_side(ws, s, l);
      if (__all(umax3(t.a.x, t.a.y, t.b.x > t.b.y ? t.b.x : t.b.y) != SENT)) break;
      if (spins > SPIN_CAP) dead = true;
    }
  }
  u32 B = Rm;
  if (__builtin_expect(__any((t.a.x | t.b.x) != 0), 0)) {
    const u32 ref = rtime();
    const int dx = t.a.x ? (int)(t.a.x - ref) : -(1 << 30), dy = t.b.x ? (int)(t.b.x - ref) : -(1 << 30);
    B = ref + (u32)wave_max(max(dx, dy)) + D4 + DX;
  }
  const u32 d = __builtin_amdgcn_readfirstlane(t.a.y);
  Rp = B + ((d >> 10) & 1023u);
  Rm = Rp + ((d >> 20) & 1023u);
}

DEV u32 gather_mem(rsrc_t ws, u32 sbp, int wr, unsigned char* dA, unsigned char* dB, int l, bool& dead) {
  u32x4 a = ld16c(ws, (u32)l * 16, sbp + (u32)wr * 1024);
  u32 b = ld4c(ws, (u32)l * 4, sbp + 16384 + (u32)wr * 256);
  SCHED_FENCE();
  *(u32x4*)dA = a;
  *(u32*)dB = b;
  u32 m = umax3(umax3(a.x, a.y, a.z), a.w, b);
  u32 spins = 0;
  if (__builtin_expect(__any(m == SENT), 0)) {
#pragma nounroll
    while (!dead) {
      ++spins;
      asm volatile("" ::: "memory");
      a = ld16c(ws, (u32)l * 16, sbp + (u32)wr * 1024);
      b = ld4c(ws, (u32)l * 4, sbp + 16384 + (u32)wr * 256);
      m = umax3(umax3(a.x, a.y, a.z), a.w, b);
      if (__all(m != SENT)) break;
      if (spins > SPIN_CAP) dead = true;
    }
    *(u32x4*)dA = a;
    *(u32*)dB = b;
  }
  return spins;
}
DEV u32 wait_prog(rsrc_t ws, u32 off, int n, u32 need, int l, bool& dead, int slp, u32 stride = 4) {
  u32 mn = 0;
#pragma nounroll
  for (u32 spins = 0;; ++spins) {
    asm volatile("" ::: "memory");
    const u32 x = ld4c(ws, (u32)(l < n ? l : 0) * stride, off);
    int d = -(int)x;
    d = row_max(d);
    mn = (u32)(-max(__builtin_amdgcn_readlane(d, 0), __builtin_amdgcn_readlane(d, 16)));
    if (mn >= need || dead) break;
    if (spins > SPIN_CAP) { dead = true; break; }
    if (slp == 2) __builtin_amdgcn_s_sleep(32); else if (slp) __builtin_amdgcn_s_sleep(6); else __builtin_amdgcn_s_sleep(1);
  }
  asm volatile("" ::: "memory");
  return mn;
}

DEV int wrow(int r) { return r * 288 + (r >> 3) * 128; }
DEV void load_qk(const Params& p, int h, int w, int l, unsigned char* lds, u32x4 (&wt)[2][20]) {
  const rsrc_t src = mk_rsrc((w >> 2) ? p.wk : p.wq, 640 * 8192);
  u32x4 st[40];
#pragma unroll
  for (int i = 0; i < 40; ++i)
    st[i] = ld16(src, (u32)((l >> 4) * 8192 + (l & 15) * 16), (u32)((32 * (w & 3) + (i >> 3) * 128 + (i & 7) * 4) * 8192 + h * 256));
  unsigned char* img = lds + (w >> 2) * 38912;
  unsigned char* wp = img + wrow(32 * (w & 3)) + (l >> 4) * 288 + (l & 15) * 16;
  const unsigned char* rp = img + wrow(8 * (l >> 4)) + ((l >> 2) & 3) * 288 + (w & 3) * 64 + (l & 3) * 8;
#pragma unroll
  for (int r = 0; r < 5; ++r) {
#pragma unroll
    for (int i = 0; i < 8; ++i) *(u32x4*)(wp + wrow(4 * i)) = st[r * 8 + i];
    __syncthreads();
#pragma unroll
    for (int kk = 0; kk < 4; ++kk)
#pragma unroll
      for (int i = 0; i < 2; ++i) wt[i][4 * r + kk] = tr_frag(rp + wrow(32 * kk) + i * 32, rp + wrow(32 * kk) + 4 * 288 + i * 32);
    __syncthreads();
  }
}
DEV void load_vu(const Params& p, int h, int w, int l, unsigned char* lds, u32x4 (&wv)[20], u32x4 (&wo)[5][4]) {
  const rsrc_t sv = mk_rsrc(p.wv, 640 * 8192), so = mk_rsrc(p.wo, 640 * 8192);
  u32x4 st[40];
#pragma unroll
  for (int i = 0; i < 20; ++i)
    st[i] = ld16(sv, (u32)((l >> 4) * 8192 + (l & 15) * 16), (u32)((40 * w + (i / 10) * 320 + (i % 10) * 4) * 8192 + h * 256));
#pragma unroll
  for (int i = 0; i < 20; ++i) st[20 + i] = ld16(so, (u32)l * 16, (u32)(h * 163840 + (w * 640 + (i / 10) * 5120 + (i % 10) * 64) * 16));
  unsigned char* wp = lds + wrow(40 * w) + (l >> 4) * 288 + (l & 15) * 16;
  const unsigned char* rp = lds + wrow(8 * (l >> 4)) + ((l >> 2) & 3) * 288 + w * 32 + (l & 3) * 8;
#pragma unroll
  for (int r = 0; r < 2; ++r) {
#pragma unroll
    for (int i = 0; i < 10; ++i) *(u32x4*)(wp + wrow(4 * i)) = st[r * 10 + i];
    __syncthreads();
#pragma unroll
    for (int kk = 0; kk < 10; ++kk) wv[10 * r + kk] = tr_frag(rp + wrow(32 * kk), rp + wrow(32 * kk) + 4 * 288);
    __syncthreads();
  }
  const unsigned char* ro = lds + (4 * (l >> 4) + ((l >> 2) & 3)) * 1312 + w * 160 + (l & 3) * 8;
#pragma unroll
  for (int r = 0; r < 2; ++r) {
#pragma unroll
    for (int i = 0; i < 10; ++i) {
      const int c = (w * 10 + i) * 64 + l;
      *(u32x4*)(lds + (c / 80) * 1312 + (c % 80) * 16) = st[20 + r * 10 + i];
    }
    __syncthreads();
#pragma unroll
    for (int kk = 0; kk < 2; ++kk)
#pragma unroll
      for (int ct = 0; ct < 5; ++ct) wo[ct][2 * r + kk] = tr_frag(ro + 32 * kk * 1312 + ct * 32, ro + (32 * kk + 16) * 1312 + ct * 32);
    __syncthreads();
  }
}

constexpr float CEXP = 0.08838834764831845f * 1.4426950408889634f;
constexpr float NEGS = -1.0e9f / 0.08838834764831845f;
constexpr int QS = 288;

DEV void read_memf(const unsigned char* memf, int l, u32x4 (&a)[10]) {
#pragma unroll
  for (int ks = 0; ks < 10; ++ks) a[ks] = *(const u32x4*)(memf + ks * 1024 + l * 16);
}
DEV void project2(const u32x4 (&a)[10], const u32x8 (&wt)[2][10], int sidx, unsigned char* dst) {
  f32x4 c0 = {0, 0, 0, 0}, c1 = {0, 0, 0, 0};
#pragma unroll
  for (int ks = 0; ks < 10; ++ks) {
    c0 = smfma(a[ks], wt[0][ks], c0, sidx);
    c1 = smfma(a[ks], wt[1][ks], c1, sidx);
  }
  *(__bf16*)(dst) = (__bf16)(c0[0] + c0[1]);
  *(__bf16*)(dst + QS) = (__bf16)(c0[2] + c0[3]);
  *(__bf16*)(dst + 8) = (__bf16)(c1[0] + c1[1]);
  *(__bf16*)(dst + 8 + QS) = (__bf16)(c1[2] + c1[3]);
}

DEV void score_role(const Params& p, rsrc_t ws, u32 cb, int h, unsigned char* lds) {
  const int tid = threadIdx.x, l = tid & 63, c16 = l & 15, g = l >> 4;
  const int w = __builtin_amdgcn_readfirstlane(tid >> 6);
  unsigned char* memf = lds;
  unsigned char* qs = lds + 10240;
  unsigned char* kms = qs + 8 * QS;
  u32* ctl = (u32*)(lds + 15360);
  unsigned char* kq = lds + 16384;
  u32x8 wt[2][10];
  {
    u32x4 wf[2][20];
    load_qk(p, h, w, l, lds, wf);
#pragma unroll
    for (int i = 0; i < 2; ++i)
#pragma unroll
      for (int ks = 0; ks < 10; ++ks) wt[i][ks] = cat(wf[i][2 * ks], wf[i][2 * ks + 1]);
  }
  GSTAMP(1);
  bool dead = false;
  if (tid < 2) ctl[2 + tid] = 0;
  u32 seen0 = 0, seen1 = 0;
  const u32 kq_src = ((w & 2) ? OFF_QX : OFF_KX) + (u32)(h * 8192 + (w & 1) * 4096);
  if (w < 2) { u32 r = probe(ws, cb, h, w, l); vote(p, w, r, l); if (l == 0) ctl[4 + w] = r; }
  else if (w >= 4) {
    seen0 = wait_prog(ws, OFF_PQK + (u32)h * 128, 1, 1, l, dead, 0);
    seen1 = wait_prog(ws, OFF_PQK + (u32)h * 128 + 4, 1, 1, l, dead, 0);
#pragma unroll
    for (int i = 0; i < 4; ++i) *(u32x4*)(kq + (w - 4) * 4096 + i * 1024 + l * 16) = ld16c(ws, l * 16 + i * 1024, kq_src);
  }
  __syncthreads();
  GSTAMP(2);
  u32 parmask = __builtin_amdgcn_readfirstlane(ctl[4] | (ctl[5] << 1));
  const int sidx = (l & 1) ? 0xEEEE : 0x4444;
  unsigned char* qk_w = qs + (w >> 2) * 8 * QS + 2 * g * QS + (32 * (w & 3) + 8 * (c16 >> 2) + (c16 & 3)) * 2;
  const unsigned char* km_r = kms + (2 * (c16 >> 2) + (c16 & 1)) * QS + g * 16;
  const int wr = (w + h) & 7;
  unsigned char* gA = memf + wr * 1024 + g * 256 + (l & 7) * 32 + ((l >> 3) & 1) * 16;
  unsigned char* gB = memf + 8192 + wr * 256 + ((l >> 2) & 7) * 32 + (l >> 5) * 16 + (l & 3) * 4;
  if (w == 0) {
    const unsigned char* q_r = qs + (c16 & 7) * QS + g * 16;
    const bool hb = c16 >= 8;
    const u32 xo = (u32)(((h >> 1) & 7) * 1024 + g * 256 + (h & 1) * 128 + (c16 & 7) * 16 + (c16 >> 3) * 8);
    const u32 mo = (u32)((c16 >> 3) * 8192 + h * 128 + (c16 & 7) * 16 + g * 4);
    for (int s = 0; s < NSEG; ++s) {
      const u32 sb = cb + (u32)s * STEP_BYTES;
      if (s == 1) parmask = vote_result(ws, l);
      const u32 par = ((parmask >> (s >> 5)) & 1) * 8192;
      const unsigned char* kxs = kq + (s & 1) * 16384;
      __syncthreads();
      { u32 sp = gather_mem(ws, sb + par, wr, gA, gB, l, dead); COUNT(5, sp); if (sp && l == 0) ctl[2 + (s & 1)] = (u32)s; }
      __syncthreads();
      { u32x4 a[10]; read_memf(memf, l, a); SCHED_FENCE(); project2(a, wt, sidx, qk_w); }
      u32x4 kx[8];
#pragma unroll
      for (int i = 0; i < 8; ++i) kx[i] = *(const u32x4*)(kxs + i * 1024 + l * 16);
      __syncthreads();
      if (s == NSEG - 1) break;
      u32x4 qf[4], km[4];
#pragma unroll
      for (int ks = 0; ks < 4; ++ks) { qf[ks] = *(const u32x4*)(q_r + ks * 64); km[ks] = *(const u32x4*)(km_r + ks * 64); }
      SCHED_FENCE();
      f32x4 s0 = {0, 0, 0, 0}, s1 = {0, 0, 0, 0}, s2 = {0, 0, 0, 0};
#pragma unroll
      for (int ks = 0; ks < 4; ++ks) {
        s1 = mfma(kx[ks], qf[ks], s1);
        s2 = mfma(kx[4 + ks], qf[ks], s2);
        s0 = mfma(km[ks], qf[ks], s0);
      }
      float v[4];
#pragma unroll
      for (int r = 0; r < 4; ++r) v[r] = hb ? s2[r] : s1[r];
      float m = fmx(fmx(fmx(v[0], v[1]), v[2]), fmx(fmx(v[3], s0[0]), s0[1]));
      m = red_max(m);
      const float mc = -m * CEXP;
#pragma unroll
      for (int r = 0; r < 4; ++r) v[r] = __builtin_amdgcn_exp2f(fmaf(v[r], CEXP, mc));
      const float e0 = __builtin_amdgcn_exp2f(fmaf(s0[0], CEXP, mc)), e1 = __builtin_amdgcn_exp2f(fmaf(s0[1], CEXP, mc));
      float sum = ((v[0] + v[1]) + (v[2] + v[3])) + (hb ? e1 : e0);
      sum = red_sum(sum);
      const float inv = __builtin_amdgcn_rcpf(sum);
      u32x2 px = {pk2s(v[0] * inv, v[1] * inv), pk2s(v[2] * inv, v[3] * inv)};
      const u32 pm = pk2s(e0 * inv, e1 * inv);
      const u32 xs = sb + (u32)(2 + (h >> 4)) * 16384;
      st8c(ws, xo, xs + par, px);
      st4c(ws, mo, sb + 16384 + 2048, pm);
      st8c(ws, xo, xs + (par ^ 8192), px);
      const u32 tnow = fine_now();
      const u32 rm = __builtin_amdgcn_readfirstlane(ctl[s & 1]), bad = __builtin_amdgcn_readfirstlane(ctl[2 + (s & 1)]);
      const u32 d1 = (s >= 2 && bad != (u32)s) ? tnow - rm : 0u;
      st4c(ws, l < 32 ? (u32)l * 4 : 0x80000000u, OFF_SIDE + (u32)s * SIDE_STEP + (u32)(80 + h) * 128, d1 < 1000u ? d1 : 0u);
      STAMPV(7, d1);
      STAMPV(4, tnow);
    }
  } else if (w < 3) {
    const int tx = 16 * (w - 1) + c16;
    for (int s = 0; s < NSEG; ++s) {
      const unsigned char* kxs = kq + (s & 1) * 16384;
      const u32 sb = cb + (u32)s * STEP_BYTES;
      if (s == 1) parmask = vote_result(ws, l);
      const u32 par = ((parmask >> (s >> 5)) & 1) * 8192;
      __syncthreads();
      { u32 sp = gather_mem(ws, sb + par, wr, gA, gB, l, dead); COUNT(5, sp); if (sp && l == 0) ctl[2 + (s & 1)] = (u32)s; }
      __syncthreads();
      if (w == 1) STAMP(1);
      { u32x4 a[10]; read_memf(memf, l, a); SCHED_FENCE(); project2(a, wt, sidx, qk_w); }
      __syncthreads();
      if (w == 1) STAMP(2);
      if (w == 1) st4c(ws, l == 0 ? 0u : 0x80000000u, OFF_PPX + (u32)h * 128, (u32)s);
      f32x4 s0 = {0, 0, 0, 0}, s1 = {0, 0, 0, 0}, s2 = {0, 0, 0, 0};
#pragma unroll
      for (int ks = 0; ks < 4; ++ks) {
        u32x4 qf = *(const u32x4*)(kxs + 8192 + ((w - 1) * 4 + ks) * 1024 + l * 16);
        s0 = mfma(*(const u32x4*)(km_r + ks * 64), qf, s0);
        s1 = mfma(*(const u32x4*)(kxs + ks * 1024 + l * 16), qf, s1);
        s2 = mfma(*(const u32x4*)(kxs + (4 + ks) * 1024 + l * 16), qf, s2);
      }
      float m = fmx(s0[0], s0[1]);
#pragma unroll
      for (int r = 0; r < 4; ++r) {
        s1[r] = (4 * g + r <= tx) ? s1[r] : NEGS;
        s2[r] = (16 + 4 * g + r <= tx) ? s2[r] : NEGS;
        m = fmx(m, fmx(s1[r], s2[r]));
      }
      m = red_max_g(m);
      const float mc = -m * CEXP;
      const float e0 = __builtin_amdgcn_exp2f(fmaf(s0[0], CEXP, mc)), e1 = __builtin_amdgcn_exp2f(fmaf(s0[1], CEXP, mc));
      float sum = e0 + e1;
#pragma unroll
      for (int r = 0; r < 4; ++r) {
        s1[r] = __builtin_amdgcn_exp2f(fmaf(s1[r], CEXP, mc));
        s2[r] = __builtin_amdgcn_exp2f(fmaf(s2[r], CEXP, mc));
        sum += s1[r] + s2[r];
      }
      sum = red_sum_g(sum);
      const float inv = __builtin_amdgcn_rcpf(sum);
      u32x4 px = {pk2(s1[0] * inv, s1[1] * inv), pk2(s1[2] * inv, s1[3] * inv), pk2(s2[0] * inv, s2[1] * inv), pk2(s2[2] * inv, s2[3] * inv)};
      st16c(ws, l * 16, OFF_PXX + (u32)(((s * 32 + h) * 2 + (w - 1)) * 1024), px);
      st4c(ws, c16 * 16 + g * 4, OFF_PXM + (u32)((((s * 2 + (w - 1)) * 32 + h) * 16) * 16), pk2(e0 * inv, e1 * inv));
      VM_DRAIN();
    }
  } else if (w == 3) {
    u32 Rm = 0, Rp;
    for (int s = 0; s < NSEG; ++s) {
      const u32 sb = cb + (u32)s * STEP_BYTES;
      if (s == 1) parmask = vote_result(ws, l);
      const u32 par = ((parmask >> (s >> 5)) & 1) * 8192;
      if (s >= 2) {
        if (l == 0) { ctl[s & 1] = Rm; if ((int)(Rm - rtime()) < 28) ctl[2 + (s & 1)] = (u32)s; }
        wait_until(Rm);
      }
      __syncthreads();
      { u32 sp = gather_mem(ws, sb + par, wr, gA, gB, l, dead); COUNT(5, sp); if (sp && l == 0) ctl[2 + (s & 1)] = (u32)s; }
      __syncthreads();
      u32x4 a[10];
      read_memf(memf, l, a);
      Side sd = load_side(ws, s, l);
      SCHED_FENCE();
      project2(a, wt, sidx, qk_w);
      __syncthreads();
      STAMPV(0, Rm);
      if (s >= 1 && s < NSEG - 1) schedule(ws, s, l, sd, Rm, Rp, dead);
    }
  } else {
    for (int s = 0; s < NSEG; ++s) {
      const u32 sb = cb + (u32)s * STEP_BYTES;
      if (s == 1) parmask = vote_result(ws, l);
      const u32 par = ((parmask >> (s >> 5)) & 1) * 8192;
      const int sn = s + 1 < NSEG ? s + 1 : s;
      __syncthreads();
      { u32 sp = gather_mem(ws, sb + par, wr, gA, gB, l, dead); COUNT(5, sp); if (sp && l == 0) ctl[2 + (s & 1)] = (u32)s; }
      __syncthreads();
      u32x4 a[10];
      read_memf(memf, l, a);
      SCHED_FENCE();
      u32x4 pk[4];
      f32x4 c0 = {0, 0, 0, 0}, c1 = {0, 0, 0, 0};
#pragma unroll
      for (int ks = 0; ks < 10; ++ks) {
        c0 = smfma(a[ks], wt[0][ks], c0, sidx);
        c1 = smfma(a[ks], wt[1][ks], c1, sidx);
        if (ks == KQPOS) {
          SCHED_FENCE();
#pragma unroll
          for (int i = 0; i < 4; ++i) pk[i] = ld16c(ws, l * 16 + i * 1024, kq_src + (u32)sn * (32 * 8192));
          SCHED_FENCE();
        }
      }
      *(__bf16*)(qk_w) = (__bf16)(c0[0] + c0[1]);
      *(__bf16*)(qk_w + QS) = (__bf16)(c0[2] + c0[3]);
      *(__bf16*)(qk_w + 8) = (__bf16)(c1[0] + c1[1]);
      *(__bf16*)(qk_w + 8 + QS) = (__bf16)(c1[2] + c1[3]);
      __syncthreads();
      unsigned char* kn = kq + ((s + 1) & 1) * 16384 + (w - 4) * 4096 + l * 16;
#pragma unroll
      for (int i = 0; i < 4; ++i) *(u32x4*)(kn + i * 1024) = pk[i];
      const int s2 = s + 2;
      if (s2 < NSEG) {
        if (seg_prod(s2)) { if (seen1 < seg_need(s2)) { __builtin_amdgcn_s_sleep(12); seen1 = wait_prog(ws, OFF_PQK + (u32)h * 128 + 4, 1, seg_need(s2), l, dead, 0); } }
        else { if (seen0 < seg_need(s2)) { __builtin_amdgcn_s_sleep(12); seen0 = wait_prog(ws, OFF_PQK + (u32)h * 128, 1, seg_need(s2), l, dead, 0); } }
      }
    }
  }
  __syncthreads();
  if (tid == 0) st4c(ws, 0u, OFF_PPX + (u32)h * 128, (u32)NSEG);
}

DEV u32x4 finish_mem(f32x4 acc) {
  auto x = __builtin_amdgcn_permlane32_swap(__float_as_uint(acc[0]), __float_as_uint(acc[2]), false, false);
  auto y = __builtin_amdgcn_permlane32_swap(__float_as_uint(acc[1]), __float_as_uint(acc[3]), false, false);
  float t0, t1;
  asm("s_nop 1\n\tv_add_f32_dpp %0, %2, %3 row_ror:8 row_mask:0xf bank_mask:0xf\n\tv_add_f32_dpp %1, %4, %5 row_ror:8 row_mask:0xf bank_mask:0xf"
      : "=&v"(t0), "=&v"(t1) : "v"(x[1]), "v"(x[0]), "v"(y[1]), "v"(y[0]));
  const u32 d = pk2s(t0, t1);
  auto s16 = __builtin_amdgcn_permlane16_swap(d, d, false, false);
  auto sa = __builtin_amdgcn_permlane32_swap(s16[0], s16[0], false, false);
  auto sb = __builtin_amdgcn_permlane32_swap(s16[1], s16[1], false, false);
  return (u32x4){sa[0], sa[1], sb[0], sb[1]};
}

DEV u32 second4(u32 a, u32 b, u32 c, u32 d) {
  const u32 h0 = max(a, b), l0 = min(a, b), h1 = max(c, d), l1 = min(c, d);
  return max(min(h0, h1), h0 > h1 ? l0 : l1);
}
DEV void value_role(const Params& p, rsrc_t ws, u32 cb, int v, unsigned char* lds) {
  const int tid = threadIdx.x, l = tid & 63, c16 = l & 15, g = l >> 4;
  const int w = __builtin_amdgcn_readfirstlane(tid >> 6);
  unsigned char* memf = lds;
  unsigned char* um = lds + 10240;
  u32* ctl = (u32*)(lds + 15360);
  unsigned char* w0s = lds + 16384;
  unsigned char* uxs = lds + 57344;
  const int sidx = (l & 1) ? 0xEEEE : 0x4444;
  const u32 um_w = (u32)((4 * w + (c16 >> 3)) * 128 + (c16 & 7) * 16 + g * 4);
  const int wr = (w + v) & 7;
  unsigned char* gA = memf + wr * 1024 + g * 256 + (l & 7) * 32 + ((l >> 3) & 1) * 16;
  unsigned char* gB = memf + 8192 + wr * 256 + ((l >> 2) & 7) * 32 + (l >> 5) * 16 + (l & 3) * 4;
  bool dead = false;
  if (w == 0) {
#pragma unroll
    for (int i = 0; i < 40; ++i) *(u32x4*)(w0s + i * 1024 + l * 16) = ld16(ws, l * 16, OFF_BF + (u32)((v * 16 * 20 + i) * 1024));
    __syncthreads();
    GSTAMP(1);
    __syncthreads();
    GSTAMP(2);
    u32 parmask = __builtin_amdgcn_readfirstlane(ctl[4] | (ctl[5] << 1));
    const u32 offX = (u32)((c16 >> 3) * 512 + g * 128 + (c16 & 7) * 16);
    const u32 L = (u32)(v * 128 + (c16 & 7) * 16);
    const u32 pub = (c16 < 8 && !(g & 1)) ? (L >> 13) * 16384 + (u32)(g >> 1) * 8192 + (L & 8191) : 0x80000000u;
    u32 Rg = 0, d3 = 0, Rm = 0, dcur = v == 0 ? ((D1 + D2) << 10) | ((D3 + D4) << 20) : 0u;
    u32 a0 = D1, a1 = D1, a2 = D1, a3 = D1, b0 = D3, b1 = D3, b2 = D3, b3 = D3;
    const bool sidel = (g == 1 && c16 < 8);
    for (int s = 0; s < NSEG; ++s) {
      const u32 sb = cb + (u32)s * STEP_BYTES;
      if (s == 1) parmask = vote_result(ws, l);
      const u32 par = ((parmask >> (s >> 5)) & 1) * 8192;
      if (s >= 1) wait_until(Rg);
      __syncthreads();
      const u32x4 ga = ld16c(ws, (u32)l * 16, sb + par + (u32)wr * 1024);
      const u32 gb = ld4c(ws, (u32)l * 4, sb + par + 16384 + (u32)wr * 256);
      SCHED_FENCE();
      __builtin_amdgcn_s_sleep(1);
      Side sd = load_side(ws, s, l);
      u32 d1s = 0;
      if (v == 0 && s >= 1) d1s = ld4c(ws, (u32)(80 + (l & 31)) * 128, OFF_SIDE + (u32)(s - 1) * SIDE_STEP);
      u32x4 uf[20];
#pragma unroll
      for (int i = 0; i < 16; ++i) uf[i] = *(const u32x4*)(uxs + (((i >> 2) + PROT) & 3) * 4096 + (i & 3) * 1024 + offX);
      SCHED_FENCE();
      *(u32x4*)gA = ga;
      *(u32*)gB = gb;
      if (__builtin_expect(__any(umax3(umax3(ga.x, ga.y, ga.z), ga.w, gb) == SENT), 0)) { u32 sp = gather_mem(ws, sb + par, wr, gA, gB, l, dead); COUNT(5, sp + 1); }
      __syncthreads();
      STAMP(0);
      if (s == NSEG - 1) {
        __syncthreads();
        __syncthreads();
        break;
      }
      u32 so[5];
#pragma unroll
      for (int i = 0; i < 4; ++i) { const u32 qd = (u32)(i + PROT) & 3; so[i] = sb + par + (2 + (qd >> 1)) * 16384 + (qd & 1) * 4096; }
      so[4] = sb + par + 16384 + 2048;
      asm volatile("" : "+s"(so[0]), "+s"(so[1]), "+s"(so[2]), "+s"(so[3]), "+s"(so[4]));
      const u32 pubo = sidel ? OFF_SIDE + (u32)(s + 1) * SIDE_STEP + (u32)(v * 128 + c16 * 16) : pub + sb + STEP_BYTES;
      u32 Rp = 0;
      if (s >= 1) schedule(ws, s, l, sd, Rm, Rp, dead);
      const u32 sy = d3 | dcur;
      const int lead = (int)(Rp - rtime());
      bool ontime = s >= 1 && lead > 0, pfail = false;
      STAMPV(1, Rp);
      STAMP(2);
      SCHED_FENCE();
      if (s >= 1) wait_until(Rp);
      SCHED_FENCE();
      u32x4 pf[20];
#pragma unroll
      for (int i = 0; i < 20; ++i) { pf[i] = ld16c(ws, (u32)l * 16 + (i & 3) * 1024, so[i >> 2]); SCHED_FENCE(); }
      f32x4 acc = {0, 0, 0, 0};
#pragma unroll
      for (int i = 0; i < 20; ++i) {
        if (i == UMPOS) {
          __syncthreads();
#pragma unroll
          for (int q = 0; q < 4; ++q) uf[16 + q] = *(const u32x4*)(um + q * 1024 + l * 16);
          SCHED_FENCE();
        }
        acc = mfma(uf[i], pf[i], acc);
        SCHED_FENCE();
      }
      const u32 tl = rtime();
      u32x4 o = finish_mem(acc);
      if (__builtin_expect(__any(acc[0] != acc[0]), 0)) {
        u32 spins = 0;
#pragma nounroll
        while (!dead) {
          u32 m = 0;
#pragma unroll
          for (int i = 0; i < 20; ++i) m = umax3(umax3(m, pf[i].x, pf[i].y), pf[i].z, pf[i].w);
          if (__all(m != SENT)) break;
          ontime = false; pfail = true;
          if (++spins > SPIN_CAP) dead = true;
          asm volatile("" ::: "memory");
#pragma unroll
          for (int i = 0; i < 20; ++i) pf[i] = ld16c(ws, (u32)l * 16 + (i & 3) * 1024, so[i >> 2]);
        }
        STAMPV(4, spins);
        acc = (f32x4){0, 0, 0, 0};
#pragma unroll
        for (int i = 0; i < 20; ++i) acc = mfma(uf[i], pf[i], acc);
        o = finish_mem(acc);
      }
      u32 sx = 0;
      if ((int)(tl + DLS - Rm) > 0) sx = (tl + DL + 4) | 1u;
      if (__builtin_expect(!ontime, 0)) sx = (rtime() + DL) | 1u;
      if (sx == SENT) sx = 1u;
      o.x = sidel ? sx : o.x;
      o.y = sidel ? sy : o.y;
      st16c(ws, pubo, 0u, o);
      const u32 tp = fine_now();
      __syncthreads();
      d3 = (ontime && lead >= 28) ? tp - Rp : 0u;
      d3 = d3 < 1000u ? d3 : 0u;
      Rg = sx == 0 ? Rm + DV : tp + D4 + DV;
      STAMPV(7, d3);
      if (v == 0) {
        const u32 ma = (u32)wave_max(d1s == SENT ? 0 : (int)d1s), mb = s >= 1 ? (u32)wave_max((int)max(sd.a.y & 1023u, sd.b.y & 1023u)) : 0u;
        if (ma) { a3 = a2; a2 = a1; a1 = a0; a0 = ma; }
        if (pfail && s >= 2) { a0 = a1 = min(second4(a0, a1, a2, a3) + 8, 300u); }
        if (mb) { b3 = b2; b2 = b1; b1 = b0; b0 = mb; }
        dcur = (min(second4(a0, a1, a2, a3) + D2, 1000u) << 10) | (min(second4(b0, b1, b2, b3) + D4, 1000u) << 20);
      }
      STAMP(3);
      STAMPV(6, ((sy >> 10) & 1023u) * 1000 + ((sy >> 20) & 1023u));
    }
  } else {
    u32x8 wb[2][10];
#pragma unroll
    for (int t = 0; t < 2; ++t)
#pragma unroll
      for (int ks = 0; ks < 10; ++ks) {
        u32 o = OFF_BF + (u32)(((v * 16 + 2 * w + t) * 20 + 2 * ks) * 1024);
        wb[t][ks] = cat(ld16(ws, l * 16, o), ld16(ws, l * 16 + 1024, o));
      }
    __builtin_amdgcn_s_waitcnt(0x0F70);
    __syncthreads();
    if (w < 3) { u32 r = probe(ws, cb, 32 + v, w - 1, l); vote(p, w - 1, r, l); if (l == 0) ctl[3 + w] = r; }
    const bool pfw = w == 3 || w >= 5;
    const u32 uxo = (u32)((w == 3 ? 0 : w - 4) * 4096 + l * 16);
    u32 seen0 = 0, seen1 = 0;
    if (pfw) {
      seen0 = wait_prog(ws, OFF_PVU, 32, 1, l, dead, 0);
      seen1 = wait_prog(ws, OFF_PVU + 128, 32, 1, l, dead, 0);
#pragma unroll
      for (int i = 0; i < 4; ++i) *(u32x4*)(uxs + uxo + i * 1024) = ld16c(ws, uxo + i * 1024, OFF_UX + (u32)(v * 16384));
    }
    __syncthreads();
    u32 parmask = __builtin_amdgcn_readfirstlane(ctl[4] | (ctl[5] << 1));
    if (w == 4) {
      for (int s = 0; s < NSEG; ++s) {
        const u32 sb = cb + (u32)s * STEP_BYTES;
        if (s == 1) parmask = vote_result(ws, l);
      const u32 par = ((parmask >> (s >> 5)) & 1) * 8192;
        u32x8 sb0[2], sb1[2];
        sb0[0] = cat(*(const u32x4*)(w0s + l * 16), *(const u32x4*)(w0s + 1024 + l * 16));
        sb1[0] = cat(*(const u32x4*)(w0s + 20480 + l * 16), *(const u32x4*)(w0s + 21504 + l * 16));
        __syncthreads();
        { u32 sp = gather_mem(ws, sb + par, wr, gA, gB, l, dead); COUNT(5, sp); }
        __syncthreads();
        u32x4 a[3];
        a[0] = *(const u32x4*)(memf + l * 16);
        a[1] = *(const u32x4*)(memf + 1024 + l * 16);
        f32x4 a0 = {0, 0, 0, 0}, a1 = {0, 0, 0, 0}, a2 = {0, 0, 0, 0}, a3 = {0, 0, 0, 0};
#pragma unroll
        for (int ks = 0; ks < 10; ++ks) {
          if (ks + 2 < 10) a[(ks + 2) % 3] = *(const u32x4*)(memf + (ks + 2) * 1024 + l * 16);
          if (ks + 1 < 10) {
            const int kn = ks + 1;
            sb0[kn & 1] = cat(*(const u32x4*)(w0s + (2 * kn) * 1024 + l * 16), *(const u32x4*)(w0s + (2 * kn + 1) * 1024 + l * 16));
            sb1[kn & 1] = cat(*(const u32x4*)(w0s + (20 + 2 * kn) * 1024 + l * 16), *(const u32x4*)(w0s + (21 + 2 * kn) * 1024 + l * 16));
          }
          SCHED_FENCE();
          a0 = smfma(a[ks % 3], wb[0][ks], a0, sidx);
          a1 = smfma(a[ks % 3], wb[1][ks], a1, sidx);
          a2 = smfma(a[ks % 3], sb0[ks & 1], a2, sidx);
          a3 = smfma(a[ks % 3], sb1[ks & 1], a3, sidx);
          SCHED_FENCE();
        }
        *(u32*)(um + um_w) = pk2(a0[0] + a0[1], a0[2] + a0[3]);
        *(u32*)(um + um_w + 256) = pk2(a1[0] + a1[1], a1[2] + a1[3]);
        *(u32*)(um + um_w - 2048) = pk2(a2[0] + a2[1], a2[2] + a2[3]);
        *(u32*)(um + um_w - 2048 + 256) = pk2(a3[0] + a3[1], a3[2] + a3[3]);
        __syncthreads();
        __syncthreads();
      }
    } else {
      for (int s = 0; s < NSEG; ++s) {
        const u32 sb = cb + (u32)s * STEP_BYTES;
        if (s == 1) parmask = vote_result(ws, l);
      const u32 par = ((parmask >> (s >> 5)) & 1) * 8192;
        __syncthreads();
        { u32 sp = gather_mem(ws, sb + par, wr, gA, gB, l, dead); COUNT(5, sp); }
        __syncthreads();
        u32x4 a[10];
        read_memf(memf, l, a);
        u32x4 pu[4];
        if (pfw && s + 1 < NSEG) {
#pragma unroll
          for (int i = 0; i < 4; ++i) pu[i] = ld16c(ws, uxo + i * 1024, OFF_UX + (u32)(((s + 1) * 80 + v) * 16384));
        }
        SCHED_FENCE();
        f32x4 a0 = {0, 0, 0, 0}, a1 = {0, 0, 0, 0};
#pragma unroll
        for (int ks = 0; ks < 10; ++ks) {
          a0 = smfma(a[ks], wb[0][ks], a0, sidx);
          a1 = smfma(a[ks], wb[1][ks], a1, sidx);
        }
        *(u32*)(um + um_w) = pk2(a0[0] + a0[1], a0[2] + a0[3]);
        *(u32*)(um + um_w + 256) = pk2(a1[0] + a1[1], a1[2] + a1[3]);
        if (w == 1) st4c(ws, l == 0 ? 0u : 0x80000000u, OFF_PUM + (u32)v * 128, (u32)s);
        __syncthreads();
        __syncthreads();
        if (pfw && s + 1 < NSEG) {
#pragma unroll
          for (int i = 0; i < 4; ++i) *(u32x4*)(uxs + uxo + i * 1024) = pu[i];
          const int sn = s + 2;
          if (sn < NSEG) {
            if (seg_prod(sn)) { if (seen1 < seg_need(sn)) seen1 = wait_prog(ws, OFF_PVU + 128, 32, seg_need(sn), l, dead, 0); }
            else { if (seen0 < seg_need(sn)) seen0 = wait_prog(ws, OFF_PVU, 32, seg_need(sn), l, dead, 0); }
          }
        }
        if (w == 1 || w == 2) {
#pragma unroll
          for (int i = 0; i < 2; ++i) {
            u32 o = (u32)(((w - 1) * 2 + i) * 1024 + l * 16);
            st16c(ws, o, OFF_UM + (u32)((s * 80 + v) * 4096), *(const u32x4*)(um + o));
          }
          VM_DRAIN();
        }
      }
    }
  }
  __syncthreads();
  if (tid == 0) st4c(ws, 0u, OFF_PUM + (u32)v * 128, (u32)NSEG);
}

DEV void glds16(const void* gsrc, u32 lds_dst) {
  u32 keep;
  asm volatile("s_mov_b32 %0, m0\n\ts_mov_b32 m0, %2\n\ts_nop 0\n\tglobal_load_lds_dwordx4 %1, off\n\ts_mov_b32 m0, %0"
               : "=&s"(keep) : "v"(gsrc), "s"(lds_dst) : "memory");
}
template <int NST, bool ROWS, class Off, class Body, class Pub>
DEV void stream_tiles(const unsigned char* src, Off off, int n, int nbody, unsigned char* lds, Body body, Pub pub) {
  const int l = threadIdx.x & 63, w = __builtin_amdgcn_readfirstlane(threadIdx.x >> 6);
  const u32 lbase = (u32)(uintptr_t)lds + (u32)w * 5120;
  constexpr int QS_ = ROWS ? 64 : 1024;
  const unsigned char* g0 = ROWS ? src + (16 * (w >> 2) + (l & 15)) * 1280 + (w & 3) * 320 + (l >> 4) * 16 : src + w * 5120 + l * 16;
#pragma unroll
  for (int q = 0; q < 5; ++q) glds16(g0 + off(0) + q * QS_, lbase + q * 1024);
#pragma nounroll
  for (int i = 0; i < nbody; ++i) {
    if (i <= 3) VM_DRAIN(); else asm volatile("s_waitcnt vmcnt(%0)" :: "n"(NST) : "memory");
    RAW_BARRIER();
    if (i >= 1) pub(i <= 3 ? i : i - 1);
    const unsigned char* g = g0 + off(i + 1 < n ? i + 1 : n - 1);
    const u32 lb = lbase + (u32)((i + 1) & 1) * 40960;
    body(i, lds + (i & 1) * 40960, [&](int q) { glds16(g + q * QS_, lb + q * 1024); });
  }
  VM_DRAIN();
  RAW_BARRIER();
  pub(nbody);
}
template <int W, class Dma, class Use>
DEV void sweep40(const unsigned char* t, Dma dma, Use use) {
  u32x4 xb[W];
#pragma unroll
  for (int i = 0; i < W; ++i) xb[i] = *(const u32x4*)(t + ((i & 1) * 20 + (i >> 1)) * 1024);
  SCHED_FENCE();
#pragma unroll
  for (int i = 0; i < 40; ++i) {
    use(i, xb[i % W]);
    if (i + W < 40) xb[i % W] = *(const u32x4*)(t + (((i + W) & 1) * 20 + ((i + W) >> 1)) * 1024);
    if (i % 2 == 1 && i < 10) dma(i >> 1);
    SCHED_FENCE();
  }
}

DEV void qk_role(const Params& p, rsrc_t ws, int h, int par, unsigned char* lds) {
  const int tid = threadIdx.x, l = tid & 63;
  const int w = __builtin_amdgcn_readfirstlane(tid >> 6);
  const int mat = w >> 2;
  u32x4 wt[2][20];
  load_qk(p, h, w, l, lds, wt);
  GSTAMP(1);
  const u32 dst = (mat ? OFF_KX : OFF_QX) + (u32)((h * 8 + (w & 3)) * 1024);
  const int n = par ? S0 / 2 : NSEG - S0 / 2;
  stream_tiles<2, false>(p.ws + OFF_XF, [&](int i) { return tile_seg(par, i) * 40960; }, n, n, lds,
    [&](int i, const unsigned char* t, auto dma) {
      f32x4 acc[2][2] = {};
      sweep40<8>(t + l * 16, dma, [&](int f, u32x4 xb) {
        acc[0][f & 1] = mfma(wt[0][f >> 1], xb, acc[0][f & 1]);
        acc[1][f & 1] = mfma(wt[1][f >> 1], xb, acc[1][f & 1]);
      });
#pragma unroll
      for (int tt = 0; tt < 2; ++tt) {
        u32x4 o = {pk2(acc[0][tt][0], acc[0][tt][1]), pk2(acc[0][tt][2], acc[0][tt][3]), pk2(acc[1][tt][0], acc[1][tt][1]), pk2(acc[1][tt][2], acc[1][tt][3])};
        st16c(ws, l * 16 + tt * 4096, dst + (u32)tile_seg(par, i) * (32 * 8192), o);
      }
    },
    [&](int k) { if (w == 0) st4c(ws, l == 0 ? 0u : 0x80000000u, OFF_PQK + (u32)(h * 128 + par * 4), (u32)k); });
}

DEV void vu_role(const Params& p, rsrc_t ws, int h, int par, unsigned char* lds) {
  const int tid = threadIdx.x, l = tid & 63, c16 = l & 15, g = l >> 4;
  const int w = __builtin_amdgcn_readfirstlane(tid >> 6);
  u32x4 wv[20], wo[5][4];
  load_vu(p, h, w, l, lds, wv, wo);
  GSTAMP(1);
  unsigned char* vs = lds + 81920;
  const u32 uoff = (u32)(((c16 >> 1) & 1) * 16384 + h * 512 + g * 128 + (2 * (c16 >> 2) + (c16 & 1)) * 16);
  const int n = par ? S0 / 2 : NSEG - S0 / 2;
  stream_tiles<5, false>(p.ws + OFF_XF, [&](int i) { return tile_seg(par, i) * 40960; }, n, n + 1, lds,
    [&](int i, const unsigned char* t, auto dma) {
      u32x4 vf[2][4];
#pragma unroll
      for (int tt = 0; tt < 2; ++tt)
#pragma unroll
        for (int ks = 0; ks < 4; ++ks) vf[tt][ks] = *(const u32x4*)(vs + ((i + 1) & 1) * 8192 + ((tt * 4 + ks) * 64 + l) * 16);
      const u32 uo = i ? uoff + OFF_UX + (u32)((tile_seg(par, i - 1) * 80 + 10 * w) * 16384) : 0x80000000u;
      f32x4 av[2] = {}, cu[2] = {};
      u32x4 ou[2];
      sweep40<6>(t + l * 16, dma, [&](int f, u32x4 xb) {
        av[f & 1] = mfma(wv[f >> 1], xb, av[f & 1]);
        cu[f & 1] = mfma(vf[f & 1][(f >> 1) & 3], wo[f >> 3][(f >> 1) & 3], cu[f & 1]);
        if ((f & 7) == 7) {
          const int ct = f >> 3;
          u32x4 o = {pk2(cu[0][0], cu[0][1]), pk2(cu[0][2], cu[0][3]), pk2(cu[1][0], cu[1][1]), pk2(cu[1][2], cu[1][3])};
          cu[0] = (f32x4){0, 0, 0, 0}; cu[1] = (f32x4){0, 0, 0, 0};
          if (ct == 0) ou[0] = o;
          else { if (ct == 1) st16c(ws, uo, 0u, ou[0]); st16c(ws, uo, (u32)ct * 32768, o); }
        }
      });
#pragma unroll
      for (int tt = 0; tt < 2; ++tt) {
        u32x2 o = {pk2(av[tt][0], av[tt][1]), pk2(av[tt][2], av[tt][3])};
        *(u32x2*)(vs + (i & 1) * 8192 + ((tt * 4 + (w >> 1)) * 64 + l) * 16 + (w & 1) * 8) = o;
      }
    },
    [&](int k) { if (w == 0 && k >= 2) st4c(ws, l == 0 ? 0u : 0x80000000u, OFF_PVU + (u32)(par * 128 + h * 4), (u32)(k - 1)); });
}

DEV f32x2 cmul(f32x2 a, f32x2 b) { return (f32x2){a.x * b.x - a.y * b.y, a.x * b.y + a.y * b.x}; }
DEV f32x2 cmulc(f32x2 a, f32x2 b) { return (f32x2){a.x * b.x + a.y * b.y, a.y * b.x - a.x * b.y}; }
template <bool INV> DEV f32x2 rot90(f32x2 a) { return INV ? (f32x2){-a.y, a.x} : (f32x2){a.y, -a.x}; }
template <bool INV> DEV void fft4(f32x2& a, f32x2& b, f32x2& c, f32x2& d) {
  f32x2 s0 = a + c, d0 = a - c, s1 = b + d, d1 = rot90<INV>(b - d);
  a = s0 + s1; b = d0 + d1; c = s0 - s1; d = d0 - d1;
}
template <bool INV> DEV f32x2 twc(f32x2 a, float c, float s) {
  return INV ? (f32x2){a.x * c - a.y * s, a.y * c + a.x * s} : (f32x2){a.x * c + a.y * s, a.y * c - a.x * s};
}
template <bool INV> DEV void fft16(f32x2 (&v)[16]) {
  constexpr float C1 = 0.92387953251128674f, S1 = 0.38268343236508977f, R = 0.70710678118654752f;
#pragma unroll
  for (int i = 0; i < 4; ++i) fft4<INV>(v[i], v[i + 4], v[i + 8], v[i + 12]);
  v[5] = twc<INV>(v[5], C1, S1);  v[9] = twc<INV>(v[9], R, R);     v[13] = twc<INV>(v[13], S1, C1);
  v[6] = twc<INV>(v[6], R, R);    v[10] = rot90<INV>(v[10]);       v[14] = twc<INV>(v[14], -R, R);
  v[7] = twc<INV>(v[7], S1, C1);  v[11] = twc<INV>(v[11], -R, R);  v[15] = twc<INV>(v[15], -C1, -S1);
#pragma unroll
  for (int k1 = 0; k1 < 4; ++k1) fft4<INV>(v[4 * k1], v[4 * k1 + 1], v[4 * k1 + 2], v[4 * k1 + 3]);
  f32x2 t;
#define SWP(a, b) t = v[a]; v[a] = v[b]; v[b] = t;
  SWP(1, 4) SWP(2, 8) SWP(3, 12) SWP(6, 9) SWP(7, 13) SWP(11, 14)
#undef SWP
}
template <bool INV> DEV void fft8(f32x2 (&v)[8]) {
  constexpr float R = 0.70710678118654752f;
  fft4<INV>(v[0], v[2], v[4], v[6]);
  fft4<INV>(v[1], v[3], v[5], v[7]);
  f32x2 o1 = twc<INV>(v[3], R, R), o2 = rot90<INV>(v[5]), o3 = twc<INV>(v[7], -R, R), o0 = v[1];
  f32x2 e0 = v[0], e1 = v[2], e2 = v[4], e3 = v[6];
  v[0] = e0 + o0; v[4] = e0 - o0; v[1] = e1 + o1; v[5] = e1 - o1; v[2] = e2 + o2; v[6] = e2 - o2; v[3] = e3 + o3; v[7] = e3 - o3;
}

DEV void fft_cols(const Params& p, rsrc_t ws, int q, unsigned char* lds) {
  const int tid = threadIdx.x, c = tid >> 7, t = tid & 127;
  f32x2* tw = (f32x2*)lds;
  f32x2* buf = (f32x2*)(lds + 16384) + c * (16 * 136);
  f32x2* fin = (f32x2*)(lds + 16384);
  __syncthreads();
  for (int m = tid; m < 2048; m += 512) { float sn, cs; sincospif((float)m * (1.0f / 1024.0f), &sn, &cs); tw[m] = (f32x2){cs, -sn}; }
  f32x2 keep[16];
  const int npass = (q == 0) ? 2 : 1;
  for (int pass = npass - 1; pass >= 0; --pass) {
    f32x2 v[16];
#pragma unroll
    for (int a = 0; a < 16; ++a)
      v[a] = __builtin_bit_cast(f32x2, ld8c(ws, (u32)(t * 16 + (c & 1) * 8 + (c >> 1) * 32768), OFF_FX + (u32)(2 * q * 32768 + a * 2048)));
    if (q == 0 && c == 0) {
#pragma unroll
      for (int a = 0; a < 16; ++a) v[a] = (f32x2){pass ? v[a].y : v[a].x, 0.f};
    }
    u32 wreg[16];
#pragma unroll
    for (int i = 0; i < 2; ++i)
#pragma unroll
      for (int kc = 0; kc < 8; ++kc) {
        const int pp = t + 128 * i, k = (pp >> 4) + 16 * (pp & 15) + 256 * kc;
        wreg[i * 8 + kc] = ld4c(ws, (u32)(k * 16 + c * 4), OFF_WT + (u32)(pass ? 80 : q) * 32768);
      }
    __syncthreads();
    fft16<false>(v);
#pragma unroll
    for (int ka = 0; ka < 16; ++ka) buf[ka * 136 + t] = ka ? cmul(v[ka], tw[t * ka]) : v[ka];
    __syncthreads();
    const int ka2 = t >> 3, c2 = t & 7;
#pragma unroll
    for (int b = 0; b < 16; ++b) v[b] = buf[ka2 * 136 + 8 * b + c2];
    fft16<false>(v);
#pragma unroll
    for (int kb = 0; kb < 16; ++kb) buf[ka2 * 136 + 8 * kb + c2] = kb ? cmul(v[kb], tw[16 * c2 * kb]) : v[kb];
    __syncthreads();
#pragma unroll
    for (int i = 0; i < 2; ++i) {
      const int pp = t + 128 * i;
      f32x2* e = buf + (pp >> 4) * 136 + 8 * (pp & 15);
      f32x2 u[8];
#pragma unroll
      for (int j = 0; j < 8; ++j) u[j] = e[j];
      fft8<false>(u);
#pragma unroll
      for (int kc = 0; kc < 8; ++kc) {
        const u32 wv = wreg[i * 8 + kc];
        const f32x2 wf = {__uint_as_float(wv << 16) * (1.0f / (2048.0f * 640.0f)), __uint_as_float(wv & 0xFFFF0000u) * (1.0f / (2048.0f * 640.0f))};
        u[kc] = cmul(u[kc], wf);
      }
      fft8<true>(u);
#pragma unroll
      for (int j = 0; j < 8; ++j) e[j] = u[j];
    }
    __syncthreads();
#pragma unroll
    for (int kb = 0; kb < 16; ++kb) { f32x2 x = buf[ka2 * 136 + 8 * kb + c2]; v[kb] = kb ? cmulc(x, tw[16 * c2 * kb]) : x; }
    fft16<true>(v);
#pragma unroll
    for (int b = 0; b < 16; ++b) buf[ka2 * 136 + 8 * b + c2] = v[b];
    __syncthreads();
#pragma unroll
    for (int ka = 0; ka < 16; ++ka) { f32x2 x = buf[ka * 136 + t]; v[ka] = ka ? cmulc(x, tw[t * ka]) : x; }
    fft16<true>(v);
    if (pass == 1) {
#pragma unroll
      for (int a = 0; a < 16; ++a) keep[a] = v[a];
    } else {
      if (q == 0 && c == 0) {
#pragma unroll
        for (int a = 0; a < 16; ++a) v[a].y = keep[a].x;
      }
      __syncthreads();
#pragma unroll
      for (int a = 0; a < 16; ++a) fin[(128 * a + t) * 4 + c] = v[a];
      __syncthreads();
    }
  }
#pragma unroll
  for (int i = 0; i < 4; ++i) {
    const int n = tid + 512 * i;
    const f32x4 x0 = *(const f32x4*)(fin + n * 4), x1 = *(const f32x4*)(fin + n * 4 + 2);
    u32x4 hi = {pk2(x0[0], x0[1]), pk2(x0[2], x0[3]), pk2(x1[0], x1[1]), pk2(x1[2], x1[3])};
    u32x4 lo = {pk2(x0[0] - __uint_as_float(hi[0] << 16), x0[1] - __uint_as_float(hi[0] & 0xFFFF0000u)),
                pk2(x0[2] - __uint_as_float(hi[1] << 16), x0[3] - __uint_as_float(hi[1] & 0xFFFF0000u)),
                pk2(x1[0] - __uint_as_float(hi[2] << 16), x1[1] - __uint_as_float(hi[2] & 0xFFFF0000u)),
                pk2(x1[2] - __uint_as_float(hi[3] << 16), x1[3] - __uint_as_float(hi[3] & 0xFFFF0000u))};
    const u32 o = (u32)((n >> 4) * 40960 + (q >> 2) * 1024 + ((q & 3) * 16 + (n & 15)) * 16);
    st16c(ws, o, OFF_FY, hi);
    st16c(ws, o + 20480, OFF_FY, lo);
  }
}

DEV void fw_task(const Params& p, rsrc_t ws, int k) {
  for (int q = k * 2560 + threadIdx.x; q < (k + 1) * 2560; q += 512) {
    const int which = q / 51200, r = q % 51200, tile = r / 1280, ks = (r % 1280) >> 6, ln = r & 63;
    u32x4 hi, lo;
    float val[8];
#pragma unroll
    for (int j = 0; j < 8; ++j) {
      const int aa = 16 * tile + (ln & 15), bb = 32 * ks + 8 * (ln >> 4) + j;
      const int kp = which ? bb : aa, d = which ? aa : bb;
      const int kk = (kp == 1) ? 320 : (kp >> 1);
      float sn, cs;
      sincospif((float)((kk * d) % 640) * (1.0f / 320.0f), &sn, &cs);
      float x = (kp & 1) && kp != 1 ? -sn : cs;
      if (which && kp > 1) x *= 2.0f;
      val[j] = x;
    }
#pragma unroll
    for (int j = 0; j < 4; ++j) { hi[j] = pk2(val[2 * j], val[2 * j + 1]); lo[j] = pk2lo(val[2 * j], val[2 * j + 1], hi[j]); }
    const u32 o = (which ? OFF_FW3 : OFF_FW1) + (u32)((tile * 40 + ks) * 1024 + ln * 16);
    st16c(ws, o, 0, hi);
    st16c(ws, o + 20480, 0, lo);
  }
  VM_DRAIN();
  __syncthreads();
  if (threadIdx.x == 0) __hip_atomic_fetch_add((u32*)(p.ws + OFF_FWC), 1u, __ATOMIC_RELAXED, __HIP_MEMORY_SCOPE_AGENT);
}
DEV void wt_task(const Params& p, rsrc_t ws, int k, unsigned char* lds) {
  const int tid = threadIdx.x;
  const rsrc_t rr = mk_rsrc(p.wr, 2048 * 642), ri = mk_rsrc(p.wi, 2048 * 642);
  for (int blk = 0; blk < 2; ++blk) {
    const int row0 = k * 128 + blk * 64;
    __syncthreads();
#pragma unroll
    for (int i = 0; i < 6; ++i) {
      const int c = tid + 512 * i;
      if (c < 2568) {
        *(u32x4*)(lds + c * 16) = ld16(rr, (u32)c * 16, (u32)row0 * 642);
        *(u32x4*)(lds + 41088 + c * 16) = ld16(ri, (u32)c * 16, (u32)row0 * 642);
      }
    }
    __syncthreads();
    const int r = tid & 63;
    const u16* a = (const u16*)lds + r * 321;
    for (int q = tid >> 6; q < 81; q += 8) {
      u32x4 o;
#pragma unroll
      for (int j = 0; j < 4; ++j) { const int col = q < 80 ? 4 * q + j : 320; o[j] = (u32)a[col] | ((u32)a[20544 + col] << 16); }
      st16c(ws, (u32)(row0 + r) * 16, OFF_WT + (u32)q * 32768, o);
    }
  }
  VM_DRAIN();
  __syncthreads();
  if (tid == 0) __hip_atomic_fetch_add((u32*)(p.ws + OFF_FWC), 1u, __ATOMIC_RELAXED, __HIP_MEMORY_SCOPE_AGENT);
}
DEV void wait_fw(rsrc_t ws) {
  if (threadIdx.x < 64) { bool dead = false; wait_prog(ws, OFF_FWC, 1, 56, threadIdx.x, dead, 1); }
  __syncthreads();
}
DEV void f1_task(const Params& p, rsrc_t ws, int type, int rg, unsigned char* lds) {
  const int tid = threadIdx.x, l = tid & 63, c16 = l & 15, g = l >> 4;
  const int w = __builtin_amdgcn_readfirstlane(tid >> 6);
  const int tile = type * 8 + w;
  wait_fw(ws);
  u32x4 wt[2][20];
#pragma unroll
  for (int i = 0; i < 2; ++i)
#pragma unroll
    for (int ks = 0; ks < 20; ++ks) { wt[i][ks] = ld16c(ws, l * 16, OFF_FW1 + (u32)((tile * 40 + i * 20 + ks) * 1024)); THROTTLE(ks); }
  __builtin_amdgcn_s_waitcnt(0x0F70);
  stream_tiles<2, true>((const unsigned char*)p.ax + rg * 4 * 40960, [](int i) { return i * 40960; }, 4, 4, lds,
    [&](int i, const unsigned char* t, auto dma) {
      f32x4 acc[2][2] = {};
      sweep40<8>(t + l * 16, dma, [&](int f, u32x4 xb) {
        acc[0][f & 1] = mfma(wt[0][f >> 1], xb, acc[0][f & 1]);
        acc[1][f & 1] = mfma(wt[1][f >> 1], xb, acc[1][f & 1]);
      });
#pragma unroll
      for (int tt = 0; tt < 2; ++tt) {
        f32x4 o = acc[0][tt] + acc[1][tt];
        st16c(ws, (u32)(g * 32768 + c16 * 16 + tt * 256), OFF_FX + (u32)(tile * 4 * 32768 + (rg * 4 + i) * 512), __builtin_bit_cast(u32x4, o));
      }
    },
    [&](int k) { if (k == 4 && tid == 0) __hip_atomic_fetch_add((u32*)(p.ws + OFF_F1C + type * 128), 1u, __ATOMIC_RELAXED, __HIP_MEMORY_SCOPE_AGENT); });
}
DEV void f3_task(const Params& p, rsrc_t ws, int type, int rg, unsigned char* lds) {
  const int tid = threadIdx.x, l = tid & 63, c16 = l & 15, g = l >> 4;
  const int w = __builtin_amdgcn_readfirstlane(tid >> 6);
  const int tile = type * 8 + w;
  wait_fw(ws);
  u32x4 wt[2][20];
#pragma unroll
  for (int i = 0; i < 2; ++i)
#pragma unroll
    for (int ks = 0; ks < 20; ++ks) { wt[i][ks] = ld16c(ws, l * 16, OFF_FW3 + (u32)((tile * 40 + i * 20 + ks) * 1024)); THROTTLE(ks); }
  __builtin_amdgcn_s_waitcnt(0x0F70);
  if (w == 0) {
    bool dead = false;
    wait_prog(ws, OFF_F2C, 1, 80, l, dead, 1);
    __builtin_amdgcn_fence(__ATOMIC_ACQUIRE, "agent");
    VM_DRAIN();
  }
  __syncthreads();
  u16* outp = p.out + (rg * 128 + c16) * 1280 + tile * 16 + 4 * g;
  stream_tiles<1, false>(p.ws + OFF_FY + rg * 8 * 40960, [](int i) { return i * 40960; }, 8, 8, lds,
    [&](int i, const unsigned char* t, auto dma) {
      f32x4 a0 = {0, 0, 0, 0}, a1 = {0, 0, 0, 0}, a2 = {0, 0, 0, 0};
      sweep40<10>(t + l * 16, dma, [&](int f, u32x4 y) {
        if (f & 1) a2 = mfma(wt[0][f >> 1], y, a2);
        else { a0 = mfma(wt[0][f >> 1], y, a0); a1 = mfma(wt[1][f >> 1], y, a1); }
      });
      f32x4 o = a0 + (a1 + a2);
      u32x2 ov = {pk2(o[0], o[1]), pk2(o[2], o[3])};
      *(u32x2*)(outp + i * 16 * 1280) = ov;
    },
    [&](int) {});
}

DEV void out_task(const Params& p, rsrc_t ws, int s, int cg, unsigned char* lds) {
  const int tid = threadIdx.x, l = tid & 63, c16 = l & 15, g = l >> 4;
  const int w = __builtin_amdgcn_readfirstlane(tid >> 6);
  const int G = 8 * cg + w;
  const int pp = (c16 >> 1) & 1, j = 2 * (c16 >> 2) + (c16 & 1);
  const u32 ox = (u32)(pp * 16384 + g * 128 + j * 16), om = (u32)(pp * 4096 + g * 128 + j * 16);
  bool dead = false;
  if (w == 0) wait_prog(ws, OFF_PVU + (u32)seg_prod(s) * 128, 32, seg_need(s), l, dead, 1);
  __syncthreads();
  u32x4 ua[32];
#pragma unroll
  for (int h = 0; h < 32; ++h) { ua[h] = ld16c(ws, ox + (h & 7) * 512, OFF_UX + (u32)((s * 80 + 2 * G) * 16384 + (h >> 3) * 4096)); THROTTLE(h); }
  SCHED_FENCE();
  if (w < 2) {
    wait_prog(ws, OFF_PPX + (u32)(cg + 5 * (s & 3)) * 128, 1, (u32)s, l, dead, 2);
    if (w == 0) wait_prog(ws, OFF_PPX, 32, (u32)s + 1, l, dead, 1, 128);
    else wait_prog(ws, OFF_PUM + (u32)cg * 2048, 16, (u32)s + 1, l, dead, 1, 128);
  }
  __syncthreads();
  u32x4 pr[10], ub[8];
#pragma unroll
  for (int i = 0; i < 8; ++i) pr[i] = ld16c(ws, tid * 16, OFF_PXX + (u32)(s * 65536 + i * 8192));
#pragma unroll
  for (int i = 0; i < 2; ++i) pr[8 + i] = ld16c(ws, tid * 16, OFF_PXM + (u32)(s * 16384 + i * 8192));
#pragma unroll
  for (int q = 0; q < 8; ++q) ub[q] = ld16c(ws, om + q * 512, OFF_UM + (u32)((s * 80 + 2 * G) * 4096));
#pragma unroll
  for (int i = 0; i < 10; ++i) *(u32x4*)(lds + (i * 512 + tid) * 16) = pr[i];
  __syncthreads();
  f32x4 acc[2] = {};
  {
    const unsigned char* px = lds + l * 16;
    const unsigned char* pm = lds + 65536 + (g * 16 + c16) * 16;
    auto rd = [&](int i) { return i < 64 ? *(const u32x4*)(px + i * 1024) : *(const u32x4*)(pm + (i & 1) * 8192 + ((i - 64) >> 1) * 1024); };
    u32x4 pb[12];
#pragma unroll
    for (int i = 0; i < 12; ++i) pb[i] = rd(i);
    SCHED_FENCE();
#pragma unroll
    for (int i = 0; i < 80; ++i) {
      acc[i & 1] = mfma(i < 64 ? ua[i >> 1] : ub[(i - 64) >> 1], pb[i % 12], acc[i & 1]);
      if (i + 12 < 80) pb[i % 12] = rd(i + 12);
      SCHED_FENCE();
    }
  }
#pragma unroll
  for (int tt = 0; tt < 2; ++tt) {
    u32x2 o = {pk2(acc[tt][0], acc[tt][1]), pk2(acc[tt][2], acc[tt][3])};
    *(u32x2*)(p.out + (32 * s + 16 * tt + c16) * 1280 + 640 + 16 * G + 4 * g) = o;
  }
}

DEV void prefetch_role(rsrc_t ws, int x, int k) {
  const int l = threadIdx.x & 63;
  const int w = __builtin_amdgcn_readfirstlane(threadIdx.x >> 6);
  if (w >= PFW) return;
  bool dead = false;
  u32 seen0 = 0, seen1 = 0;
  for (int s = 1; s < NSEG; ++s) {
    const u32 prog = wait_prog(ws, OFF_PUM + (u32)(x * 16 + k) * 128, 1, (u32)(s > PFD ? s - PFD : 0), l, dead, 1);
    if ((u32)s <= prog + 1) { s = (int)prog + 1; continue; }
    const u32 need = seg_need(s);
    if (seg_prod(s)) { if (seen1 < need) seen1 = wait_prog(ws, OFF_PVU + 128, 32, need, l, dead, 1); }
    else { if (seen0 < need) seen0 = wait_prog(ws, OFF_PVU, 32, need, l, dead, 1); }
#pragma nounroll
    for (int i = w; i < 14 * (8192 / (64 * PFSTR)); i += PFW) {
      const int u = k * 14 + i / (8192 / (64 * PFSTR));
      u32 o;
      if (x == 0) o = ((u & 1) ? OFF_QX : OFF_KX) + (u32)((s * 32 + (u >> 1)) * 8192);
      else if (x == 3 && u >= 48) o = ((u & 1) ? OFF_QX : OFF_KX) + (u32)((s * 32 + 28 + ((u - 48) >> 1)) * 8192);
      else o = OFF_UX + (u32)((s * 80 + (x - 1) * 28) * 16384 + u * 8192);
      asm volatile("buffer_load_dword v255, %0, %1, %2 offen sc1\n\ts_waitcnt vmcnt(%3)"
                   :: "v"(l * PFSTR), "s"(ws), "s"(o + (u32)(i % (8192 / (64 * PFSTR))) * (64 * PFSTR)), "n"(PFQ) : "memory", "v255");
      __builtin_amdgcn_s_sleep(PFS);
    }
  }
  VM_DRAIN();
}

constexpr int T_SET = 56, T_F1 = 80, T_F2 = 80, T_F3 = 80, T_OUT = 320;
DEV int next_ticket(const Params& p, unsigned char* lds, u32 off) {
  u32* tk = (u32*)(lds + LDS_MAIN - 16);
  __syncthreads();
  if (threadIdx.x == 0) *tk = __hip_atomic_fetch_add((u32*)(p.ws + off), 1u, __ATOMIC_RELAXED, __HIP_MEMORY_SCOPE_AGENT);
  __syncthreads();
  return __builtin_amdgcn_readfirstlane(*tk);
}

__global__ __launch_bounds__(512, 2) void k_main(Params p, int mode) {
  extern __shared__ __attribute__((aligned(16))) unsigned char lds[];
  const rsrc_t ws = mk_rsrc(p.ws, WS_BYTES);
  const u32 cb = comm_base(p.ws);
  const int b = blockIdx.x, x = b & 7, slot = b >> 3;
  if (mode & 1) GSTAMP(0);
  const int cidx = (x >= 4 || slot >= 28) ? 1000 : (x == 0 ? slot : ((x == 3 && slot >= 24) ? 4 + slot : 32 + (x - 1) * 28 + slot));
  if (cidx < 112) {
    if (!(mode & 1)) return;
    if (cidx < 32) score_role(p, ws, cb, cidx, lds);
    else value_role(p, ws, cb, cidx - 32, lds);
    GSTAMP(3);
    return;
  }
  if (x < 4) {
    if (mode & 1) {
      prefetch_role(ws, x, slot - 28);
    }
    return;
  }
  if (x >= 4 && (mode & 2)) {
    const int h = (x - 4) * 8 + (slot >> 2);
    if (slot & 2) vu_role(p, ws, h, slot & 1, lds);
    else qk_role(p, ws, h, slot & 1, lds);
    GSTAMP(2);
  }
  if (!(mode & 4)) return;
  const bool outonly = (slot & 3) == 3 && (slot & 4);
  if (!outonly) {
    int t = next_ticket(p, lds, OFF_TICKET) - T_SET;
    while (t < 0) { const int k = t + T_SET; if (k < 40) fw_task(p, ws, k); else wt_task(p, ws, k - 40, lds); t = next_ticket(p, lds, OFF_TICKET) - T_SET; }
    while (t < T_F1) { f1_task(p, ws, t % 5, t / 5, lds); t = next_ticket(p, lds, OFF_TICKET) - T_SET; }
    GSTAMP(4);
    while (t < T_F1 + T_F2) {
      const int q = t - T_F1;
      if (threadIdx.x < 64) { bool dead = false; wait_prog(ws, OFF_F1C + (u32)(q >> 4) * 128, 1, 16, threadIdx.x, dead, 1); }
      wait_fw(ws);
      fft_cols(p, ws, q, lds);
      VM_DRAIN();
      __syncthreads();
      if (threadIdx.x == 0) __hip_atomic_fetch_add((u32*)(p.ws + OFF_F2C), 1u, __ATOMIC_RELAXED, __HIP_MEMORY_SCOPE_AGENT);
      t = next_ticket(p, lds, OFF_TICKET) - T_SET;
    }
    GSTAMP(5);
    while (t < T_F1 + T_F2 + T_F3) { const int q = t - T_F1 - T_F2; f3_task(p, ws, q % 5, q / 5, lds); t = next_ticket(p, lds, OFF_TICKET) - T_SET; }
  }
  GSTAMP(6);
  for (int t = next_ticket(p, lds, OFF_TICKET2); t < T_OUT; t = next_ticket(p, lds, OFF_TICKET2)) out_task(p, ws, t / 5, t % 5, lds);
  GSTAMP(3);
}

extern "C" void kernel_launch(void* const* d_in, const int* in_sizes, int n_in, void* d_out, int out_size, void* d_ws, size_t ws_size,
                              hipStream_t stream) {
  Params p;
  p.ax = (const u16*)d_in[0]; p.wr = (const u16*)d_in[1]; p.wi = (const u16*)d_in[2];
  p.x = (const u16*)d_in[3]; p.mem0 = (const u16*)d_in[4]; p.wq = (const u16*)d_in[5]; p.wk = (const u16*)d_in[6];
  p.wv = (const u16*)d_in[7]; p.wo = (const u16*)d_in[8];
  p.out = (u16*)d_out; p.ws = (unsigned char*)d_ws;
  (void)hipFuncSetAttribute((const void*)k_prep, hipFuncAttributeMaxDynamicSharedMemorySize, 48 * 1024);
  (void)hipFuncSetAttribute((const void*)k_main, hipFuncAttributeMaxDynamicSharedMemorySize, LDS_MAIN);
  k_prep<<<256, 512, 48 * 1024, stream>>>(p);
  k_main<<<256, 512, LDS_MAIN, stream>>>(p, 7);
}
```

```cpp
#include <hip/hip_runtime.h>
#include <hip/hip_bf16.h>
#include <stdint.h>
#define BAR4
#define BULK1
#define SPREAD

typedef unsigned short u16;
typedef unsigned int u32;
typedef unsigned long long u64;
typedef __bf16 bf16x2 __attribute__((ext_vector_type(2)));
typedef __bf16 bf16x8 __attribute__((ext_vector_type(8)));
typedef __bf16 bf16x16 __attribute__((ext_vector_type(16)));
typedef float f32x2 __attribute__((ext_vector_type(2)));
typedef float f32x4 __attribute__((ext_vector_type(4)));
typedef u32 u32x2 __attribute__((ext_vector_type(2)));
typedef u32 u32x4 __attribute__((ext_vector_type(4)));
typedef u32 u32x8 __attribute__((ext_vector_type(8)));
typedef __amdgpu_buffer_rsrc_t rsrc_t;
#define DEV __device__ __forceinline__

constexpr int NSEG = 64;
constexpr u32 MB = 1u << 20;
constexpr u32 OFF_WF = 6 * MB;
constexpr u32 OFF_WOF = 21 * MB;
constexpr u32 OFF_BF = 26 * MB;
constexpr u32 OFF_XF = 51 * MB;
constexpr u32 OFF_AXF = 54 * MB;
constexpr u32 OFF_KX = 57 * MB;
constexpr u32 OFF_QX = 73 * MB;
constexpr u32 OFF_UX = 89 * MB;
constexpr u32 OFF_UM = 169 * MB;
constexpr u32 OFF_PXX = 189 * MB;
constexpr u32 OFF_PXM = 193 * MB;
constexpr u32 OFF_FX = 194 * MB;
constexpr u32 OFF_FY = 200 * MB;
constexpr u32 OFF_FLAGS = 206 * MB;
constexpr u32 OFF_FW1 = 208 * MB;
constexpr u32 OFF_FW3 = 210 * MB;
constexpr u32 OFF_SIDE = 212 * MB;
constexpr u32 SIDE_STEP = 112 * 128;
constexpr u32 OFF_WT = 213 * MB;
constexpr u32 WS_BYTES = 216 * MB;
constexpr u32 STEP_BYTES = 65536;
constexpr u32 SENT = 0xFFFFFFFFu;
constexpr u32 SPIN_CAP = 1u << 16;
constexpr u32 OFF_PQK = OFF_FLAGS + 0xA000;
constexpr u32 OFF_PVU = OFF_FLAGS + 0xB000;
constexpr u32 OFF_PPX = OFF_FLAGS;
constexpr u32 OFF_PUM = OFF_FLAGS + 0x1000;
constexpr u32 OFF_TICKET = OFF_FLAGS + 0xB400;
constexpr u32 OFF_F1C = OFF_FLAGS + 0xB480;
constexpr u32 OFF_F2C = OFF_FLAGS + 0xB800;
constexpr u32 OFF_FWC = OFF_FLAGS + 0xB880;
constexpr u32 OFF_TICKET2 = OFF_FLAGS + 0xB900;
constexpr u32 OFF_INITC = OFF_FLAGS + 0xBC00;
constexpr u32 OFF_ROLE = OFF_FLAGS + 0x9000;
constexpr u32 OFF_XFC = OFF_FLAGS + 0xBF00;
constexpr u32 OFF_BC = OFF_FLAGS + 0xBE00;
constexpr u32 FLAG_BYTES = 0xC000;
constexpr u32 OFF_VOTE = OFF_FLAGS + 0xBA00;
constexpr int LDS_MAIN = 97 * 1024;
#define HS0 4
#define HS1 20
constexpr int NHELP = (HS1 - HS0) / 2;
__host__ __device__ constexpr int seg_prod(int s) { return (s >= HS0 && s < HS1) ? (s & 1) : 0; }
__host__ __device__ constexpr u32 seg_need(int s) {
  return s < HS0 ? (u32)s + 1 : (s < HS1 ? ((s & 1) ? (u32)((s - HS0) >> 1) + 1 : (u32)(HS0 + ((s - HS0) >> 1)) + 1) : (u32)(s - NHELP) + 1);
}
__host__ __device__ constexpr int tile_seg(int par, int i) {
  return par ? HS0 + 2 * i + 1 : (i < HS0 ? i : (i < HS0 + NHELP ? HS0 + 2 * (i - HS0) : i + NHELP));
}

struct Params {
  const u16* ax; const u16* wr; const u16* wi;
  const u16* x; const u16* mem0; const u16* wq; const u16* wk; const u16* wv; const u16* wo;
  u16* out; unsigned char* ws;
};

DEV rsrc_t mk_rsrc(const void* p, u32 bytes) { return __builtin_amdgcn_make_buffer_rsrc((void*)p, 0, bytes, 0x00020000); }
DEV u32x4 ld16(rsrc_t r, u32 vo, u32 so) { return __builtin_amdgcn_raw_buffer_load_b128(r, vo, so, 0); }
DEV u32x4 ld16c(rsrc_t r, u32 vo, u32 so) { return __builtin_amdgcn_raw_buffer_load_b128(r, vo, so, 16); }
DEV u32x2 ld8c(rsrc_t r, u32 vo, u32 so) { return __builtin_amdgcn_raw_buffer_load_b64(r, vo, so, 16); }
DEV u32 ld4c(rsrc_t r, u32 vo, u32 so) { return __builtin_amdgcn_raw_buffer_load_b32(r, vo, so, 16); }
DEV void st16(rsrc_t r, u32 vo, u32 so, u32x4 v) { __builtin_amdgcn_raw_buffer_store_b128(v, r, vo + so, 0, 0); }
DEV void st16c(rsrc_t r, u32 vo, u32 so, u32x4 v) { __builtin_amdgcn_raw_buffer_store_b128(v, r, vo + so, 0, 16); }
DEV void st8c(rsrc_t r, u32 vo, u32 so, u32x2 v) { __builtin_amdgcn_raw_buffer_store_b64(v, r, vo, so, 16); }
DEV void st4c(rsrc_t r, u32 vo, u32 so, u32 v) { __builtin_amdgcn_raw_buffer_store_b32(v, r, vo, so, 16); }
DEV u32 pk2(float a, float b) { bf16x2 v = {(__bf16)a, (__bf16)b}; return __builtin_bit_cast(u32, v); }
DEV u32 pk2s(float a, float b) { u32 v = pk2(a, b); return v < 0xFFFFFFFEu ? v : 0xFFFFFFFEu; }
DEV u32 pk2lo(float a, float b, u32 hi) { return pk2(a - __uint_as_float(hi << 16), b - __uint_as_float(hi & 0xFFFF0000u)); }
DEV f32x4 mfma(u32x4 a, u32x4 b, f32x4 c) {
  return __builtin_amdgcn_mfma_f32_16x16x32_bf16(__builtin_bit_cast(bf16x8, a), __builtin_bit_cast(bf16x8, b), c, 0, 0, 0);
}
DEV f32x4 smfma(u32x4 a, u32x8 b, f32x4 c, int idx) {
  return __builtin_amdgcn_smfmac_f32_16x16x64_bf16(__builtin_bit_cast(bf16x8, a), __builtin_bit_cast(bf16x16, b), c, idx, 0, 0);
}
DEV u32x8 cat(u32x4 a, u32x4 b) { return __builtin_shufflevector(a, b, 0, 1, 2, 3, 4, 5, 6, 7); }
DEV u32 comm_base(const unsigned char* ws) { return (u32)((2 * MB - ((uintptr_t)ws & (2 * MB - 1))) & (2 * MB - 1)); }
DEV u64 stamp_cyc() { u64 t; asm volatile("s_memtime %0\n\ts_waitcnt lgkmcnt(0)" : "=s"(t) :: "memory"); return t; }
DEV u32 umax3(u32 a, u32 b, u32 c) { u32 m = a > b ? a : b; return m > c ? m : c; }
DEV float fmx(float a, float b) { return __builtin_elementwise_maximum(a, b); }
#define SCHED_FENCE() __builtin_amdgcn_sched_barrier(0)
#define VM_DRAIN() asm volatile("s_waitcnt vmcnt(0)" ::: "memory")
#define RAW_BARRIER() do { asm volatile("s_waitcnt lgkmcnt(0)" ::: "memory"); __builtin_amdgcn_s_barrier(); asm volatile("" ::: "memory"); } while (0)

#define STAMP(ev)
#define STAMPV(ev, val)
#define COUNT(ev, val)
#define GSTAMP(slot)

typedef short s16x4 __attribute__((ext_vector_type(4)));
#define LAS __attribute__((address_space(3)))
DEV u32x4 tr_frag(const unsigned char* a0, const unsigned char* a1) {
  const u32x2 x = __builtin_bit_cast(u32x2, __builtin_amdgcn_ds_read_tr16_b64_v4i16((LAS s16x4*)a0));
  const u32x2 y = __builtin_bit_cast(u32x2, __builtin_amdgcn_ds_read_tr16_b64_v4i16((LAS s16x4*)a1));
  return (u32x4){x.x, x.y, y.x, y.y};
}
DEV float dpp_ror8(float x) { return __uint_as_float(__builtin_amdgcn_update_dpp(0, (int)__float_as_uint(x), 0x128, 0xF, 0xF, true)); }
DEV float red_max_g(float m) {
  auto a = __builtin_amdgcn_permlane16_swap(__float_as_uint(m), __float_as_uint(m), false, false);
  m = fmx(__uint_as_float(a[0]), __uint_as_float(a[1]));
  auto b = __builtin_amdgcn_permlane32_swap(__float_as_uint(m), __float_as_uint(m), false, false);
  return fmx(__uint_as_float(b[0]), __uint_as_float(b[1]));
}
DEV float red_sum_g(float m) {
  auto a = __builtin_amdgcn_permlane16_swap(__float_as_uint(m), __float_as_uint(m), false, false);
  m = __uint_as_float(a[0]) + __uint_as_float(a[1]);
  auto b = __builtin_amdgcn_permlane32_swap(__float_as_uint(m), __float_as_uint(m), false, false);
  return __uint_as_float(b[0]) + __uint_as_float(b[1]);
}
DEV float red_max(float m) { return red_max_g(fmx(m, dpp_ror8(m))); }
DEV float red_sum(float m) { return red_sum_g(m + dpp_ror8(m)); }

#define NPROBE 3
DEV u32 probe(rsrc_t ws, u32 cb, int idx, int page, int l) {
  const u32 vo = (l == 0) ? 0u : 0x80000000u;
  const u32 so = cb + (u32)(page * 32 + 30) * STEP_BYTES + (u32)(2 + (idx >> 6)) * 16384 + (u32)(idx & 63) * 128;
  u32 b0 = ~0u, b1 = ~0u;
#pragma nounroll
  for (int rep = 0; rep < 2 * NPROBE; ++rep) {
    u64 t0 = stamp_cyc();
    st4c(ws, vo, so + (u32)(rep & 1) * 8192, SENT);
    VM_DRAIN();
    u64 t1 = stamp_cyc();
    u32 d = (u32)(t1 - t0);
    if (rep & 1) b1 = d < b1 ? d : b1; else b0 = d < b0 ? d : b0;
  }
  return b1 < b0 ? 1u : 0u;
}

DEV u32 xcc_group() { return ((u32)__builtin_amdgcn_s_getreg(20 | (3 << 11)) >> 2) & 3u; }
DEV void vote(const Params& p, int page, u32 r, int l) {
  if (l == 0) __hip_atomic_fetch_add((u32*)(p.ws + OFF_VOTE + (xcc_group() * 2 + page) * 64), r + 0x10000u, __ATOMIC_RELAXED, __HIP_MEMORY_SCOPE_AGENT);
}
DEV u32 vote_result(rsrc_t ws, int l) {
  const u32 x = ld4c(ws, (u32)(l & 1) * 64, OFF_VOTE + xcc_group() * 128);
  const u32 m = (2 * (x & 0xFFFFu) > (x >> 16)) ? 1u : 0u;
  return (u32)__builtin_amdgcn_readlane(m, 0) | ((u32)__builtin_amdgcn_readlane(m, 1) << 1);
}
#define D1 140
#define D2 12
#define D3 60
#define D4 12
#define DV 15
#define DX 25
#define DL 5
#define PFD 2
#define DLS 16
#define THR 6
#define THR_S2(x) #x
#define THR_S(x) THR_S2(x)
#define THROTTLE(i) asm volatile("s_waitcnt vmcnt(" THR_S(THR) ")" ::: "memory")
#define UMPOS 0
#define PSLEEP 0
#define PFW 1
#define PFQ 4
#define PFS 2
#define PFSTR 128
#define PROT v
#define KQPOS 0
#define DA 12
DEV u32 rtime() { return (u32)__builtin_amdgcn_s_memrealtime(); }
DEV void wait_until(u32 T) {
  u32 r = rtime();
  if ((int)(T - r) < 28) {
#pragma nounroll
    for (int i = 0; i < 64 && (int)(T - r) > 0; ++i) r = rtime();
    return;
  }
#pragma nounroll
  for (int i = 0; i < 4096 && (int)(T - r) > 35; ++i) r = rtime();
  u32 n;
  asm volatile(
    "s_memrealtime s[80:81]\n\ts_nop 2\n\ts_memrealtime s[82:83]\n\ts_nop 2\n\ts_memrealtime s[84:85]\n\ts_nop 2\n\ts_memrealtime s[86:87]\n\ts_nop 2\n\t"
    "s_memrealtime s[88:89]\n\ts_nop 2\n\ts_memrealtime s[90:91]\n\ts_nop 2\n\ts_memrealtime s[92:93]\n\ts_nop 2\n\ts_memrealtime s[94:95]\n\ts_waitcnt lgkmcnt(0)\n\t"
    "s_mov_b32 s81, 0\n\t"
    "s_cmp_eq_u32 s82, s80\n\ts_addc_u32 s81, s81, 0\n\t"
    "s_cmp_eq_u32 s84, s80\n\ts_addc_u32 s81, s81, 0\n\t"
    "s_cmp_eq_u32 s86, s80\n\ts_addc_u32 s81, s81, 0\n\t"
    "s_cmp_eq_u32 s88, s80\n\ts_addc_u32 s81, s81, 0\n\t"
    "s_cmp_eq_u32 s90, s80\n\ts_addc_u32 s81, s81, 0\n\t"
    "s_cmp_eq_u32 s92, s80\n\ts_addc_u32 s81, s81, 0\n\t"
    "s_cmp_eq_u32 s94, s80\n\ts_addc_u32 s81, s81, 0\n\t"
    "s_sub_u32 %0, %1, s80\n\t"
    "s_mul_i32 %0, %0, 3\n\t"
    "s_lshl1_add_u32 %0, s81, %0\n\t"
    "s_sub_i32 %0, %0, 64\n\t"
    "s_max_i32 %0, %0, 0\n\t"
    "s_min_i32 %0, %0, 63\n\t"
    "s_bitcmp1_b32 %0, 0\n\ts_cbranch_scc0 1f\n\ts_nop 5\n\t1:\n\t"
    "s_bitcmp1_b32 %0, 1\n\ts_cbranch_scc0 2f\n\ts_nop 7\n\t2:\n\t"
    "s_bitcmp1_b32 %0, 2\n\ts_cbranch_scc0 3f\n\ts_nop 11\n\t3:\n\t"
    "s_bitcmp1_b32 %0, 3\n\ts_cbranch_scc0 4f\n\ts_nop 15\n\ts_nop 3\n\t4:\n\t"
    "s_bitcmp1_b32 %0, 4\n\ts_cbranch_scc0 5f\n\ts_nop 15\n\ts_nop 15\n\ts_nop 3\n\t5:\n\t"
    "s_bitcmp1_b32 %0, 5\n\ts_cbranch_scc0 6f\n\ts_nop 15\n\ts_nop 15\n\ts_nop 15\n\ts_nop 15\n\ts_nop 3\n\t6:\n\t"
    : "=&s"(n) : "s"(T)
    : "scc", "memory", "s80", "s81", "s82", "s83", "s84", "s85", "s86", "s87", "s88", "s89", "s90", "s91", "s92", "s93", "s94", "s95");
}
template <int CTRL> DEV int dpp_i(int x) { return __builtin_amdgcn_update_dpp(x, x, CTRL, 0xF, 0xF, false); }
DEV int row_max(int d) {
  d = max(d, dpp_i<0x121>(d));
  d = max(d, dpp_i<0x122>(d));
  d = max(d, dpp_i<0x124>(d));
  return max(d, dpp_i<0x128>(d));
}
DEV int wave_max(int d) {
  d = row_max(d);
  return max(max(__builtin_amdgcn_readlane(d, 0), __builtin_amdgcn_readlane(d, 16)), max(__builtin_amdgcn_readlane(d, 32), __builtin_amdgcn_readlane(d, 48)));
}
DEV u32 fine_now() {
  u32 n;
  asm volatile(
    "s_memrealtime s[80:81]\n\ts_nop 2\n\ts_memrealtime s[82:83]\n\ts_nop 2\n\ts_memrealtime s[84:85]\n\ts_nop 2\n\ts_memrealtime s[86:87]\n\ts_nop 2\n\t"
    "s_memrealtime s[88:89]\n\ts_nop 2\n\ts_memrealtime s[90:91]\n\ts_nop 2\n\ts_memrealtime s[92:93]\n\ts_waitcnt lgkmcnt(0)\n\t"
    "s_mov_b32 s81, 0\n\t"
    "s_cmp_eq_u32 s82, s80\n\ts_addc_u32 s81, s81, 0\n\t"
    "s_cmp_eq_u32 s84, s80\n\ts_addc_u32 s81, s81, 0\n\t"
    "s_cmp_eq_u32 s86, s80\n\ts_addc_u32 s81, s81, 0\n\t"
    "s_cmp_eq_u32 s88, s80\n\ts_addc_u32 s81, s81, 0\n\t"
    "s_cmp_eq_u32 s90, s80\n\ts_addc_u32 s81, s81, 0\n\t"
    "s_cmp_eq_u32 s92, s80\n\ts_addc_u32 s81, s81, 0\n\t"
    "s_mul_i32 s81, s81, 11\n\t"
    "s_lshr_b32 s81, s81, 4\n\t"
    "s_add_u32 %0, s80, 4\n\t"
    "s_sub_u32 %0, %0, s81\n\t"
    : "=&s"(n) :
    : "scc", "memory", "s80", "s81", "s82", "s83", "s84", "s85", "s86", "s87", "s88", "s89", "s90", "s91", "s92", "s93");
  return n;
}
struct Side { u32x2 a, b; };
DEV Side load_side(rsrc_t ws, int s, int l) {
  Side t;
  t.a = ld8c(ws, (u32)l * 128, OFF_SIDE + (u32)s * SIDE_STEP);
  t.b = ld8c(ws, (u32)(64 + (l & 15)) * 128, OFF_SIDE + (u32)s * SIDE_STEP);
  return t;
}
DEV void schedule(rsrc_t ws, int s, int l, Side& t, u32& Rm, u32& Rp, bool& dead) {
  if (__builtin_expect(__any(umax3(t.a.x, t.a.y, t.b.x > t.b.y ? t.b.x : t.b.y) == SENT), 0)) {
#pragma nounroll
    for (u32 spins = 0; !dead; ++spins) {
      asm volatile("" ::: "memory");
      t = load_side(ws, s, l);
      if (__all(umax3(t.a.x, t.a.y, t.b.x > t.b.y ? t.b.x : t.b.y) != SENT)) break;
      if (spins > SPIN_CAP) dead = true;
    }
  }
  u32 B = Rm;
  if (__builtin_expect(__any((t.a.x | t.b.x) != 0), 0)) {
    const u32 ref = rtime();
    const int dx = t.a.x ? (int)(t.a.x - ref) : -(1 << 30), dy = t.b.x ? (int)(t.b.x - ref) : -(1 << 30);
    B = ref + (u32)wave_max(max(dx, dy)) + D4 + DX;
  }
  const u32 d = __builtin_amdgcn_readfirstlane(t.a.y);
  Rp = B + ((d >> 10) & 1023u);
  Rm = Rp + ((d >> 20) & 1023u);
}

DEV u32 gather_mem(rsrc_t ws, u32 sbp, int wr, unsigned char* dA, unsigned char* dB, int l, bool& dead) {
  u32x4 a = ld16c(ws, (u32)l * 16, sbp + (u32)wr * 1024);
  u32 b = ld4c(ws, (u32)l * 4, sbp + 16384 + (u32)wr * 256);
  SCHED_FENCE();
  *(u32x4*)dA = a;
  *(u32*)dB = b;
  u32 m = umax3(umax3(a.x, a.y, a.z), a.w, b);
  u32 spins = 0;
  if (__builtin_expect(__any(m == SENT), 0)) {
#pragma nounroll
    while (!dead) {
      ++spins;
      asm volatile("" ::: "memory");
      a = ld16c(ws, (u32)l * 16, sbp + (u32)wr * 1024);
      b = ld4c(ws, (u32)l * 4, sbp + 16384 + (u32)wr * 256);
      m = umax3(umax3(a.x, a.y, a.z), a.w, b);
      if (__all(m != SENT)) break;
      if (spins > SPIN_CAP) dead = true;
    }
    *(u32x4*)dA = a;
    *(u32*)dB = b;
  }
  return spins;
}
DEV u32 wait_prog(rsrc_t ws, u32 off, int n, u32 need, int l, bool& dead, int slp, u32 stride = 4) {
  u32 mn = 0;
#pragma nounroll
  for (u32 spins = 0;; ++spins) {
    asm volatile("" ::: "memory");
    const u32 x = ld4c(ws, (u32)(l < n ? l : 0) * stride, off);
    int d = -(int)x;
    d = row_max(d);
    mn = (u32)(-max(__builtin_amdgcn_readlane(d, 0), __builtin_amdgcn_readlane(d, 16)));
    if (mn >= need || dead) break;
    if (spins > SPIN_CAP) { dead = true; break; }
    if (slp == 2) __builtin_amdgcn_s_sleep(32); else if (slp) __builtin_amdgcn_s_sleep(6); else __builtin_amdgcn_s_sleep(1);
  }
  asm volatile("" ::: "memory");
  return mn;
}

DEV void init_signal(const Params& p, bool on) {
  if (on && threadIdx.x == 0) __hip_atomic_fetch_add((u32*)(p.ws + OFF_INITC + (blockIdx.x & 7) * 64), 1u, __ATOMIC_RELAXED, __HIP_MEMORY_SCOPE_AGENT);
}
DEV int wrow(int r) { return r * 288 + (r >> 3) * 128; }
DEV void load_qk(const Params& p, int h, int w, int l, unsigned char* lds, u32x4 (&wt)[2][20], bool sig) {
  const rsrc_t src = mk_rsrc((w >> 2) ? p.wk : p.wq, 640 * 8192);
  u32x4 st[40];
#pragma unroll
  for (int i = 0; i < 40; ++i)
    st[i] = ld16(src, (u32)((l >> 4) * 8192 + (l & 15) * 16), (u32)((32 * (w & 3) + (i >> 3) * 128 + (i & 7) * 4) * 8192 + h * 256));
  unsigned char* img = lds + (w >> 2) * 38912;
  unsigned char* wp = img + wrow(32 * (w & 3)) + (l >> 4) * 288 + (l & 15) * 16;
  const unsigned char* rp = img + wrow(8 * (l >> 4)) + ((l >> 2) & 3) * 288 + (w & 3) * 64 + (l & 3) * 8;
#pragma unroll
  for (int r = 0; r < 5; ++r) {
#pragma unroll
    for (int i = 0; i < 8; ++i) *(u32x4*)(wp + wrow(4 * i)) = st[r * 8 + i];
    __syncthreads();
    if (r == 0) init_signal(p, sig);
#pragma unroll
    for (int kk = 0; kk < 4; ++kk)
#pragma unroll
      for (int i = 0; i < 2; ++i) wt[i][4 * r + kk] = tr_frag(rp + wrow(32 * kk) + i * 32, rp + wrow(32 * kk) + 4 * 288 + i * 32);
    __syncthreads();
  }
}
DEV void load_vu(const Params& p, int h, int w, int l, unsigned char* lds, u32x4 (&wv)[20], u32x4 (&wo)[5][4], bool sig) {
  const rsrc_t sv = mk_rsrc(p.wv, 640 * 8192), so = mk_rsrc(p.wo, 640 * 8192);
  u32x4 st[40];
#pragma unroll
  for (int i = 0; i < 20; ++i)
    st[i] = ld16(sv, (u32)((l >> 4) * 8192 + (l & 15) * 16), (u32)((40 * w + (i / 10) * 320 + (i % 10) * 4) * 8192 + h * 256));
#pragma unroll
  for (int i = 0; i < 20; ++i) st[20 + i] = ld16(so, (u32)l * 16, (u32)(h * 163840 + (w * 640 + (i / 10) * 5120 + (i % 10) * 64) * 16));
  unsigned char* wp = lds + wrow(40 * w) + (l >> 4) * 288 + (l & 15) * 16;
  const unsigned char* rp = lds + wrow(8 * (l >> 4)) + ((l >> 2) & 3) * 288 + w * 32 + (l & 3) * 8;
#pragma unroll
  for (int r = 0; r < 2; ++r) {
#pragma unroll
    for (int i = 0; i < 10; ++i) *(u32x4*)(wp + wrow(4 * i)) = st[r * 10 + i];
    __syncthreads();
    if (r == 0) init_signal(p, sig);
#pragma unroll
    for (int kk = 0; kk < 10; ++kk) wv[10 * r + kk] = tr_frag(rp + wrow(32 * kk), rp + wrow(32 * kk) + 4 * 288);
    __syncthreads();
  }
  const unsigned char* ro = lds + (4 * (l >> 4) + ((l >> 2) & 3)) * 1312 + w * 160 + (l & 3) * 8;
#pragma unroll
  for (int r = 0; r < 2; ++r) {
#pragma unroll
    for (int i = 0; i < 10; ++i) {
      const int c = (w * 10 + i) * 64 + l;
      *(u32x4*)(lds + (c / 80) * 1312 + (c % 80) * 16) = st[20 + r * 10 + i];
    }
    __syncthreads();
#pragma unroll
    for (int kk = 0; kk < 2; ++kk)
#pragma unroll
      for (int ct = 0; ct < 5; ++ct) wo[ct][2 * r + kk] = tr_frag(ro + 32 * kk * 1312 + ct * 32, ro + (32 * kk + 16) * 1312 + ct * 32);
    __syncthreads();
  }
}

DEV void count_done(const Params& p, u32 off) {
  VM_DRAIN();
  __syncthreads();
  if (threadIdx.x == 0) __hip_atomic_fetch_add((u32*)(p.ws + off), 1u, __ATOMIC_RELAXED, __HIP_MEMORY_SCOPE_AGENT);
}
DEV void wait_init(rsrc_t ws) {
  if (threadIdx.x < 64) { bool dead = false; wait_prog(ws, OFF_XFC, 1, 16, threadIdx.x, dead, 0); }
  __syncthreads();
}
DEV void b_unit(const Params& p, rsrc_t ws, int u, unsigned char* lds) {
  const int tid = threadIdx.x, l = tid & 63, c16 = l & 15, g = l >> 4;
  const int w = __builtin_amdgcn_readfirstlane(tid >> 6);
  const int h = u >> 2, dh = (u >> 1) & 1, ch = u & 1;
  const rsrc_t rv = mk_rsrc(p.wv, 640 * 8192), ro = mk_rsrc(p.wo, 640 * 8192);
  u32x4 wo_r[10];
#pragma unroll
  for (int i = 0; i < 10; ++i) {
    const int c = tid + 512 * i, e = c / 40, cc = c % 40;
    wo_r[i] = ld16(ro, (u32)(e * 1280 + cc * 16), (u32)(h * 163840 + ch * 640));
  }
  u32x4 a[3][2][4];
#pragma unroll
  for (int i = 0; i < 3; ++i) {
    const int bl = (w & 3) + 4 * i;
    const u32 so = bl < 10 ? (u32)((32 * (10 * dh + bl)) * 8192 + h * 256) : 0x80000000u;
#pragma unroll
    for (int t = 0; t < 2; ++t)
#pragma unroll
      for (int ks = 0; ks < 4; ++ks)
        a[i][t][ks] = ld16(rv, (u32)((8 * (c16 >> 2) + (c16 & 3) + 4 * t) * 8192 + g * 16 + ks * 64), so);
  }
  SCHED_FENCE();
#pragma unroll
  for (int i = 0; i < 10; ++i) {
    const int c = tid + 512 * i, e = c / 40, cc = c % 40;
    *(u32x4*)(lds + e * 672 + (e >> 3) * 128 + cc * 16) = wo_r[i];
  }
  __syncthreads();
  init_signal(p, true);
  const u32 vo = (u32)(((c16 >> 1) & 1) * (16 * 20 * 1024) + (16 * g + 8 * (h & 1) + 2 * (c16 >> 2) + (c16 & 1)) * 16);
#pragma nounroll
  for (int half = 0; half < 2; ++half) {
    u32x4 bf[5][4];
    const unsigned char* rp = lds + (8 * g + ((l >> 2) & 3)) * 672 + g * 128 + half * 320 + (w >> 2) * 160 + (l & 3) * 8;
#pragma unroll
    for (int ct = 0; ct < 5; ++ct)
#pragma unroll
      for (int ks = 0; ks < 4; ++ks) bf[ct][ks] = tr_frag(rp + ks * (32 * 672 + 512) + ct * 32, rp + ks * (32 * 672 + 512) + 4 * 672 + ct * 32);
#pragma unroll
    for (int i = 0; i < 3; ++i) {
      const int bl = (w & 3) + 4 * i;
      if (bl < 10) {
#pragma unroll
        for (int ct = 0; ct < 5; ++ct) {
          f32x4 c0 = {0, 0, 0, 0}, c1 = {0, 0, 0, 0};
#pragma unroll
          for (int ks = 0; ks < 4; ++ks) { c0 = mfma(a[i][0][ks], bf[ct][ks], c0); c1 = mfma(a[i][1][ks], bf[ct][ks], c1); }
          u32x4 o = {pk2(c0[0], c0[1]), pk2(c0[2], c0[3]), pk2(c1[0], c1[1]), pk2(c1[2], c1[3])};
          st16c(ws, vo, OFF_BF + (u32)(((2 * (20 * ch + 10 * half + 5 * (w >> 2) + ct) * 16 + (h >> 1)) * 20 + 10 * dh + bl) * 1024), o);
        }
      }
    }
  }
  count_done(p, OFF_BC + (u32)ch * 128);
}
DEV void init_part(const Params& p, rsrc_t ws, u32 cb, int k) {
  const int tid = threadIdx.x;
  u32x4 xw[20];
#pragma unroll
  for (int i = 0; i < 20; ++i) {
    const int seg = k + 16 * (i / 5), r = (i % 5) * 512 + tid, tt = r / 1280, ks = (r % 1280) >> 6, ln = r & 63;
    xw[i] = *(const u32x4*)(p.x + (32 * seg + 16 * tt + (ln & 15)) * 640 + 32 * ks + 8 * (ln >> 4));
    if (i == 4) SCHED_FENCE();
  }
  if (tid < 256) {
    const u32 o = (u32)(k * 256 + tid) * 16;
    const u32 L = (o >> 14) * 8192 + (o & 8191);
    u32x4 v = {SENT, SENT, SENT, SENT};
    if (L < 10240) {
      const int vv = L >> 7, t = (L >> 4) & 7;
      const u16* m = p.mem0 + t * 640 + 16 * (vv >> 1) + 2 * (vv & 1);
      for (int q = 0; q < 4; ++q) { u32 x = *(const u32*)(m + 4 * q); v[q] = x < 0xFFFFFFFEu ? x : 0xFFFFFFFEu; }
    }
    st16c(ws, cb + o, 0, v);
  }
#pragma unroll
  for (int i = 0; i < 5; ++i) st16c(ws, (u32)(i * 512 + tid) * 16, OFF_XF + (u32)k * 40960, xw[i]);
  count_done(p, OFF_XFC);
  init_signal(p, true);
#pragma unroll
  for (int i = 5; i < 20; ++i) st16c(ws, (u32)((i % 5) * 512 + tid) * 16, OFF_XF + (u32)(k + 16 * (i / 5)) * 40960, xw[i]);
  count_done(p, OFF_XFC + 64);
}

constexpr float CEXP = 0.08838834764831845f * 1.4426950408889634f;
constexpr float NEGS = -1.0e9f / 0.08838834764831845f;
constexpr int QS = 288;

DEV void read_memf(const unsigned char* memf, int l, u32x4 (&a)[10]) {
#pragma unroll
  for (int ks = 0; ks < 10; ++ks) a[ks] = *(const u32x4*)(memf + ks * 1024 + l * 16);
}
DEV void project2(const u32x4 (&a)[10], const u32x8 (&wt)[2][10], int sidx, unsigned char* dst) {
  f32x4 c0 = {0, 0, 0, 0}, c1 = {0, 0, 0, 0};
#pragma unroll
  for (int ks = 0; ks < 10; ++ks) {
    c0 = smfma(a[ks], wt[0][ks], c0, sidx);
    c1 = smfma(a[ks], wt[1][ks], c1, sidx);
  }
  *(__bf16*)(dst) = (__bf16)(c0[0] + c0[1]);
  *(__bf16*)(dst + QS) = (__bf16)(c0[2] + c0[3]);
  *(__bf16*)(dst + 8) = (__bf16)(c1[0] + c1[1]);
  *(__bf16*)(dst + 8 + QS) = (__bf16)(c1[2] + c1[3]);
}

DEV void score_role(const Params& p, rsrc_t ws, u32 cb, int h, unsigned char* lds) {
  const int tid = threadIdx.x, l = tid & 63, c16 = l & 15, g = l >> 4;
  const int w = __builtin_amdgcn_readfirstlane(tid >> 6);
  unsigned char* memf = lds;
  unsigned char* qs = lds + 10240;
  unsigned char* kms = qs + 8 * QS;
  u32* ctl = (u32*)(lds + 15360);
  unsigned char* kq = lds + 16384;
  u32x8 wt[2][10];
  {
    u32x4 wf[2][20];
    load_qk(p, h, w, l, lds, wf, true);
#pragma unroll
    for (int i = 0; i < 2; ++i)
#pragma unroll
      for (int ks = 0; ks < 10; ++ks) wt[i][ks] = cat(wf[i][2 * ks], wf[i][2 * ks + 1]);
  }
  GSTAMP(1);
  bool dead = false;
  if (w == 0) wait_prog(ws, OFF_INITC, 8, 32, l, dead, 0, 64);
  __syncthreads();
  if (tid < 2) ctl[2 + tid] = 0;
  u32 seen0 = 0, seen1 = 0;
  const u32 kq_src = ((w & 2) ? OFF_QX : OFF_KX) + (u32)(h * 8192 + (w & 1) * 4096);
  if (w < 2) { u32 r = probe(ws, cb, h, w, l); vote(p, w, r, l); if (l == 0) ctl[4 + w] = r; }
  else if (w >= 4) {
    seen0 = wait_prog(ws, OFF_PQK + (u32)h * 128, 1, 2, l, dead, 0);
#pragma unroll
    for (int i = 0; i < 4; ++i) *(u32x4*)(kq + (w - 4) * 4096 + i * 1024 + l * 16) = ld16c(ws, l * 16 + i * 1024, kq_src);
  }
  __syncthreads();
  GSTAMP(2);
  u32 parmask = __builtin_amdgcn_readfirstlane(ctl[4] | (ctl[5] << 1));
  const int sidx = (l & 1) ? 0xEEEE : 0x4444;
  unsigned char* qk_w = qs + (w >> 2) * 8 * QS + 2 * g * QS + (32 * (w & 3) + 8 * (c16 >> 2) + (c16 & 3)) * 2;
  const unsigned char* km_r = kms + (2 * (c16 >> 2) + (c16 & 1)) * QS + g * 16;
  const int wr = (w + h) & 7;
  unsigned char* gA = memf + wr * 1024 + g * 256 + (l & 7) * 32 + ((l >> 3) & 1) * 16;
  unsigned char* gB = memf + 8192 + wr * 256 + ((l >> 2) & 7) * 32 + (l >> 5) * 16 + (l & 3) * 4;
  if (w == 0) {
    const unsigned char* q_r = qs + (c16 & 7) * QS + g * 16;
    const bool hb = c16 >= 8;
    const u32 xo = (u32)(((h >> 1) & 7) * 1024 + g * 256 + (h & 1) * 128 + (c16 & 7) * 16 + (c16 >> 3) * 8);
    const u32 mo = (u32)((c16 >> 3) * 8192 + h * 128 + (c16 & 7) * 16 + g * 4);
    for (int s = 0; s < NSEG; ++s) {
      const u32 sb = cb + (u32)s * STEP_BYTES;
      if (s == 1) parmask = vote_result(ws, l);
      const u32 par = ((parmask >> (s >> 5)) & 1) * 8192;
      const unsigned char* kxs = kq + (s & 1) * 16384;
      __syncthreads();
      { u32 sp = gather_mem(ws, sb + par, wr, gA, gB, l, dead); COUNT(5, sp); if (sp && l == 0) ctl[2 + (s & 1)] = (u32)s; }
      __syncthreads();
      { u32x4 a[10]; read_memf(memf, l, a); SCHED_FENCE(); project2(a, wt, sidx, qk_w); }
      u32x4 kx[8];
#pragma unroll
      for (int i = 0; i < 8; ++i) kx[i] = *(const u32x4*)(kxs + i * 1024 + l * 16);
      __syncthreads();
      if (s == NSEG - 1) break;
      u32x4 qf[4], km[4];
#pragma unroll
      for (int ks = 0; ks < 4; ++ks) { qf[ks] = *(const u32x4*)(q_r + ks * 64); km[ks] = *(const u32x4*)(km_r + ks * 64); }
      SCHED_FENCE();
      f32x4 s0 = {0, 0, 0, 0}, s1 = {0, 0, 0, 0}, s2 = {0, 0, 0, 0};
#pragma unroll
      for (int ks = 0; ks < 4; ++ks) {
        s1 = mfma(kx[ks], qf[ks], s1);
        s2 = mfma(kx[4 + ks], qf[ks], s2);
        s0 = mfma(km[ks], qf[ks], s0);
      }
      float v[4];
#pragma unroll
      for (int r = 0; r < 4; ++r) v[r] = hb ? s2[r] : s1[r];
      float m = fmx(fmx(fmx(v[0], v[1]), v[2]), fmx(fmx(v[3], s0[0]), s0[1]));
      m = red_max(m);
      const float mc = -m * CEXP;
#pragma unroll
      for (int r = 0; r < 4; ++r) v[r] = __builtin_amdgcn_exp2f(fmaf(v[r], CEXP, mc));
      const float e0 = __builtin_amdgcn_exp2f(fmaf(s0[0], CEXP, mc)), e1 = __builtin_amdgcn_exp2f(fmaf(s0[1], CEXP, mc));
      float sum = ((v[0] + v[1]) + (v[2] + v[3])) + (hb ? e1 : e0);
      sum = red_sum(sum);
      const float inv = __builtin_amdgcn_rcpf(sum);
      u32x2 px = {pk2s(v[0] * inv, v[1] * inv), pk2s(v[2] * inv, v[3] * inv)};
      const u32 pm = pk2s(e0 * inv, e1 * inv);
      const u32 xs = sb + (u32)(2 + (h >> 4)) * 16384;
      st8c(ws, xo, xs + par, px);
      st4c(ws, mo, sb + 16384 + 2048, pm);
      st8c(ws, xo, xs + (par ^ 8192), px);
      const u32 tnow = fine_now();
      const u32 rm = __builtin_amdgcn_readfirstlane(ctl[s & 1]), bad = __builtin_amdgcn_readfirstlane(ctl[2 + (s & 1)]);
      const u32 d1 = (s >= 2 && bad != (u32)s) ? tnow - rm : 0u;
      st4c(ws, l < 32 ? (u32)l * 4 : 0x80000000u, OFF_SIDE + (u32)s * SIDE_STEP + (u32)(80 + h) * 128, d1 < 1000u ? d1 : 0u);
      STAMPV(7, d1);
      STAMPV(4, tnow);
    }
  } else if (w < 3) {
    const int tx = 16 * (w - 1) + c16;
    for (int s = 0; s < NSEG; ++s) {
      const unsigned char* kxs = kq + (s & 1) * 16384;
      const u32 sb = cb + (u32)s * STEP_BYTES;
      if (s == 1) parmask = vote_result(ws, l);
      const u32 par = ((parmask >> (s >> 5)) & 1) * 8192;
      __syncthreads();
      { u32 sp = gather_mem(ws, sb + par, wr, gA, gB, l, dead); COUNT(5, sp); if (sp && l == 0) ctl[2 + (s & 1)] = (u32)s; }
      __syncthreads();
      if (w == 1) STAMP(1);
      { u32x4 a[10]; read_memf(memf, l, a); SCHED_FENCE(); project2(a, wt, sidx, qk_w); }
      __syncthreads();
      if (w == 1) STAMP(2);
      if (w == 1) st4c(ws, l == 0 ? 0u : 0x80000000u, OFF_PPX + (u32)h * 128, (u32)s);
      f32x4 s0 = {0, 0, 0, 0}, s1 = {0, 0, 0, 0}, s2 = {0, 0, 0, 0};
#pragma unroll
      for (int ks = 0; ks < 4; ++ks) {
        u32x4 qf = *(const u32x4*)(kxs + 8192 + ((w - 1) * 4 + ks) * 1024 + l * 16);
        s0 = mfma(*(const u32x4*)(km_r + ks * 64), qf, s0);
        s1 = mfma(*(const u32x4*)(kxs + ks * 1024 + l * 16), qf, s1);
        s2 = mfma(*(const u32x4*)(kxs + (4 + ks) * 1024 + l * 16), qf, s2);
      }
      float m = fmx(s0[0], s0[1]);
#pragma unroll
      for (int r = 0; r < 4; ++r) {
        s1[r] = (4 * g + r <= tx) ? s1[r] : NEGS;
        s2[r] = (16 + 4 * g + r <= tx) ? s2[r] : NEGS;
        m = fmx(m, fmx(s1[r], s2[r]));
      }
      m = red_max_g(m);
      const float mc = -m * CEXP;
      const float e0 = __builtin_amdgcn_exp2f(fmaf(s0[0], CEXP, mc)), e1 = __builtin_amdgcn_exp2f(fmaf(s0[1], CEXP, mc));
      float sum = e0 + e1;
#pragma unroll
      for (int r = 0; r < 4; ++r) {
        s1[r] = __builtin_amdgcn_exp2f(fmaf(s1[r], CEXP, mc));
        s2[r] = __builtin_amdgcn_exp2f(fmaf(s2[r], CEXP, mc));
        sum += s1[r] + s2[r];
      }
      sum = red_sum_g(sum);
      const float inv = __builtin_amdgcn_rcpf(sum);
      u32x4 px = {pk2(s1[0] * inv, s1[1] * inv), pk2(s1[2] * inv, s1[3] * inv), pk2(s2[0] * inv, s2[1] * inv), pk2(s2[2] * inv, s2[3] * inv)};
      st16c(ws, l * 16, OFF_PXX + (u32)(((s * 32 + h) * 2 + (w - 1)) * 1024), px);
      st4c(ws, c16 * 16 + g * 4, OFF_PXM + (u32)((((s * 2 + (w - 1)) * 32 + h) * 16) * 16), pk2(e0 * inv, e1 * inv));
      VM_DRAIN();
    }
  } else if (w == 3) {
    u32 Rm = 0, Rp;
    for (int s = 0; s < NSEG; ++s) {
      const u32 sb = cb + (u32)s * STEP_BYTES;
      if (s == 1) parmask = vote_result(ws, l);
      const u32 par = ((parmask >> (s >> 5)) & 1) * 8192;
      if (s >= 2) {
        if (l == 0) { ctl[s & 1] = Rm; if ((int)(Rm - rtime()) < 28) ctl[2 + (s & 1)] = (u32)s; }
        wait_until(Rm);
      }
      __syncthreads();
      { u32 sp = gather_mem(ws, sb + par, wr, gA, gB, l, dead); COUNT(5, sp); if (sp && l == 0) ctl[2 + (s & 1)] = (u32)s; }
      __syncthreads();
      u32x4 a[10];
      read_memf(memf, l, a);
      Side sd = load_side(ws, s, l);
      SCHED_FENCE();
      project2(a, wt, sidx, qk_w);
      __syncthreads();
      STAMPV(0, Rm);
      if (s >= 1 && s < NSEG - 1) schedule(ws, s, l, sd, Rm, Rp, dead);
    }
  } else {
    for (int s = 0; s < NSEG; ++s) {
      const u32 sb = cb + (u32)s * STEP_BYTES;
      if (s == 1) parmask = vote_result(ws, l);
      const u32 par = ((parmask >> (s >> 5)) & 1) * 8192;
      const int sn = s + 1 < NSEG ? s + 1 : s;
      __syncthreads();
      { u32 sp = gather_mem(ws, sb + par, wr, gA, gB, l, dead); COUNT(5, sp); if (sp && l == 0) ctl[2 + (s & 1)] = (u32)s; }
      __syncthreads();
      u32x4 a[10];
      read_memf(memf, l, a);
      SCHED_FENCE();
      u32x4 pk[4];
      f32x4 c0 = {0, 0, 0, 0}, c1 = {0, 0, 0, 0};
#pragma unroll
      for (int ks = 0; ks < 10; ++ks) {
        c0 = smfma(a[ks], wt[0][ks], c0, sidx);
        c1 = smfma(a[ks], wt[1][ks], c1, sidx);
        if (ks == KQPOS) {
          SCHED_FENCE();
#pragma unroll
          for (int i = 0; i < 4; ++i) pk[i] = ld16c(ws, l * 16 + i * 1024, kq_src + (u32)sn * (32 * 8192));
          SCHED_FENCE();
        }
      }
      *(__bf16*)(qk_w) = (__bf16)(c0[0] + c0[1]);
      *(__bf16*)(qk_w + QS) = (__bf16)(c0[2] + c0[3]);
      *(__bf16*)(qk_w + 8) = (__bf16)(c1[0] + c1[1]);
      *(__bf16*)(qk_w + 8 + QS) = (__bf16)(c1[2] + c1[3]);
      __syncthreads();
      unsigned char* kn = kq + ((s + 1) & 1) * 16384 + (w - 4) * 4096 + l * 16;
#pragma unroll
      for (int i = 0; i < 4; ++i) *(u32x4*)(kn + i * 1024) = pk[i];
      const int s2 = s + 2;
      if (s2 < NSEG) {
        if (seg_prod(s2)) { if (seen1 < seg_need(s2)) { __builtin_amdgcn_s_sleep(12); seen1 = wait_prog(ws, OFF_PQK + (u32)h * 128 + 4, 1, seg_need(s2), l, dead, 0); } }
        else { if (seen0 < seg_need(s2)) { __builtin_amdgcn_s_sleep(12); seen0 = wait_prog(ws, OFF_PQK + (u32)h * 128, 1, seg_need(s2), l, dead, 0); } }
      }
    }
  }
  __syncthreads();
  if (tid == 0) st4c(ws, 0u, OFF_PPX + (u32)h * 128, (u32)NSEG);
}

DEV u32x4 finish_mem(f32x4 acc) {
  auto x = __builtin_amdgcn_permlane32_swap(__float_as_uint(acc[0]), __float_as_uint(acc[2]), false, false);
  auto y = __builtin_amdgcn_permlane32_swap(__float_as_uint(acc[1]), __float_as_uint(acc[3]), false, false);
  float t0, t1;
  asm("s_nop 1\n\tv_add_f32_dpp %0, %2, %3 row_ror:8 row_mask:0xf bank_mask:0xf\n\tv_add_f32_dpp %1, %4, %5 row_ror:8 row_mask:0xf bank_mask:0xf"
      : "=&v"(t0), "=&v"(t1) : "v"(x[1]), "v"(x[0]), "v"(y[1]), "v"(y[0]));
  const u32 d = pk2s(t0, t1);
  auto s16 = __builtin_amdgcn_permlane16_swap(d, d, false, false);
  auto sa = __builtin_amdgcn_permlane32_swap(s16[0], s16[0], false, false);
  auto sb = __builtin_amdgcn_permlane32_swap(s16[1], s16[1], false, false);
  return (u32x4){sa[0], sa[1], sb[0], sb[1]};
}

DEV u32 second4(u32 a, u32 b, u32 c, u32 d) {
  const u32 h0 = max(a, b), l0 = min(a, b), h1 = max(c, d), l1 = min(c, d);
  return max(min(h0, h1), h0 > h1 ? l0 : l1);
}
DEV void value_role(const Params& p, rsrc_t ws, u32 cb, int v, unsigned char* lds) {
  const int tid = threadIdx.x, l = tid & 63, c16 = l & 15, g = l >> 4;
  const int w = __builtin_amdgcn_readfirstlane(tid >> 6);
  unsigned char* memf = lds;
  unsigned char* um = lds + 10240;
  u32* ctl = (u32*)(lds + 15360);
  unsigned char* w0s = lds + 16384;
  unsigned char* uxs = lds + 57344;
  const int sidx = (l & 1) ? 0xEEEE : 0x4444;
  const u32 um_w = (u32)((4 * w + (c16 >> 3)) * 128 + (c16 & 7) * 16 + g * 4);
  const int wr = (w + v) & 7;
  unsigned char* gA = memf + wr * 1024 + g * 256 + (l & 7) * 32 + ((l >> 3) & 1) * 16;
  unsigned char* gB = memf + 8192 + wr * 256 + ((l >> 2) & 7) * 32 + (l >> 5) * 16 + (l & 3) * 4;
  bool dead = false;
  b_unit(p, ws, v, lds);
  GSTAMP(4);
  if (w == 0) wait_prog(ws, OFF_BC + (u32)(v / 40) * 128, 1, 64, l, dead, 0);
  if (w == 1) wait_prog(ws, OFF_INITC, 8, 32, l, dead, 0, 64);
  __syncthreads();
  GSTAMP(5);
  if (w == 0) {
#pragma unroll
    for (int i = 0; i < 40; ++i) *(u32x4*)(w0s + i * 1024 + l * 16) = ld16c(ws, l * 16, OFF_BF + (u32)((v * 16 * 20 + i) * 1024));
    __syncthreads();
    GSTAMP(1);
    __syncthreads();
    GSTAMP(2);
    u32 parmask = __builtin_amdgcn_readfirstlane(ctl[4] | (ctl[5] << 1));
    const u32 offX = (u32)((c16 >> 3) * 512 + g * 128 + (c16 & 7) * 16);
    const u32 L = (u32)(v * 128 + (c16 & 7) * 16);
    const u32 pub = (c16 < 8 && !(g & 1)) ? (L >> 13) * 16384 + (u32)(g >> 1) * 8192 + (L & 8191) : 0x80000000u;
    u32 Rg = 0, d3 = 0, Rm = 0, dcur = v == 0 ? ((D1 + D2) << 10) | ((D3 + D4) << 20) : 0u;
    u32 a0 = D1, a1 = D1, a2 = D1, a3 = D1, b0 = D3, b1 = D3, b2 = D3, b3 = D3;
    const bool sidel = (g == 1 && c16 < 8);
    for (int s = 0; s < NSEG; ++s) {
      const u32 sb = cb + (u32)s * STEP_BYTES;
      if (s == 1) parmask = vote_result(ws, l);
      const u32 par = ((parmask >> (s >> 5)) & 1) * 8192;
      if (s >= 1) wait_until(Rg);
      __syncthreads();
      const u32x4 ga = ld16c(ws, (u32)l * 16, sb + par + (u32)wr * 1024);
      const u32 gb = ld4c(ws, (u32)l * 4, sb + par + 16384 + (u32)wr * 256);
      SCHED_FENCE();
      __builtin_amdgcn_s_sleep(1);
      Side sd = load_side(ws, s, l);
      u32 d1s = 0;
      if (v == 0 && s >= 1) d1s = ld4c(ws, (u32)(80 + (l & 31)) * 128, OFF_SIDE + (u32)(s - 1) * SIDE_STEP);
      u32x4 uf[20];
#pragma unroll
      for (int i = 0; i < 16; ++i) uf[i] = *(const u32x4*)(uxs + (((i >> 2) + PROT) & 3) * 4096 + (i & 3) * 1024 + offX);
      SCHED_FENCE();
      *(u32x4*)gA = ga;
      *(u32*)gB = gb;
      if (__builtin_expect(__any(umax3(umax3(ga.x, ga.y, ga.z), ga.w, gb) == SENT), 0)) { u32 sp = gather_mem(ws, sb + par, wr, gA, gB, l, dead); COUNT(5, sp + 1); }
      __syncthreads();
      STAMP(0);
      if (s == NSEG - 1) {
        __syncthreads();
        __syncthreads();
        break;
      }
      u32 so[5];
#pragma unroll
      for (int i = 0; i < 4; ++i) { const u32 qd = (u32)(i + PROT) & 3; so[i] = sb + par + (2 + (qd >> 1)) * 16384 + (qd & 1) * 4096; }
      so[4] = sb + par + 16384 + 2048;
      asm volatile("" : "+s"(so[0]), "+s"(so[1]), "+s"(so[2]), "+s"(so[3]), "+s"(so[4]));
      const u32 pubo = sidel ? OFF_SIDE + (u32)(s + 1) * SIDE_STEP + (u32)(v * 128 + c16 * 16) : pub + sb + STEP_BYTES;
      u32 Rp = 0;
      if (s >= 1) schedule(ws, s, l, sd, Rm, Rp, dead);
      const u32 sy = d3 | dcur;
      const int lead = (int)(Rp - rtime());
      bool ontime = s >= 1 && lead > 0, pfail = false;
      STAMPV(1, Rp);
      STAMP(2);
      SCHED_FENCE();
      if (s >= 1) wait_until(Rp);
      SCHED_FENCE();
      u32x4 pf[20];
#pragma unroll
      for (int i = 0; i < 20; ++i) { pf[i] = ld16c(ws, (u32)l * 16 + (i & 3) * 1024, so[i >> 2]); SCHED_FENCE(); }
      f32x4 acc = {0, 0, 0, 0};
#pragma unroll
      for (int i = 0; i < 20; ++i) {
        if (i == UMPOS) {
          __syncthreads();
#pragma unroll
          for (int q = 0; q < 4; ++q) uf[16 + q] = *(const u32x4*)(um + q * 1024 + l * 16);
          SCHED_FENCE();
        }
        acc = mfma(uf[i], pf[i], acc);
        SCHED_FENCE();
      }
      const u32 tl = rtime();
      u32x4 o = finish_mem(acc);
      if (__builtin_expect(__any(acc[0] != acc[0]), 0)) {
        u32 spins = 0;
#pragma nounroll
        while (!dead) {
          u32 m = 0;
#pragma unroll
          for (int i = 0; i < 20; ++i) m = umax3(umax3(m, pf[i].x, pf[i].y), pf[i].z, pf[i].w);
          if (__all(m != SENT)) break;
          ontime = false; pfail = true;
          if (++spins > SPIN_CAP) dead = true;
          asm volatile("" ::: "memory");
#pragma unroll
          for (int i = 0; i < 20; ++i) pf[i] = ld16c(ws, (u32)l * 16 + (i & 3) * 1024, so[i >> 2]);
        }
        STAMPV(4, spins);
        acc = (f32x4){0, 0, 0, 0};
#pragma unroll
        for (int i = 0; i < 20; ++i) acc = mfma(uf[i], pf[i], acc);
        o = finish_mem(acc);
      }
      u32 sx = 0;
      if ((int)(tl + DLS - Rm) > 0) sx = (tl + DL + 4) | 1u;
      if (__builtin_expect(!ontime, 0)) sx = (rtime() + DL) | 1u;
      if (sx == SENT) sx = 1u;
      o.x = sidel ? sx : o.x;
      o.y = sidel ? sy : o.y;
      st16c(ws, pubo, 0u, o);
      const u32 tp = fine_now();
      __syncthreads();
      d3 = (ontime && lead >= 28) ? tp - Rp : 0u;
      d3 = d3 < 1000u ? d3 : 0u;
      Rg = sx == 0 ? Rm + DV : tp + D4 + DV;
      STAMPV(7, d3);
      if (v == 0) {
        const u32 ma = (u32)wave_max(d1s == SENT ? 0 : (int)d1s), mb = s >= 1 ? (u32)wave_max((int)max(sd.a.y & 1023u, sd.b.y & 1023u)) : 0u;
        if (ma) { a3 = a2; a2 = a1; a1 = a0; a0 = ma; }
        if (pfail && s >= 2) { a0 = a1 = min(second4(a0, a1, a2, a3) + 8, 300u); }
        if (mb) { b3 = b2; b2 = b1; b1 = b0; b0 = mb; }
        dcur = (min(second4(a0, a1, a2, a3) + D2, 1000u) << 10) | (min(second4(b0, b1, b2, b3) + D4, 1000u) << 20);
      }
      STAMP(3);
      STAMPV(6, ((sy >> 10) & 1023u) * 1000 + ((sy >> 20) & 1023u));
    }
  } else {
    u32x8 wb[2][10];
#pragma unroll
    for (int t = 0; t < 2; ++t)
#pragma unroll
      for (int ks = 0; ks < 10; ++ks) {
        u32 o = OFF_BF + (u32)(((v * 16 + 2 * w + t) * 20 + 2 * ks) * 1024);
        wb[t][ks] = cat(ld16c(ws, l * 16, o), ld16c(ws, l * 16 + 1024, o));
      }
    __builtin_amdgcn_s_waitcnt(0x0F70);
    __syncthreads();
    if (w < 3) { u32 r = probe(ws, cb, 32 + v, w - 1, l); vote(p, w - 1, r, l); if (l == 0) ctl[3 + w] = r; }
    const bool pfw = w == 3 || w >= 5;
    const u32 uxo = (u32)((w == 3 ? 0 : w - 4) * 4096 + l * 16);
    u32 seen0 = 0, seen1 = 0;
    if (pfw) {
      seen0 = wait_prog(ws, OFF_PVU, 32, 2, l, dead, 0);
#pragma unroll
      for (int i = 0; i < 4; ++i) *(u32x4*)(uxs + uxo + i * 1024) = ld16c(ws, uxo + i * 1024, OFF_UX + (u32)(v * 16384));
    }
    __syncthreads();
    u32 parmask = __builtin_amdgcn_readfirstlane(ctl[4] | (ctl[5] << 1));
    if (w == 4) {
      for (int s = 0; s < NSEG; ++s) {
        const u32 sb = cb + (u32)s * STEP_BYTES;
        if (s == 1) parmask = vote_result(ws, l);
      const u32 par = ((parmask >> (s >> 5)) & 1) * 8192;
        u32x8 sb0[2], sb1[2];
        sb0[0] = cat(*(const u32x4*)(w0s + l * 16), *(const u32x4*)(w0s + 1024 + l * 16));
        sb1[0] = cat(*(const u32x4*)(w0s + 20480 + l * 16), *(const u32x4*)(w0s + 21504 + l * 16));
        __syncthreads();
        { u32 sp = gather_mem(ws, sb + par, wr, gA, gB, l, dead); COUNT(5, sp); }
        __syncthreads();
        u32x4 a[3];
        a[0] = *(const u32x4*)(memf + l * 16);
        a[1] = *(const u32x4*)(memf + 1024 + l * 16);
        f32x4 a0 = {0, 0, 0, 0}, a1 = {0, 0, 0, 0}, a2 = {0, 0, 0, 0}, a3 = {0, 0, 0, 0};
#pragma unroll
        for (int ks = 0; ks < 10; ++ks) {
          if (ks + 2 < 10) a[(ks + 2) % 3] = *(const u32x4*)(memf + (ks + 2) * 1024 + l * 16);
          if (ks + 1 < 10) {
            const int kn = ks + 1;
            sb0[kn & 1] = cat(*(const u32x4*)(w0s + (2 * kn) * 1024 + l * 16), *(const u32x4*)(w0s + (2 * kn + 1) * 1024 + l * 16));
            sb1[kn & 1] = cat(*(const u32x4*)(w0s + (20 + 2 * kn) * 1024 + l * 16), *(const u32x4*)(w0s + (21 + 2 * kn) * 1024 + l * 16));
          }
          SCHED_FENCE();
          a0 = smfma(a[ks % 3], wb[0][ks], a0, sidx);
          a1 = smfma(a[ks % 3], wb[1][ks], a1, sidx);
          a2 = smfma(a[ks % 3], sb0[ks & 1], a2, sidx);
          a3 = smfma(a[ks % 3], sb1[ks & 1], a3, sidx);
          SCHED_FENCE();
        }
        *(u32*)(um + um_w) = pk2(a0[0] + a0[1], a0[2] + a0[3]);
        *(u32*)(um + um_w + 256) = pk2(a1[0] + a1[1], a1[2] + a1[3]);
        *(u32*)(um + um_w - 2048) = pk2(a2[0] + a2[1], a2[2] + a2[3]);
        *(u32*)(um + um_w - 2048 + 256) = pk2(a3[0] + a3[1], a3[2] + a3[3]);
        __syncthreads();
        __syncthreads();
      }
    } else {
      for (int s = 0; s < NSEG; ++s) {
        const u32 sb = cb + (u32)s * STEP_BYTES;
        if (s == 1) parmask = vote_result(ws, l);
      const u32 par = ((parmask >> (s >> 5)) & 1) * 8192;
        __syncthreads();
        { u32 sp = gather_mem(ws, sb + par, wr, gA, gB, l, dead); COUNT(5, sp); }
        __syncthreads();
        u32x4 a[10];
        read_memf(memf, l, a);
        u32x4 pu[4];
        if (pfw && s + 1 < NSEG) {
#pragma unroll
          for (int i = 0; i < 4; ++i) pu[i] = ld16c(ws, uxo + i * 1024, OFF_UX + (u32)(((s + 1) * 80 + v) * 16384));
        }
        SCHED_FENCE();
        f32x4 a0 = {0, 0, 0, 0}, a1 = {0, 0, 0, 0};
#pragma unroll
        for (int ks = 0; ks < 10; ++ks) {
          a0 = smfma(a[ks], wb[0][ks], a0, sidx);
          a1 = smfma(a[ks], wb[1][ks], a1, sidx);
        }
        *(u32*)(um + um_w) = pk2(a0[0] + a0[1], a0[2] + a0[3]);
        *(u32*)(um + um_w + 256) = pk2(a1[0] + a1[1], a1[2] + a1[3]);
        if (w == 1) st4c(ws, l == 0 ? 0u : 0x80000000u, OFF_PUM + (u32)v * 128, (u32)s);
        __syncthreads();
        __syncthreads();
        if (pfw && s + 1 < NSEG) {
#pragma unroll
          for (int i = 0; i < 4; ++i) *(u32x4*)(uxs + uxo + i * 1024) = pu[i];
          const int sn = s + 2;
          if (sn < NSEG) {
            if (seg_prod(sn)) { if (seen1 < seg_need(sn)) seen1 = wait_prog(ws, OFF_PVU + 128, 32, seg_need(sn), l, dead, 0); }
            else { if (seen0 < seg_need(sn)) seen0 = wait_prog(ws, OFF_PVU, 32, seg_need(sn), l, dead, 0); }
          }
        }
        if (w == 1 || w == 2) {
#pragma unroll
          for (int i = 0; i < 2; ++i) {
            u32 o = (u32)(((w - 1) * 2 + i) * 1024 + l * 16);
            st16c(ws, o, OFF_UM + (u32)((s * 80 + v) * 4096), *(const u32x4*)(um + o));
          }
          VM_DRAIN();
        }
      }
    }
  }
  __syncthreads();
  if (tid == 0) st4c(ws, 0u, OFF_PUM + (u32)v * 128, (u32)NSEG);
}

DEV void glds16(const void* gsrc, u32 lds_dst) {
  u32 keep;
  asm volatile("s_mov_b32 %0, m0\n\ts_mov_b32 m0, %2\n\ts_nop 0\n\tglobal_load_lds_dwordx4 %1, off sc1\n\ts_mov_b32 m0, %0"
               : "=&s"(keep) : "v"(gsrc), "s"(lds_dst) : "memory");
}
template <int NST, bool ROWS, class Off, class Body, class Pub>
DEV void stream_tiles(const unsigned char* src, Off off, int n, int nbody, unsigned char* lds, Body body, Pub pub) {
  const int l = threadIdx.x & 63, w = __builtin_amdgcn_readfirstlane(threadIdx.x >> 6);
  const u32 lbase = (u32)(uintptr_t)lds + (u32)w * 5120;
  constexpr int QS_ = ROWS ? 64 : 1024;
  const unsigned char* g0 = ROWS ? src + (16 * (w >> 2) + (l & 15)) * 1280 + (w & 3) * 320 + (l >> 4) * 16 : src + w * 5120 + l * 16;
#pragma unroll
  for (int q = 0; q < 5; ++q) glds16(g0 + off(0) + q * QS_, lbase + q * 1024);
#pragma nounroll
  for (int i = 0; i < nbody; ++i) {
    if (i <= 3) VM_DRAIN(); else asm volatile("s_waitcnt vmcnt(%0)" :: "n"(NST) : "memory");
    RAW_BARRIER();
    if (i >= 1) pub(i <= 3 ? i : i - 1);
    const unsigned char* g = g0 + off(i + 1 < n ? i + 1 : n - 1);
    const u32 lb = lbase + (u32)((i + 1) & 1) * 40960;
    body(i, lds + (i & 1) * 40960, [&](int q) { glds16(g + q * QS_, lb + q * 1024); });
  }
  VM_DRAIN();
  RAW_BARRIER();
  pub(nbody);
}
template <int W, class Dma, class Use>
DEV void sweep40(const unsigned char* t, Dma dma, Use use) {
  u32x4 xb[W];
#pragma unroll
  for (int i = 0; i < W; ++i) xb[i] = *(const u32x4*)(t + ((i & 1) * 20 + (i >> 1)) * 1024);
  SCHED_FENCE();
#pragma unroll
  for (int i = 0; i < 40; ++i) {
    use(i, xb[i % W]);
    if (i + W < 40) xb[i % W] = *(const u32x4*)(t + (((i + W) & 1) * 20 + ((i + W) >> 1)) * 1024);
    if (i % 2 == 1 && i < 10) dma(i >> 1);
    SCHED_FENCE();
  }
}

DEV void qk_role(const Params& p, rsrc_t ws, int h, int par, unsigned char* lds, bool sig) {
  const int tid = threadIdx.x, l = tid & 63;
  const int w = __builtin_amdgcn_readfirstlane(tid >> 6);
  const int mat = w >> 2;
  u32x4 wt[2][20];
  load_qk(p, h, w, l, lds, wt, sig);
  GSTAMP(1);
  const u32 dst = (mat ? OFF_KX : OFF_QX) + (u32)((h * 8 + (w & 3)) * 1024);
  const int n = par ? NHELP : NSEG - NHELP;
  wait_init(ws);
  stream_tiles<2, false>(p.ws + OFF_XF, [&](int i) { return tile_seg(par, i) * 40960; }, n, n, lds,
    [&](int i, const unsigned char* t, auto dma) {
      f32x4 acc[2][2] = {};
      sweep40<8>(t + l * 16, dma, [&](int f, u32x4 xb) {
        acc[0][f & 1] = mfma(wt[0][f >> 1], xb, acc[0][f & 1]);
        acc[1][f & 1] = mfma(wt[1][f >> 1], xb, acc[1][f & 1]);
      });
#pragma unroll
      for (int tt = 0; tt < 2; ++tt) {
        u32x4 o = {pk2(acc[0][tt][0], acc[0][tt][1]), pk2(acc[0][tt][2], acc[0][tt][3]), pk2(acc[1][tt][0], acc[1][tt][1]), pk2(acc[1][tt][2], acc[1][tt][3])};
        st16c(ws, l * 16 + tt * 4096, dst + (u32)tile_seg(par, i) * (32 * 8192), o);
      }
    },
    [&](int k) {
      if (w == 0) {
        if (k == 2) { bool dead = false; wait_prog(ws, OFF_XFC + 64, 1, 16, l, dead, 0); }
        st4c(ws, l == 0 ? 0u : 0x80000000u, OFF_PQK + (u32)(h * 128 + par * 4), (u32)k);
      }
    });
}

DEV void vu_role(const Params& p, rsrc_t ws, int h, int par, unsigned char* lds, bool sig) {
  const int tid = threadIdx.x, l = tid & 63, c16 = l & 15, g = l >> 4;
  const int w = __builtin_amdgcn_readfirstlane(tid >> 6);
  u32x4 wv[20], wo[5][4];
  load_vu(p, h, w, l, lds, wv, wo, sig);
  GSTAMP(1);
  unsigned char* vs = lds + 81920;
  const u32 uoff = (u32)(((c16 >> 1) & 1) * 16384 + h * 512 + g * 128 + (2 * (c16 >> 2) + (c16 & 1)) * 16);
  const int n = par ? NHELP : NSEG - NHELP;
  wait_init(ws);
  stream_tiles<5, false>(p.ws + OFF_XF, [&](int i) { return tile_seg(par, i) * 40960; }, n, n + 1, lds,
    [&](int i, const unsigned char* t, auto dma) {
      u32x4 vf[2][4];
#pragma unroll
      for (int tt = 0; tt < 2; ++tt)
#pragma unroll
        for (int ks = 0; ks < 4; ++ks) vf[tt][ks] = *(const u32x4*)(vs + ((i + 1) & 1) * 8192 + ((tt * 4 + ks) * 64 + l) * 16);
      const u32 uo = i ? uoff + OFF_UX + (u32)((tile_seg(par, i - 1) * 80 + 10 * w) * 16384) : 0x80000000u;
      f32x4 av[2] = {}, cu[2] = {};
      u32x4 ou[2];
      sweep40<6>(t + l * 16, dma, [&](int f, u32x4 xb) {
        av[f & 1] = mfma(wv[f >> 1], xb, av[f & 1]);
        cu[f & 1] = mfma(vf[f & 1][(f >> 1) & 3], wo[f >> 3][(f >> 1) & 3], cu[f & 1]);
        if ((f & 7) == 7) {
          const int ct = f >> 3;
          u32x4 o = {pk2(cu[0][0], cu[0][1]), pk2(cu[0][2], cu[0][3]), pk2(cu[1][0], cu[1][1]), pk2(cu[1][2], cu[1][3])};
          cu[0] = (f32x4){0, 0, 0, 0}; cu[1] = (f32x4){0, 0, 0, 0};
          if (ct == 0) ou[0] = o;
          else { if (ct == 1) st16c(ws, uo, 0u, ou[0]); st16c(ws, uo, (u32)ct * 32768, o); }
        }
      });
#pragma unroll
      for (int tt = 0; tt < 2; ++tt) {
        u32x2 o = {pk2(av[tt][0], av[tt][1]), pk2(av[tt][2], av[tt][3])};
        *(u32x2*)(vs + (i & 1) * 8192 + ((tt * 4 + (w >> 1)) * 64 + l) * 16 + (w & 1) * 8) = o;
      }
    },
    [&](int k) {
      if (w == 0 && k >= 2) {
        st4c(ws, l == 0 ? 0u : 0x80000000u, OFF_PVU + (u32)(par * 128 + h * 4), (u32)(k - 1));
        if (k == 3) { bool dead = false; wait_prog(ws, OFF_XFC + 64, 1, 16, l, dead, 0); }
      }
    });
}

DEV f32x2 cmul(f32x2 a, f32x2 b) { return (f32x2){a.x * b.x - a.y * b.y, a.x * b.y + a.y * b.x}; }
DEV f32x2 cmulc(f32x2 a, f32x2 b) { return (f32x2){a.x * b.x + a.y * b.y, a.y * b.x - a.x * b.y}; }
template <bool INV> DEV f32x2 rot90(f32x2 a) { return INV ? (f32x2){-a.y, a.x} : (f32x2){a.y, -a.x}; }
template <bool INV> DEV void fft4(f32x2& a, f32x2& b, f32x2& c, f32x2& d) {
  f32x2 s0 = a + c, d0 = a - c, s1 = b + d, d1 = rot90<INV>(b - d);
  a = s0 + s1; b = d0 + d1; c = s0 - s1; d = d0 - d1;
}
template <bool INV> DEV f32x2 twc(f32x2 a, float c, float s) {
  return INV ? (f32x2){a.x * c - a.y * s, a.y * c + a.x * s} : (f32x2){a.x * c + a.y * s, a.y * c - a.x * s};
}
template <bool INV> DEV void fft16(f32x2 (&v)[16]) {
  constexpr float C1 = 0.92387953251128674f, S1 = 0.38268343236508977f, R = 0.70710678118654752f;
#pragma unroll
  for (int i = 0; i < 4; ++i) fft4<INV>(v[i], v[i + 4], v[i + 8], v[i + 12]);
  v[5] = twc<INV>(v[5], C1, S1);  v[9] = twc<INV>(v[9], R, R);     v[13] = twc<INV>(v[13], S1, C1);
  v[6] = twc<INV>(v[6], R, R);    v[10] = rot90<INV>(v[10]);       v[14] = twc<INV>(v[14], -R, R);
  v[7] = twc<INV>(v[7], S1, C1);  v[11] = twc<INV>(v[11], -R, R);  v[15] = twc<INV>(v[15], -C1, -S1);
#pragma unroll
  for (int k1 = 0; k1 < 4; ++k1) fft4<INV>(v[4 * k1], v[4 * k1 + 1], v[4 * k1 + 2], v[4 * k1 + 3]);
  f32x2 t;
#define SWP(a, b) t = v[a]; v[a] = v[b]; v[b] = t;
  SWP(1, 4) SWP(2, 8) SWP(3, 12) SWP(6, 9) SWP(7, 13) SWP(11, 14)
#undef SWP
}
template <bool INV> DEV void fft8(f32x2 (&v)[8]) {
  constexpr float R = 0.70710678118654752f;
  fft4<INV>(v[0], v[2], v[4], v[6]);
  fft4<INV>(v[1], v[3], v[5], v[7]);
  f32x2 o1 = twc<INV>(v[3], R, R), o2 = rot90<INV>(v[5]), o3 = twc<INV>(v[7], -R, R), o0 = v[1];
  f32x2 e0 = v[0], e1 = v[2], e2 = v[4], e3 = v[6];
  v[0] = e0 + o0; v[4] = e0 - o0; v[1] = e1 + o1; v[5] = e1 - o1; v[2] = e2 + o2; v[6] = e2 - o2; v[3] = e3 + o3; v[7] = e3 - o3;
}

DEV void fft_cols(const Params& p, rsrc_t ws, int q, unsigned char* lds) {
  const int tid = threadIdx.x, c = tid >> 7, t = tid & 127;
  f32x2* tw = (f32x2*)lds;
  f32x2* buf = (f32x2*)(lds + 16384) + c * (16 * 136);
  f32x2* fin = (f32x2*)(lds + 16384);
  __syncthreads();
  for (int m = tid; m < 2048; m += 512) { float sn, cs; sincospif((float)m * (1.0f / 1024.0f), &sn, &cs); tw[m] = (f32x2){cs, -sn}; }
  f32x2 keep[16];
  const int npass = (q == 0) ? 2 : 1;
  for (int pass = npass - 1; pass >= 0; --pass) {
    f32x2 v[16];
#pragma unroll
    for (int a = 0; a < 16; ++a)
      v[a] = __builtin_bit_cast(f32x2, ld8c(ws, (u32)(t * 16 + (c & 1) * 8 + (c >> 1) * 32768), OFF_FX + (u32)(2 * q * 32768 + a * 2048)));
    if (q == 0 && c == 0) {
#pragma unroll
      for (int a = 0; a < 16; ++a) v[a] = (f32x2){pass ? v[a].y : v[a].x, 0.f};
    }
    u32 wreg[16];
#pragma unroll
    for (int i = 0; i < 2; ++i)
#pragma unroll
      for (int kc = 0; kc < 8; ++kc) {
        const int pp = t + 128 * i, k = (pp >> 4) + 16 * (pp & 15) + 256 * kc;
        wreg[i * 8 + kc] = ld4c(ws, (u32)(k * 16 + c * 4), OFF_WT + (u32)(pass ? 80 : q) * 32768);
      }
    __syncthreads();
    fft16<false>(v);
#pragma unroll
    for (int ka = 0; ka < 16; ++ka) buf[ka * 136 + t] = ka ? cmul(v[ka], tw[t * ka]) : v[ka];
    __syncthreads();
    const int ka2 = t >> 3, c2 = t & 7;
#pragma unroll
    for (int b = 0; b < 16; ++b) v[b] = buf[ka2 * 136 + 8 * b + c2];
    fft16<false>(v);
#pragma unroll
    for (int kb = 0; kb < 16; ++kb) buf[ka2 * 136 + 8 * kb + c2] = kb ? cmul(v[kb], tw[16 * c2 * kb]) : v[kb];
    __syncthreads();
#pragma unroll
    for (int i = 0; i < 2; ++i) {
      const int pp = t + 128 * i;
      f32x2* e = buf + (pp >> 4) * 136 + 8 * (pp & 15);
      f32x2 u[8];
#pragma unroll
      for (int j = 0; j < 8; ++j) u[j] = e[j];
      fft8<false>(u);
#pragma unroll
      for (int kc = 0; kc < 8; ++kc) {
        const u32 wv = wreg[i * 8 + kc];
        const f32x2 wf = {__uint_as_float(wv << 16) * (1.0f / (2048.0f * 640.0f)), __uint_as_float(wv & 0xFFFF0000u) * (1.0f / (2048.0f * 640.0f))};
        u[kc] = cmul(u[kc], wf);
      }
      fft8<true>(u);
#pragma unroll
      for (int j = 0; j < 8; ++j) e[j] = u[j];
    }
    __syncthreads();
#pragma unroll
    for (int kb = 0; kb < 16; ++kb) { f32x2 x = buf[ka2 * 136 + 8 * kb + c2]; v[kb] = kb ? cmulc(x, tw[16 * c2 * kb]) : x; }
    fft16<true>(v);
#pragma unroll
    for (int b = 0; b < 16; ++b) buf[ka2 * 136 + 8 * b + c2] = v[b];
    __syncthreads();
#pragma unroll
    for (int ka = 0; ka < 16; ++ka) { f32x2 x = buf[ka * 136 + t]; v[ka] = ka ? cmulc(x, tw[t * ka]) : x; }
    fft16<true>(v);
    if (pass == 1) {
#pragma unroll
      for (int a = 0; a < 16; ++a) keep[a] = v[a];
    } else {
      if (q == 0 && c == 0) {
#pragma unroll
        for (int a = 0; a < 16; ++a) v[a].y = keep[a].x;
      }
      __syncthreads();
#pragma unroll
      for (int a = 0; a < 16; ++a) fin[(128 * a + t) * 4 + c] = v[a];
      __syncthreads();
    }
  }
#pragma unroll
  for (int i = 0; i < 4; ++i) {
    const int n = tid + 512 * i;
    const f32x4 x0 = *(const f32x4*)(fin + n * 4), x1 = *(const f32x4*)(fin + n * 4 + 2);
    u32x4 hi = {pk2(x0[0], x0[1]), pk2(x0[2], x0[3]), pk2(x1[0], x1[1]), pk2(x1[2], x1[3])};
    u32x4 lo = {pk2(x0[0] - __uint_as_float(hi[0] << 16), x0[1] - __uint_as_float(hi[0] & 0xFFFF0000u)),
                pk2(x0[2] - __uint_as_float(hi[1] << 16), x0[3] - __uint_as_float(hi[1] & 0xFFFF0000u)),
                pk2(x1[0] - __uint_as_float(hi[2] << 16), x1[1] - __uint_as_float(hi[2] & 0xFFFF0000u)),
                pk2(x1[2] - __uint_as_float(hi[3] << 16), x1[3] - __uint_as_float(hi[3] & 0xFFFF0000u))};
    const u32 o = (u32)((n >> 4) * 40960 + (q >> 2) * 1024 + ((q & 3) * 16 + (n & 15)) * 16);
    st16c(ws, o, OFF_FY, hi);
    st16c(ws, o + 20480, OFF_FY, lo);
  }
}

DEV void fw_task(const Params& p, rsrc_t ws, int k) {
  for (int q = k * 2560 + threadIdx.x; q < (k + 1) * 2560; q += 512) {
    const int which = q / 51200, r = q % 51200, tile = r / 1280, ks = (r % 1280) >> 6, ln = r & 63;
    u32x4 hi, lo;
    float val[8];
#pragma unroll
    for (int j = 0; j < 8; ++j) {
      const int aa = 16 * tile + (ln & 15), bb = 32 * ks + 8 * (ln >> 4) + j;
      const int kp = which ? bb : aa, d = which ? aa : bb;
      const int kk = (kp == 1) ? 320 : (kp >> 1);
      float sn, cs;
      sincospif((float)((kk * d) % 640) * (1.0f / 320.0f), &sn, &cs);
      float x = (kp & 1) && kp != 1 ? -sn : cs;
      if (which && kp > 1) x *= 2.0f;
      val[j] = x;
    }
#pragma unroll
    for (int j = 0; j < 4; ++j) { hi[j] = pk2(val[2 * j], val[2 * j + 1]); lo[j] = pk2lo(val[2 * j], val[2 * j + 1], hi[j]); }
    const u32 o = (which ? OFF_FW3 : OFF_FW1) + (u32)((tile * 40 + ks) * 1024 + ln * 16);
    st16c(ws, o, 0, hi);
    st16c(ws, o + 20480, 0, lo);
  }
  VM_DRAIN();
  __syncthreads();
  if (threadIdx.x == 0) __hip_atomic_fetch_add((u32*)(p.ws + OFF_FWC), 1u, __ATOMIC_RELAXED, __HIP_MEMORY_SCOPE_AGENT);
}
DEV void wt_task(const Params& p, rsrc_t ws, int k, unsigned char* lds) {
  const int tid = threadIdx.x;
  const rsrc_t rr = mk_rsrc(p.wr, 2048 * 642), ri = mk_rsrc(p.wi, 2048 * 642);
  for (int blk = 0; blk < 2; ++blk) {
    const int row0 = k * 128 + blk * 64;
    __syncthreads();
#pragma unroll
    for (int i = 0; i < 6; ++i) {
      const int c = tid + 512 * i;
      if (c < 2568) {
        *(u32x4*)(lds + c * 16) = ld16(rr, (u32)c * 16, (u32)row0 * 642);
        *(u32x4*)(lds + 41088 + c * 16) = ld16(ri, (u32)c * 16, (u32)row0 * 642);
      }
    }
    __syncthreads();
    const int r = tid & 63;
    const u16* a = (const u16*)lds + r * 321;
    for (int q = tid >> 6; q < 81; q += 8) {
      u32x4 o;
#pragma unroll
      for (int j = 0; j < 4; ++j) { const int col = q < 80 ? 4 * q + j : 320; o[j] = (u32)a[col] | ((u32)a[20544 + col] << 16); }
      st16c(ws, (u32)(row0 + r) * 16, OFF_WT + (u32)q * 32768, o);
    }
  }
  VM_DRAIN();
  __syncthreads();
  if (tid == 0) __hip_atomic_fetch_add((u32*)(p.ws + OFF_FWC), 1u, __ATOMIC_RELAXED, __HIP_MEMORY_SCOPE_AGENT);
}
DEV void wait_fw(rsrc_t ws) {
  if (threadIdx.x < 64) { bool dead = false; wait_prog(ws, OFF_FWC, 1, 56, threadIdx.x, dead, 1); }
  __syncthreads();
}
DEV void f1_task(const Params& p, rsrc_t ws, int type, int rg, unsigned char* lds) {
  const int tid = threadIdx.x, l = tid & 63, c16 = l & 15, g = l >> 4;
  const int w = __builtin_amdgcn_readfirstlane(tid >> 6);
  const int tile = type * 8 + w;
  wait_fw(ws);
  u32x4 wt[2][20];
#pragma unroll
  for (int i = 0; i < 2; ++i)
#pragma unroll
    for (int ks = 0; ks < 20; ++ks) { wt[i][ks] = ld16c(ws, l * 16, OFF_FW1 + (u32)((tile * 40 + i * 20 + ks) * 1024)); THROTTLE(ks); }
  __builtin_amdgcn_s_waitcnt(0x0F70);
  stream_tiles<2, true>((const unsigned char*)p.ax + rg * 4 * 40960, [](int i) { return i * 40960; }, 4, 4, lds,
    [&](int i, const unsigned char* t, auto dma) {
      f32x4 acc[2][2] = {};
      sweep40<8>(t + l * 16, dma, [&](int f, u32x4 xb) {
        acc[0][f & 1] = mfma(wt[0][f >> 1], xb, acc[0][f & 1]);
        acc[1][f & 1] = mfma(wt[1][f >> 1], xb, acc[1][f & 1]);
      });
#pragma unroll
      for (int tt = 0; tt < 2; ++tt) {
        f32x4 o = acc[0][tt] + acc[1][tt];
        st16c(ws, (u32)(g * 32768 + c16 * 16 + tt * 256), OFF_FX + (u32)(tile * 4 * 32768 + (rg * 4 + i) * 512), __builtin_bit_cast(u32x4, o));
      }
    },
    [&](int k) { if (k == 4 && tid == 0) __hip_atomic_fetch_add((u32*)(p.ws + OFF_F1C + type * 128), 1u, __ATOMIC_RELAXED, __HIP_MEMORY_SCOPE_AGENT); });
}
DEV void f3_task(const Params& p, rsrc_t ws, int type, int rg, unsigned char* lds) {
  const int tid = threadIdx.x, l = tid & 63, c16 = l & 15, g = l >> 4;
  const int w = __builtin_amdgcn_readfirstlane(tid >> 6);
  const int tile = type * 8 + w;
  wait_fw(ws);
  u32x4 wt[2][20];
#pragma unroll
  for (int i = 0; i < 2; ++i)
#pragma unroll
    for (int ks = 0; ks < 20; ++ks) { wt[i][ks] = ld16c(ws, l * 16, OFF_FW3 + (u32)((tile * 40 + i * 20 + ks) * 1024)); THROTTLE(ks); }
  __builtin_amdgcn_s_waitcnt(0x0F70);
  if (w == 0) {
    bool dead = false;
    wait_prog(ws, OFF_F2C, 1, 80, l, dead, 1);
    __builtin_amdgcn_fence(__ATOMIC_ACQUIRE, "agent");
    VM_DRAIN();
  }
  __syncthreads();
  u16* outp = p.out + (rg * 128 + c16) * 1280 + tile * 16 + 4 * g;
  stream_tiles<1, false>(p.ws + OFF_FY + rg * 8 * 40960, [](int i) { return i * 40960; }, 8, 8, lds,
    [&](int i, const unsigned char* t, auto dma) {
      f32x4 a0 = {0, 0, 0, 0}, a1 = {0, 0, 0, 0}, a2 = {0, 0, 0, 0};
      sweep40<10>(t + l * 16, dma, [&](int f, u32x4 y) {
        if (f & 1) a2 = mfma(wt[0][f >> 1], y, a2);
        else { a0 = mfma(wt[0][f >> 1], y, a0); a1 = mfma(wt[1][f >> 1], y, a1); }
      });
      f32x4 o = a0 + (a1 + a2);
      u32x2 ov = {pk2(o[0], o[1]), pk2(o[2], o[3])};
      *(u32x2*)(outp + i * 16 * 1280) = ov;
    },
    [&](int) {});
}

DEV void out_task(const Params& p, rsrc_t ws, int s, int cg, unsigned char* lds) {
  const int tid = threadIdx.x, l = tid & 63, c16 = l & 15, g = l >> 4;
  const int w = __builtin_amdgcn_readfirstlane(tid >> 6);
  const int G = 8 * cg + w;
  const int pp = (c16 >> 1) & 1, j = 2 * (c16 >> 2) + (c16 & 1);
  const u32 ox = (u32)(pp * 16384 + g * 128 + j * 16), om = (u32)(pp * 4096 + g * 128 + j * 16);
  bool dead = false;
  if (w == 0) wait_prog(ws, OFF_PVU + (u32)seg_prod(s) * 128, 32, seg_need(s), l, dead, 1);
  __syncthreads();
  u32x4 ua[32];
#pragma unroll
  for (int h = 0; h < 32; ++h) { ua[h] = ld16c(ws, ox + (h & 7) * 512, OFF_UX + (u32)((s * 80 + 2 * G) * 16384 + (h >> 3) * 4096)); THROTTLE(h); }
  SCHED_FENCE();
  if (w < 2) {
    wait_prog(ws, OFF_PPX + (u32)(cg + 5 * (s & 3)) * 128, 1, (u32)s, l, dead, 2);
    if (w == 0) wait_prog(ws, OFF_PPX, 32, (u32)s + 1, l, dead, 1, 128);
    else wait_prog(ws, OFF_PUM + (u32)cg * 2048, 16, (u32)s + 1, l, dead, 1, 128);
  }
  __syncthreads();
  u32x4 pr[10], ub[8];
#pragma unroll
  for (int i = 0; i < 8; ++i) pr[i] = ld16c(ws, tid * 16, OFF_PXX + (u32)(s * 65536 + i * 8192));
#pragma unroll
  for (int i = 0; i < 2; ++i) pr[8 + i] = ld16c(ws, tid * 16, OFF_PXM + (u32)(s * 16384 + i * 8192));
#pragma unroll
  for (int q = 0; q < 8; ++q) ub[q] = ld16c(ws, om + q * 512, OFF_UM + (u32)((s * 80 + 2 * G) * 4096));
#pragma unroll
  for (int i = 0; i < 10; ++i) *(u32x4*)(lds + (i * 512 + tid) * 16) = pr[i];
  __syncthreads();
  f32x4 acc[2] = {};
  {
    const unsigned char* px = lds + l * 16;
    const unsigned char* pm = lds + 65536 + (g * 16 + c16) * 16;
    auto rd = [&](int i) { return i < 64 ? *(const u32x4*)(px + i * 1024) : *(const u32x4*)(pm + (i & 1) * 8192 + ((i - 64) >> 1) * 1024); };
    u32x4 pb[12];
#pragma unroll
    for (int i = 0; i < 12; ++i) pb[i] = rd(i);
    SCHED_FENCE();
#pragma unroll
    for (int i = 0; i < 80; ++i) {
      acc[i & 1] = mfma(i < 64 ? ua[i >> 1] : ub[(i - 64) >> 1], pb[i % 12], acc[i & 1]);
      if (i + 12 < 80) pb[i % 12] = rd(i + 12);
      SCHED_FENCE();
    }
  }
#pragma unroll
  for (int tt = 0; tt < 2; ++tt) {
    u32x2 o = {pk2(acc[tt][0], acc[tt][1]), pk2(acc[tt][2], acc[tt][3])};
    *(u32x2*)(p.out + (32 * s + 16 * tt + c16) * 1280 + 640 + 16 * G + 4 * g) = o;
  }
}

DEV void prefetch_role(rsrc_t ws, int x, int k) {
  const int l = threadIdx.x & 63;
  const int w = __builtin_amdgcn_readfirstlane(threadIdx.x >> 6);
  if (w >= PFW) return;
  bool dead = false;
  u32 seen0 = 0, seen1 = 0;
  for (int s = 1; s < NSEG; ++s) {
    const u32 prog = wait_prog(ws, OFF_PUM + (u32)(x * 16 + k) * 128, 1, (u32)(s > PFD ? s - PFD : 0), l, dead, 1);
    if ((u32)s <= prog + 1) { s = (int)prog + 1; continue; }
    const u32 need = seg_need(s);
    if (seg_prod(s)) { if (seen1 < need) seen1 = wait_prog(ws, OFF_PVU + 128, 32, need, l, dead, 1); }
    else { if (seen0 < need) seen0 = wait_prog(ws, OFF_PVU, 32, need, l, dead, 1); }
#pragma nounroll
    for (int i = w; i < 14 * (8192 / (64 * PFSTR)); i += PFW) {
      const int u = k * 14 + i / (8192 / (64 * PFSTR));
      u32 o;
      if (u >= 40) o = ((u & 1) ? OFF_QX : OFF_KX) + (u32)((s * 32 + x * 8 + ((u - 40) >> 1)) * 8192);
      else o = OFF_UX + (u32)((s * 80 + x * 20) * 16384 + u * 8192);
      if (false) {}
      asm volatile("buffer_load_dword v255, %0, %1, %2 offen sc1\n\ts_waitcnt vmcnt(%3)"
                   :: "v"(l * PFSTR), "s"(ws), "s"(o + (u32)(i % (8192 / (64 * PFSTR))) * (64 * PFSTR)), "n"(PFQ) : "memory", "v255");
      __builtin_amdgcn_s_sleep(PFS);
    }
  }
  VM_DRAIN();
}

constexpr int T_SET = 56, T_F1 = 80, T_F2 = 80, T_F3 = 80, T_OUT = 320;
DEV int next_ticket(const Params& p, unsigned char* lds, u32 off) {
  u32* tk = (u32*)(lds + LDS_MAIN - 16);
  __syncthreads();
  if (threadIdx.x == 0) *tk = __hip_atomic_fetch_add((u32*)(p.ws + off), 1u, __ATOMIC_RELAXED, __HIP_MEMORY_SCOPE_AGENT);
  __syncthreads();
  return __builtin_amdgcn_readfirstlane(*tk);
}

DEV u32 claim_role(const Params& p, rsrc_t ws, unsigned char* lds) {
  u32* tk = (u32*)(lds + LDS_MAIN - 16);
  if (threadIdx.x < 64) {
    const int l = threadIdx.x;
    const u32 xcc = (u32)__builtin_amdgcn_s_getreg(20 | (3 << 11)) & 7u;
    u32 t = 0;
    if (l == 0) t = __hip_atomic_fetch_add((u32*)(p.ws + OFF_ROLE + xcc * 128), 1u, __ATOMIC_RELAXED, __HIP_MEMORY_SCOPE_AGENT);
    t = __builtin_amdgcn_readfirstlane(t);
    u32 pos = xcc * 32 + t;
    if (__builtin_expect(t >= 32, 0)) {
      u32 k = 0;
      if (l == 0) k = __hip_atomic_fetch_add((u32*)(p.ws + OFF_ROLE + 1024), 1u, __ATOMIC_RELAXED, __HIP_MEMORY_SCOPE_AGENT);
      k = __builtin_amdgcn_readfirstlane(k);
      u32 c = 0;
#pragma nounroll
      for (u32 spins = 0; spins < SPIN_CAP; ++spins) {
        asm volatile("" ::: "memory");
        c = ld4c(ws, (u32)(l & 7) * 128, OFF_ROLE);
        u32 sum = 0;
        for (int j = 0; j < 8; ++j) sum += (u32)__builtin_amdgcn_readlane(c, j);
        if (sum >= 256) break;
        __builtin_amdgcn_s_sleep(4);
      }
      pos = 255;
      for (int j = 0; j < 8; ++j) {
        const u32 cj = (u32)__builtin_amdgcn_readlane(c, j), fr = cj < 32 ? 32 - cj : 0;
        if (k < fr) { pos = (u32)j * 32 + cj + k; break; }
        k -= fr;
      }
    }
    if (l == 0) *tk = pos;
  }
  __syncthreads();
  return __builtin_amdgcn_readfirstlane(*tk);
}

__global__ __launch_bounds__(512, 2) void k_main(Params p, int mode) {
  extern __shared__ __attribute__((aligned(16))) unsigned char lds[];
  const rsrc_t ws = mk_rsrc(p.ws, WS_BYTES);
  const u32 cb = comm_base(p.ws);
  const int b = blockIdx.x;
  if (mode & 1) GSTAMP(0);
  if (b >= 4) {
    st16c(ws, cb + (u32)(b * 1024 + threadIdx.x) * 16, 0, (u32x4){SENT, SENT, SENT, SENT});
    st16c(ws, cb + (u32)(b * 1024 + 512 + threadIdx.x) * 16, 0, (u32x4){SENT, SENT, SENT, SENT});
  }
  if (b < 112) st16c(ws, OFF_SIDE + (u32)(b * 512 + threadIdx.x) * 16, 0, (u32x4){SENT, SENT, SENT, SENT});
  asm volatile("" ::: "memory");
  SCHED_FENCE();
  const u32 pos = claim_role(p, ws, lds);
  const int x = (int)(pos >> 5), slot = (int)(pos & 31);
  const int cidx = (x >= 4 || slot >= 28) ? 1000 : (slot < 8 ? x * 8 + slot : 32 + x * 20 + slot - 8);
  if (cidx < 112) {
    if (!(mode & 1)) return;
    if (cidx < 32) score_role(p, ws, cb, cidx, lds);
    else value_role(p, ws, cb, cidx - 32, lds);
    GSTAMP(3);
    return;
  }
  if (x < 4) {
    if (mode & 1) {
      init_part(p, ws, cb, x * 4 + slot - 28);
      prefetch_role(ws, x, slot - 28);
    }
    return;
  }
  if (x >= 4 && (mode & 2)) {
    const int h = (x - 4) * 8 + (slot >> 2);
    const bool bw = (slot & 1) && slot < 24;
    if (bw) b_unit(p, ws, 80 + (x - 4) * 12 + (slot >> 1), lds);
    if (slot & 2) vu_role(p, ws, h, slot & 1, lds, !bw);
    else qk_role(p, ws, h, slot & 1, lds, !bw);
    GSTAMP(2);
  }
  if (!(mode & 4)) return;
  const bool outonly = (slot & 3) == 3 && (slot & 4);
  if (!outonly) {
    int t = next_ticket(p, lds, OFF_TICKET) - T_SET;
    while (t < 0) { const int k = t + T_SET; if (k < 40) fw_task(p, ws, k); else wt_task(p, ws, k - 40, lds); t = next_ticket(p, lds, OFF_TICKET) - T_SET; }
    while (t < T_F1) { f1_task(p, ws, t % 5, t / 5, lds); t = next_ticket(p, lds, OFF_TICKET) - T_SET; }
    GSTAMP(4);
    while (t < T_F1 + T_F2) {
      const int q = t - T_F1;
      if (threadIdx.x < 64) { bool dead = false; wait_prog(ws, OFF_F1C + (u32)(q >> 4) * 128, 1, 16, threadIdx.x, dead, 1); }
      wait_fw(ws);
      fft_cols(p, ws, q, lds);
      VM_DRAIN();
      __syncthreads();
      if (threadIdx.x == 0) __hip_atomic_fetch_add((u32*)(p.ws + OFF_F2C), 1u, __ATOMIC_RELAXED, __HIP_MEMORY_SCOPE_AGENT);
      t = next_ticket(p, lds, OFF_TICKET) - T_SET;
    }
    GSTAMP(5);
    while (t < T_F1 + T_F2 + T_F3) { const int q = t - T_F1 - T_F2; f3_task(p, ws, q % 5, q / 5, lds); t = next_ticket(p, lds, OFF_TICKET) - T_SET; }
  }
  GSTAMP(6);
  for (int t = next_ticket(p, lds, OFF_TICKET2); t < T_OUT; t = next_ticket(p, lds, OFF_TICKET2)) out_task(p, ws, t / 5, t % 5, lds);
  GSTAMP(3);
}

extern "C" void kernel_launch(void* const* d_in, const int* in_sizes, int n_in, void* d_out, int out_size, void* d_ws, size_t ws_size,
                              hipStream_t stream) {
  Params p;
  p.ax = (const u16*)d_in[0]; p.wr = (const u16*)d_in[1]; p.wi = (const u16*)d_in[2];
  p.x = (const u16*)d_in[3]; p.mem0 = (const u16*)d_in[4]; p.wq = (const u16*)d_in[5]; p.wk = (const u16*)d_in[6];
  p.wv = (const u16*)d_in[7]; p.wo = (const u16*)d_in[8];
  p.out = (u16*)d_out; p.ws = (unsigned char*)d_ws;
  (void)hipFuncSetAttribute((const void*)k_main, hipFuncAttributeMaxDynamicSharedMemorySize, LDS_MAIN);
  (void)hipMemsetAsync(p.ws + OFF_FLAGS, 0, FLAG_BYTES, stream);
  k_main<<<256, 512, LDS_MAIN, stream>>>(p, 7);
}
```

```cpp
#include <hip/hip_runtime.h>
#include <hip/hip_bf16.h>
#include <stdint.h>
#define BAR4
#define BULK1
#define SPREAD

typedef unsigned short u16;
typedef unsigned int u32;
typedef unsigned long long u64;
typedef __bf16 bf16x2 __attribute__((ext_vector_type(2)));
typedef __bf16 bf16x8 __attribute__((ext_vector_type(8)));
typedef __bf16 bf16x16 __attribute__((ext_vector_type(16)));
typedef float f32x2 __attribute__((ext_vector_type(2)));
typedef float f32x4 __attribute__((ext_vector_type(4)));
typedef u32 u32x2 __attribute__((ext_vector_type(2)));
typedef u32 u32x4 __attribute__((ext_vector_type(4)));
typedef u32 u32x8 __attribute__((ext_vector_type(8)));
typedef __amdgpu_buffer_rsrc_t rsrc_t;
#define DEV __device__ __forceinline__

constexpr int NSEG = 64;
constexpr u32 MB = 1u << 20;
constexpr u32 OFF_WF = 6 * MB;
constexpr u32 OFF_WOF = 21 * MB;
constexpr u32 OFF_BF = 26 * MB;
constexpr u32 OFF_XF = 51 * MB;
constexpr u32 OFF_AXF = 54 * MB;
constexpr u32 OFF_KX = 57 * MB;
constexpr u32 OFF_QX = 73 * MB;
constexpr u32 OFF_UX = 89 * MB;
constexpr u32 OFF_UM = 169 * MB;
constexpr u32 OFF_PXX = 189 * MB;
constexpr u32 OFF_PXM = 193 * MB;
constexpr u32 OFF_FX = 194 * MB;
constexpr u32 OFF_FY = 200 * MB;
constexpr u32 OFF_FLAGS = 206 * MB;
constexpr u32 OFF_FW1 = 208 * MB;
constexpr u32 OFF_FW3 = 210 * MB;
constexpr u32 OFF_SIDE = 212 * MB;
constexpr u32 SIDE_STEP = 112 * 128;
constexpr u32 OFF_WT = 213 * MB;
constexpr u32 WS_BYTES = 216 * MB;
constexpr u32 STEP_BYTES = 65536;
constexpr u32 SENT = 0xFFFFFFFFu;
constexpr u32 SPIN_CAP = 1u << 16;
constexpr u32 OFF_PQK = OFF_FLAGS + 0xA000;
constexpr u32 OFF_PVU = OFF_FLAGS + 0xB000;
constexpr u32 OFF_PPX = OFF_FLAGS;
constexpr u32 OFF_PUM = OFF_FLAGS + 0x1000;
constexpr u32 OFF_TICKET = OFF_FLAGS + 0xB400;
constexpr u32 OFF_F1C = OFF_FLAGS + 0xB480;
constexpr u32 OFF_F2C = OFF_FLAGS + 0xB800;
constexpr u32 OFF_FWC = OFF_FLAGS + 0xB880;
constexpr u32 OFF_TICKET2 = OFF_FLAGS + 0xB900;
constexpr u32 OFF_INITC = OFF_FLAGS + 0xBC00;
constexpr u32 OFF_ROLE = OFF_FLAGS + 0x9000;
constexpr u32 OFF_XFC = OFF_FLAGS + 0xBF00;
constexpr u32 OFF_BC = OFF_FLAGS + 0xBE00;
constexpr u32 FLAG_BYTES = 0xC000;
constexpr u32 OFF_VOTE = OFF_FLAGS + 0xBA00;
constexpr int LDS_MAIN = 97 * 1024;
#define HS0 4
#define HS1 20
constexpr int NHELP = (HS1 - HS0) / 2;
__host__ __device__ constexpr int seg_prod(int s) { return (s >= HS0 && s < HS1) ? (s & 1) : 0; }
__host__ __device__ constexpr u32 seg_need(int s) {
  return s < HS0 ? (u32)s + 1 : (s < HS1 ? ((s & 1) ? (u32)((s - HS0) >> 1) + 1 : (u32)(HS0 + ((s - HS0) >> 1)) + 1) : (u32)(s - NHELP) + 1);
}
__host__ __device__ constexpr int tile_seg(int par, int i) {
  return par ? HS0 + 2 * i + 1 : (i < HS0 ? i : (i < HS0 + NHELP ? HS0 + 2 * (i - HS0) : i + NHELP));
}

struct Params {
  const u16* ax; const u16* wr; const u16* wi;
  const u16* x; const u16* mem0; const u16* wq; const u16* wk; const u16* wv; const u16* wo;
  u16* out; unsigned char* ws;
};

DEV rsrc_t mk_rsrc(const void* p, u32 bytes) { return __builtin_amdgcn_make_buffer_rsrc((void*)p, 0, bytes, 0x00020000); }
DEV u32x4 ld16(rsrc_t r, u32 vo, u32 so) { return __builtin_amdgcn_raw_buffer_load_b128(r, vo, so, 0); }
DEV u32x4 ld16c(rsrc_t r, u32 vo, u32 so) { return __builtin_amdgcn_raw_buffer_load_b128(r, vo, so, 16); }
DEV u32x2 ld8c(rsrc_t r, u32 vo, u32 so) { return __builtin_amdgcn_raw_buffer_load_b64(r, vo, so, 16); }
DEV u32 ld4c(rsrc_t r, u32 vo, u32 so) { return __builtin_amdgcn_raw_buffer_load_b32(r, vo, so, 16); }
DEV void st16(rsrc_t r, u32 vo, u32 so, u32x4 v) { __builtin_amdgcn_raw_buffer_store_b128(v, r, vo + so, 0, 0); }
DEV void st16c(rsrc_t r, u32 vo, u32 so, u32x4 v) { __builtin_amdgcn_raw_buffer_store_b128(v, r, vo + so, 0, 16); }
DEV void st8c(rsrc_t r, u32 vo, u32 so, u32x2 v) { __builtin_amdgcn_raw_buffer_store_b64(v, r, vo, so, 16); }
DEV void st4c(rsrc_t r, u32 vo, u32 so, u32 v) { __builtin_amdgcn_raw_buffer_store_b32(v, r, vo, so, 16); }
DEV u32 pk2(float a, float b) { bf16x2 v = {(__bf16)a, (__bf16)b}; return __builtin_bit_cast(u32, v); }
DEV u32 pk2s(float a, float b) { u32 v = pk2(a, b); return v < 0xFFFFFFFEu ? v : 0xFFFFFFFEu; }
DEV u32 pk2lo(float a, float b, u32 hi) { return pk2(a - __uint_as_float(hi << 16), b - __uint_as_float(hi & 0xFFFF0000u)); }
DEV f32x4 mfma(u32x4 a, u32x4 b, f32x4 c) {
  return __builtin_amdgcn_mfma_f32_16x16x32_bf16(__builtin_bit_cast(bf16x8, a), __builtin_bit_cast(bf16x8, b), c, 0, 0, 0);
}
DEV f32x4 smfma(u32x4 a, u32x8 b, f32x4 c, int idx) {
  return __builtin_amdgcn_smfmac_f32_16x16x64_bf16(__builtin_bit_cast(bf16x8, a), __builtin_bit_cast(bf16x16, b), c, idx, 0, 0);
}
DEV u32x8 cat(u32x4 a, u32x4 b) { return __builtin_shufflevector(a, b, 0, 1, 2, 3, 4, 5, 6, 7); }
DEV u32 comm_base(const unsigned char* ws) { return (u32)((2 * MB - ((uintptr_t)ws & (2 * MB - 1))) & (2 * MB - 1)); }
DEV u64 stamp_cyc() { u64 t; asm volatile("s_memtime %0\n\ts_waitcnt lgkmcnt(0)" : "=s"(t) :: "memory"); return t; }
DEV u32 umax3(u32 a, u32 b, u32 c) { u32 m = a > b ? a : b; return m > c ? m : c; }
DEV float fmx(float a, float b) { return __builtin_elementwise_maximum(a, b); }
#define SCHED_FENCE() __builtin_amdgcn_sched_barrier(0)
#define VM_DRAIN() asm volatile("s_waitcnt vmcnt(0)" ::: "memory")
#define RAW_BARRIER() do { asm volatile("s_waitcnt lgkmcnt(0)" ::: "memory"); __builtin_amdgcn_s_barrier(); asm volatile("" ::: "memory"); } while (0)

#define STAMP(ev)
#define STAMPV(ev, val)
#define COUNT(ev, val)
#define GSTAMP(slot)

typedef short s16x4 __attribute__((ext_vector_type(4)));
#define LAS __attribute__((address_space(3)))
DEV u32x4 tr_frag(const unsigned char* a0, const unsigned char* a1) {
  const u32x2 x = __builtin_bit_cast(u32x2, __builtin_amdgcn_ds_read_tr16_b64_v4i16((LAS s16x4*)a0));
  const u32x2 y = __builtin_bit_cast(u32x2, __builtin_amdgcn_ds_read_tr16_b64_v4i16((LAS s16x4*)a1));
  return (u32x4){x.x, x.y, y.x, y.y};
}
DEV float dpp_ror8(float x) { return __uint_as_float(__builtin_amdgcn_update_dpp(0, (int)__float_as_uint(x), 0x128, 0xF, 0xF, true)); }
DEV float red_max_g(float m) {
  auto a = __builtin_amdgcn_permlane16_swap(__float_as_uint(m), __float_as_uint(m), false, false);
  m = fmx(__uint_as_float(a[0]), __uint_as_float(a[1]));
  auto b = __builtin_amdgcn_permlane32_swap(__float_as_uint(m), __float_as_uint(m), false, false);
  return fmx(__uint_as_float(b[0]), __uint_as_float(b[1]));
}
DEV float red_sum_g(float m) {
  auto a = __builtin_amdgcn_permlane16_swap(__float_as_uint(m), __float_as_uint(m), false, false);
  m = __uint_as_float(a[0]) + __uint_as_float(a[1]);
  auto b = __builtin_amdgcn_permlane32_swap(__float_as_uint(m), __float_as_uint(m), false, false);
  return __uint_as_float(b[0]) + __uint_as_float(b[1]);
}
DEV float red_max(float m) { return red_max_g(fmx(m, dpp_ror8(m))); }
DEV float red_sum(float m) { return red_sum_g(m + dpp_ror8(m)); }

#define NPROBE 3
DEV u32 probe(rsrc_t ws, u32 cb, int idx, int page, int l) {
  const u32 vo = (l == 0) ? 0u : 0x80000000u;
  const u32 so = cb + (u32)(page * 32 + 30) * STEP_BYTES + (u32)(2 + (idx >> 6)) * 16384 + (u32)(idx & 63) * 128;
  u32 b0 = ~0u, b1 = ~0u;
#pragma nounroll
  for (int rep = 0; rep < 2 * NPROBE; ++rep) {
    u64 t0 = stamp_cyc();
    st4c(ws, vo, so + (u32)(rep & 1) * 8192, SENT);
    VM_DRAIN();
    u64 t1 = stamp_cyc();
    u32 d = (u32)(t1 - t0);
    if (rep & 1) b1 = d < b1 ? d : b1; else b0 = d < b0 ? d : b0;
  }
  return b1 < b0 ? 1u : 0u;
}

DEV u32 xcc_group() { return ((u32)__builtin_amdgcn_s_getreg(20 | (3 << 11)) >> 2) & 3u; }
DEV void vote(const Params& p, int page, u32 r, int l) {
  if (l == 0) __hip_atomic_fetch_add((u32*)(p.ws + OFF_VOTE + (xcc_group() * 2 + page) * 64), r + 0x10000u, __ATOMIC_RELAXED, __HIP_MEMORY_SCOPE_AGENT);
}
DEV u32 vote_result(rsrc_t ws, int l) {
  const u32 x = ld4c(ws, (u32)(l & 1) * 64, OFF_VOTE + xcc_group() * 128);
  const u32 m = (2 * (x & 0xFFFFu) > (x >> 16)) ? 1u : 0u;
  return (u32)__builtin_amdgcn_readlane(m, 0) | ((u32)__builtin_amdgcn_readlane(m, 1) << 1);
}
#define D1 140
#define D2 12
#define D3 60
#define D4 12
#define DV 15
#define DX 25
#define DL 5
#define PFD 2
#define DLS 16
#define THR 6
#define THR_S2(x) #x
#define THR_S(x) THR_S2(x)
#define THROTTLE(i) asm volatile("s_waitcnt vmcnt(" THR_S(THR) ")" ::: "memory")
#define UMPOS 0
#define PSLEEP 0
#define PFW 1
#define PFQ 4
#define PFS 2
#define PFSTR 128
#define PROT v
#define KQPOS 0
#define DA 12
DEV u32 rtime() { return (u32)__builtin_amdgcn_s_memrealtime(); }
DEV void wait_until(u32 T) {
  u32 r = rtime();
  if ((int)(T - r) < 28) {
#pragma nounroll
    for (int i = 0; i < 64 && (int)(T - r) > 0; ++i) r = rtime();
    return;
  }
#pragma nounroll
  for (int i = 0; i < 4096 && (int)(T - r) > 35; ++i) r = rtime();
  u32 n;
  asm volatile(
    "s_memrealtime s[80:81]\n\ts_nop 2\n\ts_memrealtime s[82:83]\n\ts_nop 2\n\ts_memrealtime s[84:85]\n\ts_nop 2\n\ts_memrealtime s[86:87]\n\ts_nop 2\n\t"
    "s_memrealtime s[88:89]\n\ts_nop 2\n\ts_memrealtime s[90:91]\n\ts_nop 2\n\ts_memrealtime s[92:93]\n\ts_nop 2\n\ts_memrealtime s[94:95]\n\ts_waitcnt lgkmcnt(0)\n\t"
    "s_mov_b32 s81, 0\n\t"
    "s_cmp_eq_u32 s82, s80\n\ts_addc_u32 s81, s81, 0\n\t"
    "s_cmp_eq_u32 s84, s80\n\ts_addc_u32 s81, s81, 0\n\t"
    "s_cmp_eq_u32 s86, s80\n\ts_addc_u32 s81, s81, 0\n\t"
    "s_cmp_eq_u32 s88, s80\n\ts_addc_u32 s81, s81, 0\n\t"
    "s_cmp_eq_u32 s90, s80\n\ts_addc_u32 s81, s81, 0\n\t"
    "s_cmp_eq_u32 s92, s80\n\ts_addc_u32 s81, s81, 0\n\t"
    "s_cmp_eq_u32 s94, s80\n\ts_addc_u32 s81, s81, 0\n\t"
    "s_sub_u32 %0, %1, s80\n\t"
    "s_mul_i32 %0, %0, 3\n\t"
    "s_lshl1_add_u32 %0, s81, %0\n\t"
    "s_sub_i32 %0, %0, 64\n\t"
    "s_max_i32 %0, %0, 0\n\t"
    "s_min_i32 %0, %0, 63\n\t"
    "s_bitcmp1_b32 %0, 0\n\ts_cbranch_scc0 1f\n\ts_nop 5\n\t1:\n\t"
    "s_bitcmp1_b32 %0, 1\n\ts_cbranch_scc0 2f\n\ts_nop 7\n\t2:\n\t"
    "s_bitcmp1_b32 %0, 2\n\ts_cbranch_scc0 3f\n\ts_nop 11\n\t3:\n\t"
    "s_bitcmp1_b32 %0, 3\n\ts_cbranch_scc0 4f\n\ts_nop 15\n\ts_nop 3\n\t4:\n\t"
    "s_bitcmp1_b32 %0, 4\n\ts_cbranch_scc0 5f\n\ts_nop 15\n\ts_nop 15\n\ts_nop 3\n\t5:\n\t"
    "s_bitcmp1_b32 %0, 5\n\ts_cbranch_scc0 6f\n\ts_nop 15\n\ts_nop 15\n\ts_nop 15\n\ts_nop 15\n\ts_nop 3\n\t6:\n\t"
    : "=&s"(n) : "s"(T)
    : "scc", "memory", "s80", "s81", "s82", "s83", "s84", "s85", "s86", "s87", "s88", "s89", "s90", "s91", "s92", "s93", "s94", "s95");
}
template <int CTRL> DEV int dpp_i(int x) { return __builtin_amdgcn_update_dpp(x, x, CTRL, 0xF, 0xF, false); }
DEV int row_max(int d) {
  d = max(d, dpp_i<0x121>(d));
  d = max(d, dpp_i<0x122>(d));
  d = max(d, dpp_i<0x124>(d));
  return max(d, dpp_i<0x128>(d));
}
DEV int wave_max(int d) {
  d = row_max(d);
  return max(max(__builtin_amdgcn_readlane(d, 0), __builtin_amdgcn_readlane(d, 16)), max(__builtin_amdgcn_readlane(d, 32), __builtin_amdgcn_readlane(d, 48)));
}
DEV u32 fine_now() {
  u32 n;
  asm volatile(
    "s_memrealtime s[80:81]\n\ts_nop 2\n\ts_memrealtime s[82:83]\n\ts_nop 2\n\ts_memrealtime s[84:85]\n\ts_nop 2\n\ts_memrealtime s[86:87]\n\ts_nop 2\n\t"
    "s_memrealtime s[88:89]\n\ts_nop 2\n\ts_memrealtime s[90:91]\n\ts_nop 2\n\ts_memrealtime s[92:93]\n\ts_waitcnt lgkmcnt(0)\n\t"
    "s_mov_b32 s81, 0\n\t"
    "s_cmp_eq_u32 s82, s80\n\ts_addc_u32 s81, s81, 0\n\t"
    "s_cmp_eq_u32 s84, s80\n\ts_addc_u32 s81, s81, 0\n\t"
    "s_cmp_eq_u32 s86, s80\n\ts_addc_u32 s81, s81, 0\n\t"
    "s_cmp_eq_u32 s88, s80\n\ts_addc_u32 s81, s81, 0\n\t"
    "s_cmp_eq_u32 s90, s80\n\ts_addc_u32 s81, s81, 0\n\t"
    "s_cmp_eq_u32 s92, s80\n\ts_addc_u32 s81, s81, 0\n\t"
    "s_mul_i32 s81, s81, 11\n\t"
    "s_lshr_b32 s81, s81, 4\n\t"
    "s_add_u32 %0, s80, 4\n\t"
    "s_sub_u32 %0, %0, s81\n\t"
    : "=&s"(n) :
    : "scc", "memory", "s80", "s81", "s82", "s83", "s84", "s85", "s86", "s87", "s88", "s89", "s90", "s91", "s92", "s93");
  return n;
}
struct Side { u32x2 a, b; };
DEV Side load_side(rsrc_t ws, int s, int l) {
  Side t;
  t.a = ld8c(ws, (u32)l * 128, OFF_SIDE + (u32)s * SIDE_STEP);
  t.b = ld8c(ws, (u32)(64 + (l & 15)) * 128, OFF_SIDE + (u32)s * SIDE_STEP);
  return t;
}
DEV void schedule(rsrc_t ws, int s, int l, Side& t, u32& Rm, u32& Rp, bool& dead) {
  if (__builtin_expect(__any(umax3(t.a.x, t.a.y, t.b.x > t.b.y ? t.b.x : t.b.y) == SENT), 0)) {
#pragma nounroll
    for (u32 spins = 0; !dead; ++spins) {
      asm volatile("" ::: "memory");
      t = load_side(ws, s, l);
      if (__all(umax3(t.a.x, t.a.y, t.b.x > t.b.y ? t.b.x : t.b.y) != SENT)) break;
      if (spins > SPIN_CAP) dead = true;
    }
  }
  u32 B = Rm;
  if (__builtin_expect(__any((t.a.x | t.b.x) != 0), 0)) {
    const u32 ref = rtime();
    const int dx = t.a.x ? (int)(t.a.x - ref) : -(1 << 30), dy = t.b.x ? (int)(t.b.x - ref) : -(1 << 30);
    B = ref + (u32)wave_max(max(dx, dy)) + D4 + DX;
  }
  const u32 d = __builtin_amdgcn_readfirstlane(t.a.y);
  Rp = B + ((d >> 10) & 1023u);
  Rm = Rp + ((d >> 20) & 1023u);
}

DEV u32 gather_mem(rsrc_t ws, u32 sbp, int wr, unsigned char* dA, unsigned char* dB, int l, bool& dead) {
  u32x4 a = ld16c(ws, (u32)l * 16, sbp + (u32)wr * 1024);
  u32 b = ld4c(ws, (u32)l * 4, sbp + 16384 + (u32)wr * 256);
  SCHED_FENCE();
  *(u32x4*)dA = a;
  *(u32*)dB = b;
  u32 m = umax3(umax3(a.x, a.y, a.z), a.w, b);
  u32 spins = 0;
  if (__builtin_expect(__any(m == SENT), 0)) {
#pragma nounroll
    while (!dead) {
      ++spins;
      asm volatile("" ::: "memory");
      a = ld16c(ws, (u32)l * 16, sbp + (u32)wr * 1024);
      b = ld4c(ws, (u32)l * 4, sbp + 16384 + (u32)wr * 256);
      m = umax3(umax3(a.x, a.y, a.z), a.w, b);
      if (__all(m != SENT)) break;
      if (spins > SPIN_CAP) dead = true;
    }
    *(u32x4*)dA = a;
    *(u32*)dB = b;
  }
  return spins;
}
DEV u32 wait_prog(rsrc_t ws, u32 off, int n, u32 need, int l, bool& dead, int slp, u32 stride = 4) {
  u32 mn = 0;
#pragma nounroll
  for (u32 spins = 0;; ++spins) {
    asm volatile("" ::: "memory");
    const u32 x = ld4c(ws, (u32)(l < n ? l : 0) * stride, off);
    int d = -(int)x;
    d = row_max(d);
    mn = (u32)(-max(__builtin_amdgcn_readlane(d, 0), __builtin_amdgcn_readlane(d, 16)));
    if (mn >= need || dead) break;
    if (spins > SPIN_CAP) { dead = true; break; }
    if (slp == 2) __builtin_amdgcn_s_sleep(32); else if (slp) __builtin_amdgcn_s_sleep(6); else __builtin_amdgcn_s_sleep(1);
  }
  asm volatile("" ::: "memory");
  return mn;
}

DEV void init_signal(const Params& p, bool on) {
  if (on && threadIdx.x == 0) __hip_atomic_fetch_add((u32*)(p.ws + OFF_INITC + (blockIdx.x & 7) * 64), 1u, __ATOMIC_RELAXED, __HIP_MEMORY_SCOPE_AGENT);
}
DEV int wrow(int r) { return r * 288 + (r >> 3) * 128; }
DEV void load_qk(const Params& p, int h, int w, int l, unsigned char* lds, u32x4 (&wt)[2][20], bool sig) {
  const rsrc_t src = mk_rsrc((w >> 2) ? p.wk : p.wq, 640 * 8192);
  u32x4 st[40];
#pragma unroll
  for (int i = 0; i < 40; ++i)
    st[i] = ld16(src, (u32)((l >> 4) * 8192 + (l & 15) * 16), (u32)((32 * (w & 3) + (i >> 3) * 128 + (i & 7) * 4) * 8192 + h * 256));
  unsigned char* img = lds + (w >> 2) * 38912;
  unsigned char* wp = img + wrow(32 * (w & 3)) + (l >> 4) * 288 + (l & 15) * 16;
  const unsigned char* rp = img + wrow(8 * (l >> 4)) + ((l >> 2) & 3) * 288 + (w & 3) * 64 + (l & 3) * 8;
#pragma unroll
  for (int r = 0; r < 5; ++r) {
#pragma unroll
    for (int i = 0; i < 8; ++i) *(u32x4*)(wp + wrow(4 * i)) = st[r * 8 + i];
    __syncthreads();
    if (r == 0) init_signal(p, sig);
#pragma unroll
    for (int kk = 0; kk < 4; ++kk)
#pragma unroll
      for (int i = 0; i < 2; ++i) wt[i][4 * r + kk] = tr_frag(rp + wrow(32 * kk) + i * 32, rp + wrow(32 * kk) + 4 * 288 + i * 32);
    __syncthreads();
  }
}
DEV void load_vu(const Params& p, int h, int w, int l, unsigned char* lds, u32x4 (&wv)[20], u32x4 (&wo)[5][4], bool sig) {
  const rsrc_t sv = mk_rsrc(p.wv, 640 * 8192), so = mk_rsrc(p.wo, 640 * 8192);
  u32x4 st[40];
#pragma unroll
  for (int i = 0; i < 20; ++i)
    st[i] = ld16(sv, (u32)((l >> 4) * 8192 + (l & 15) * 16), (u32)((40 * w + (i / 10) * 320 + (i % 10) * 4) * 8192 + h * 256));
#pragma unroll
  for (int i = 0; i < 20; ++i) st[20 + i] = ld16(so, (u32)l * 16, (u32)(h * 163840 + (w * 640 + (i / 10) * 5120 + (i % 10) * 64) * 16));
  unsigned char* wp = lds + wrow(40 * w) + (l >> 4) * 288 + (l & 15) * 16;
  const unsigned char* rp = lds + wrow(8 * (l >> 4)) + ((l >> 2) & 3) * 288 + w * 32 + (l & 3) * 8;
#pragma unroll
  for (int r = 0; r < 2; ++r) {
#pragma unroll
    for (int i = 0; i < 10; ++i) *(u32x4*)(wp + wrow(4 * i)) = st[r * 10 + i];
    __syncthreads();
    if (r == 0) init_signal(p, sig);
#pragma unroll
    for (int kk = 0; kk < 10; ++kk) wv[10 * r + kk] = tr_frag(rp + wrow(32 * kk), rp + wrow(32 * kk) + 4 * 288);
    __syncthreads();
  }
  const unsigned char* ro = lds + (4 * (l >> 4) + ((l >> 2) & 3)) * 1312 + w * 160 + (l & 3) * 8;
#pragma unroll
  for (int r = 0; r < 2; ++r) {
#pragma unroll
    for (int i = 0; i < 10; ++i) {
      const int c = (w * 10 + i) * 64 + l;
      *(u32x4*)(lds + (c / 80) * 1312 + (c % 80) * 16) = st[20 + r * 10 + i];
    }
    __syncthreads();
#pragma unroll
    for (int kk = 0; kk < 2; ++kk)
#pragma unroll
      for (int ct = 0; ct < 5; ++ct) wo[ct][2 * r + kk] = tr_frag(ro + 32 * kk * 1312 + ct * 32, ro + (32 * kk + 16) * 1312 + ct * 32);
    __syncthreads();
  }
}

DEV void count_done(const Params& p, u32 off) {
  VM_DRAIN();
  __syncthreads();
  if (threadIdx.x == 0) __hip_atomic_fetch_add((u32*)(p.ws + off), 1u, __ATOMIC_RELAXED, __HIP_MEMORY_SCOPE_AGENT);
}
DEV void wait_init(rsrc_t ws) {
  if (threadIdx.x < 64) { bool dead = false; wait_prog(ws, OFF_XFC, 1, 16, threadIdx.x, dead, 0); }
  __syncthreads();
}
DEV void b_unit(const Params& p, rsrc_t ws, int u, unsigned char* lds) {
  const int tid = threadIdx.x, l = tid & 63, c16 = l & 15, g = l >> 4;
  const int w = __builtin_amdgcn_readfirstlane(tid >> 6);
  const int h = u >> 2, dh = (u >> 1) & 1, ch = u & 1;
  const rsrc_t rv = mk_rsrc(p.wv, 640 * 8192), ro = mk_rsrc(p.wo, 640 * 8192);
  u32x4 wo_r[10];
#pragma unroll
  for (int i = 0; i < 10; ++i) {
    const int c = tid + 512 * i, e = c / 40, cc = c % 40;
    wo_r[i] = ld16(ro, (u32)(e * 1280 + cc * 16), (u32)(h * 163840 + ch * 640));
  }
  u32x4 a[3][2][4];
#pragma unroll
  for (int i = 0; i < 3; ++i) {
    const int bl = (w & 3) + 4 * i;
    const u32 so = bl < 10 ? (u32)((32 * (10 * dh + bl)) * 8192 + h * 256) : 0x80000000u;
#pragma unroll
    for (int t = 0; t < 2; ++t)
#pragma unroll
      for (int ks = 0; ks < 4; ++ks)
        a[i][t][ks] = ld16(rv, (u32)((8 * (c16 >> 2) + (c16 & 3) + 4 * t) * 8192 + g * 16 + ks * 64), so);
  }
  SCHED_FENCE();
#pragma unroll
  for (int i = 0; i < 10; ++i) {
    const int c = tid + 512 * i, e = c / 40, cc = c % 40;
    *(u32x4*)(lds + e * 672 + (e >> 3) * 128 + cc * 16) = wo_r[i];
  }
  __syncthreads();
  init_signal(p, true);
  const u32 vo = (u32)(((c16 >> 1) & 1) * (16 * 20 * 1024) + (16 * g + 8 * (h & 1) + 2 * (c16 >> 2) + (c16 & 1)) * 16);
#pragma nounroll
  for (int half = 0; half < 2; ++half) {
    u32x4 bf[5][4];
    const unsigned char* rp = lds + (8 * g + ((l >> 2) & 3)) * 672 + g * 128 + half * 320 + (w >> 2) * 160 + (l & 3) * 8;
#pragma unroll
    for (int ct = 0; ct < 5; ++ct)
#pragma unroll
      for (int ks = 0; ks < 4; ++ks) bf[ct][ks] = tr_frag(rp + ks * (32 * 672 + 512) + ct * 32, rp + ks * (32 * 672 + 512) + 4 * 672 + ct * 32);
#pragma unroll
    for (int i = 0; i < 3; ++i) {
      const int bl = (w & 3) + 4 * i;
      if (bl < 10) {
#pragma unroll
        for (int ct = 0; ct < 5; ++ct) {
          f32x4 c0 = {0, 0, 0, 0}, c1 = {0, 0, 0, 0};
#pragma unroll
          for (int ks = 0; ks < 4; ++ks) { c0 = mfma(a[i][0][ks], bf[ct][ks], c0); c1 = mfma(a[i][1][ks], bf[ct][ks], c1); }
          u32x4 o = {pk2(c0[0], c0[1]), pk2(c0[2], c0[3]), pk2(c1[0], c1[1]), pk2(c1[2], c1[3])};
          st16c(ws, vo, OFF_BF + (u32)(((2 * (20 * ch + 10 * half + 5 * (w >> 2) + ct) * 16 + (h >> 1)) * 20 + 10 * dh + bl) * 1024), o);
        }
      }
    }
  }
  count_done(p, OFF_BC + (u32)ch * 128);
}
DEV void init_part(const Params& p, rsrc_t ws, u32 cb, int k) {
  const int tid = threadIdx.x;
  u32x4 xw[20];
#pragma unroll
  for (int i = 0; i < 20; ++i) {
    const int seg = k + 16 * (i / 5), r = (i % 5) * 512 + tid, tt = r / 1280, ks = (r % 1280) >> 6, ln = r & 63;
    xw[i] = *(const u32x4*)(p.x + (32 * seg + 16 * tt + (ln & 15)) * 640 + 32 * ks + 8 * (ln >> 4));
    if (i == 4) SCHED_FENCE();
  }
  if (tid < 256) {
    const u32 o = (u32)(k * 256 + tid) * 16;
    const u32 L = (o >> 14) * 8192 + (o & 8191);
    u32x4 v = {SENT, SENT, SENT, SENT};
    if (L < 10240) {
      const int vv = L >> 7, t = (L >> 4) & 7;
      const u16* m = p.mem0 + t * 640 + 16 * (vv >> 1) + 2 * (vv & 1);
      for (int q = 0; q < 4; ++q) { u32 x = *(const u32*)(m + 4 * q); v[q] = x < 0xFFFFFFFEu ? x : 0xFFFFFFFEu; }
    }
    st16c(ws, cb + o, 0, v);
  }
#pragma unroll
  for (int i = 0; i < 5; ++i) st16c(ws, (u32)(i * 512 + tid) * 16, OFF_XF + (u32)k * 40960, xw[i]);
  count_done(p, OFF_XFC);
  init_signal(p, true);
#pragma unroll
  for (int i = 5; i < 20; ++i) st16c(ws, (u32)((i % 5) * 512 + tid) * 16, OFF_XF + (u32)(k + 16 * (i / 5)) * 40960, xw[i]);
  count_done(p, OFF_XFC + 64);
}

constexpr float CEXP = 0.08838834764831845f * 1.4426950408889634f;
constexpr float NEGS = -1.0e9f / 0.08838834764831845f;
constexpr int QS = 288;

DEV void read_memf(const unsigned char* memf, int l, u32x4 (&a)[10]) {
#pragma unroll
  for (int ks = 0; ks < 10; ++ks) a[ks] = *(const u32x4*)(memf + ks * 1024 + l * 16);
}
DEV void project2(const u32x4 (&a)[10], const u32x8 (&wt)[2][10], int sidx, unsigned char* dst) {
  f32x4 c0 = {0, 0, 0, 0}, c1 = {0, 0, 0, 0};
#pragma unroll
  for (int ks = 0; ks < 10; ++ks) {
    c0 = smfma(a[ks], wt[0][ks], c0, sidx);
    c1 = smfma(a[ks], wt[1][ks], c1, sidx);
  }
  *(__bf16*)(dst) = (__bf16)(c0[0] + c0[1]);
  *(__bf16*)(dst + QS) = (__bf16)(c0[2] + c0[3]);
  *(__bf16*)(dst + 8) = (__bf16)(c1[0] + c1[1]);
  *(__bf16*)(dst + 8 + QS) = (__bf16)(c1[2] + c1[3]);
}

DEV void score_role(const Params& p, rsrc_t ws, u32 cb, int h, unsigned char* lds) {
  const int tid = threadIdx.x, l = tid & 63, c16 = l & 15, g = l >> 4;
  const int w = __builtin_amdgcn_readfirstlane(tid >> 6);
  unsigned char* memf = lds;
  unsigned char* qs = lds + 10240;
  unsigned char* kms = qs + 8 * QS;
  u32* ctl = (u32*)(lds + 15360);
  unsigned char* kq = lds + 16384;
  u32x8 wt[2][10];
  {
    u32x4 wf[2][20];
    load_qk(p, h, w, l, lds, wf, true);
#pragma unroll
    for (int i = 0; i < 2; ++i)
#pragma unroll
      for (int ks = 0; ks < 10; ++ks) wt[i][ks] = cat(wf[i][2 * ks], wf[i][2 * ks + 1]);
  }
  GSTAMP(1);
  bool dead = false;
  if (w == 0) wait_prog(ws, OFF_INITC, 8, 32, l, dead, 0, 64);
  __syncthreads();
  if (tid < 2) ctl[2 + tid] = 0;
  u32 seen0 = 0, seen1 = 0;
  const u32 kq_src = ((w & 2) ? OFF_QX : OFF_KX) + (u32)(h * 8192 + (w & 1) * 4096);
  if (w < 2) { u32 r = probe(ws, cb, h, w, l); vote(p, w, r, l); if (l == 0) ctl[4 + w] = r; }
  else if (w >= 4) {
    seen0 = wait_prog(ws, OFF_PQK + (u32)h * 128, 1, 2, l, dead, 0);
#pragma unroll
    for (int i = 0; i < 4; ++i) *(u32x4*)(kq + (w - 4) * 4096 + i * 1024 + l * 16) = ld16c(ws, l * 16 + i * 1024, kq_src);
  }
  __syncthreads();
  GSTAMP(2);
  u32 parmask = __builtin_amdgcn_readfirstlane(ctl[4] | (ctl[5] << 1));
  const int sidx = (l & 1) ? 0xEEEE : 0x4444;
  unsigned char* qk_w = qs + (w >> 2) * 8 * QS + 2 * g * QS + (32 * (w & 3) + 8 * (c16 >> 2) + (c16 & 3)) * 2;
  const unsigned char* km_r = kms + (2 * (c16 >> 2) + (c16 & 1)) * QS + g * 16;
  const int wr = (w + h) & 7;
  unsigned char* gA = memf + wr * 1024 + g * 256 + (l & 7) * 32 + ((l >> 3) & 1) * 16;
  unsigned char* gB = memf + 8192 + wr * 256 + ((l >> 2) & 7) * 32 + (l >> 5) * 16 + (l & 3) * 4;
  if (w == 0) {
    const unsigned char* q_r = qs + (c16 & 7) * QS + g * 16;
    const bool hb = c16 >= 8;
    const u32 xo = (u32)(((h >> 1) & 7) * 1024 + g * 256 + (h & 1) * 128 + (c16 & 7) * 16 + (c16 >> 3) * 8);
    const u32 mo = (u32)((c16 >> 3) * 8192 + h * 128 + (c16 & 7) * 16 + g * 4);
    for (int s = 0; s < NSEG; ++s) {
      const u32 sb = cb + (u32)s * STEP_BYTES;
      if (s == 1) parmask = vote_result(ws, l);
      const u32 par = ((parmask >> (s >> 5)) & 1) * 8192;
      const unsigned char* kxs = kq + (s & 1) * 16384;
      __syncthreads();
      { u32 sp = gather_mem(ws, sb + par, wr, gA, gB, l, dead); COUNT(5, sp); if (sp && l == 0) ctl[2 + (s & 1)] = (u32)s; }
      __syncthreads();
      { u32x4 a[10]; read_memf(memf, l, a); SCHED_FENCE(); project2(a, wt, sidx, qk_w); }
      u32x4 kx[8];
#pragma unroll
      for (int i = 0; i < 8; ++i) kx[i] = *(const u32x4*)(kxs + i * 1024 + l * 16);
      __syncthreads();
      if (s == NSEG - 1) break;
      u32x4 qf[4], km[4];
#pragma unroll
      for (int ks = 0; ks < 4; ++ks) { qf[ks] = *(const u32x4*)(q_r + ks * 64); km[ks] = *(const u32x4*)(km_r + ks * 64); }
      SCHED_FENCE();
      f32x4 s0 = {0, 0, 0, 0}, s1 = {0, 0, 0, 0}, s2 = {0, 0, 0, 0};
#pragma unroll
      for (int ks = 0; ks < 4; ++ks) {
        s1 = mfma(kx[ks], qf[ks], s1);
        s2 = mfma(kx[4 + ks], qf[ks], s2);
        s0 = mfma(km[ks], qf[ks], s0);
      }
      float v[4];
#pragma unroll
      for (int r = 0; r < 4; ++r) v[r] = hb ? s2[r] : s1[r];
      float m = fmx(fmx(fmx(v[0], v[1]), v[2]), fmx(fmx(v[3], s0[0]), s0[1]));
      m = red_max(m);
      const float mc = -m * CEXP;
#pragma unroll
      for (int r = 0; r < 4; ++r) v[r] = __builtin_amdgcn_exp2f(fmaf(v[r], CEXP, mc));
      const float e0 = __builtin_amdgcn_exp2f(fmaf(s0[0], CEXP, mc)), e1 = __builtin_amdgcn_exp2f(fmaf(s0[1], CEXP, mc));
      float sum = ((v[0] + v[1]) + (v[2] + v[3])) + (hb ? e1 : e0);
      sum = red_sum(sum);
      const float inv = __builtin_amdgcn_rcpf(sum);
      u32x2 px = {pk2s(v[0] * inv, v[1] * inv), pk2s(v[2] * inv, v[3] * inv)};
      const u32 pm = pk2s(e0 * inv, e1 * inv);
      const u32 xs = sb + (u32)(2 + (h >> 4)) * 16384;
      st8c(ws, xo, xs + par, px);
      st4c(ws, mo, sb + 16384 + 2048, pm);
      st8c(ws, xo, xs + (par ^ 8192), px);
      const u32 tnow = fine_now();
      const u32 rm = __builtin_amdgcn_readfirstlane(ctl[s & 1]), bad = __builtin_amdgcn_readfirstlane(ctl[2 + (s & 1)]);
      const u32 d1 = (s >= 2 && bad != (u32)s) ? tnow - rm : 0u;
      st4c(ws, l < 32 ? (u32)l * 4 : 0x80000000u, OFF_SIDE + (u32)s * SIDE_STEP + (u32)(80 + h) * 128, d1 < 1000u ? d1 : 0u);
      STAMPV(7, d1);
      STAMPV(4, tnow);
    }
  } else if (w < 3) {
    const int tx = 16 * (w - 1) + c16;
    for (int s = 0; s < NSEG; ++s) {
      const unsigned char* kxs = kq + (s & 1) * 16384;
      const u32 sb = cb + (u32)s * STEP_BYTES;
      if (s == 1) parmask = vote_result(ws, l);
      const u32 par = ((parmask >> (s >> 5)) & 1) * 8192;
      __syncthreads();
      { u32 sp = gather_mem(ws, sb + par, wr, gA, gB, l, dead); COUNT(5, sp); if (sp && l == 0) ctl[2 + (s & 1)] = (u32)s; }
      __syncthreads();
      if (w == 1) STAMP(1);
      { u32x4 a[10]; read_memf(memf, l, a); SCHED_FENCE(); project2(a, wt, sidx, qk_w); }
      __syncthreads();
      if (w == 1) STAMP(2);
      if (w == 1) st4c(ws, l == 0 ? 0u : 0x80000000u, OFF_PPX + (u32)h * 128, (u32)s);
      f32x4 s0 = {0, 0, 0, 0}, s1 = {0, 0, 0, 0}, s2 = {0, 0, 0, 0};
#pragma unroll
      for (int ks = 0; ks < 4; ++ks) {
        u32x4 qf = *(const u32x4*)(kxs + 8192 + ((w - 1) * 4 + ks) * 1024 + l * 16);
        s0 = mfma(*(const u32x4*)(km_r + ks * 64), qf, s0);
        s1 = mfma(*(const u32x4*)(kxs + ks * 1024 + l * 16), qf, s1);
        s2 = mfma(*(const u32x4*)(kxs + (4 + ks) * 1024 + l * 16), qf, s2);
      }
      float m = fmx(s0[0], s0[1]);
#pragma unroll
      for (int r = 0; r < 4; ++r) {
        s1[r] = (4 * g + r <= tx) ? s1[r] : NEGS;
        s2[r] = (16 + 4 * g + r <= tx) ? s2[r] : NEGS;
        m = fmx(m, fmx(s1[r], s2[r]));
      }
      m = red_max_g(m);
      const float mc = -m * CEXP;
      const float e0 = __builtin_amdgcn_exp2f(fmaf(s0[0], CEXP, mc)), e1 = __builtin_amdgcn_exp2f(fmaf(s0[1], CEXP, mc));
      float sum = e0 + e1;
#pragma unroll
      for (int r = 0; r < 4; ++r) {
        s1[r] = __builtin_amdgcn_exp2f(fmaf(s1[r], CEXP, mc));
        s2[r] = __builtin_amdgcn_exp2f(fmaf(s2[r], CEXP, mc));
        sum += s1[r] + s2[r];
      }
      sum = red_sum_g(sum);
      const float inv = __builtin_amdgcn_rcpf(sum);
      u32x4 px = {pk2(s1[0] * inv, s1[1] * inv), pk2(s1[2] * inv, s1[3] * inv), pk2(s2[0] * inv, s2[1] * inv), pk2(s2[2] * inv, s2[3] * inv)};
      st16c(ws, l * 16, OFF_PXX + (u32)(((s * 32 + h) * 2 + (w - 1)) * 1024), px);
      st4c(ws, c16 * 16 + g * 4, OFF_PXM + (u32)((((s * 2 + (w - 1)) * 32 + h) * 16) * 16), pk2(e0 * inv, e1 * inv));
      VM_DRAIN();
    }
  } else if (w == 3) {
    u32 Rm = 0, Rp;
    for (int s = 0; s < NSEG; ++s) {
      const u32 sb = cb + (u32)s * STEP_BYTES;
      if (s == 1) parmask = vote_result(ws, l);
      const u32 par = ((parmask >> (s >> 5)) & 1) * 8192;
      if (s >= 2) {
        if (l == 0) { ctl[s & 1] = Rm; if ((int)(Rm - rtime()) < 28) ctl[2 + (s & 1)] = (u32)s; }
        wait_until(Rm);
      }
      __syncthreads();
      { u32 sp = gather_mem(ws, sb + par, wr, gA, gB, l, dead); COUNT(5, sp); if (sp && l == 0) ctl[2 + (s & 1)] = (u32)s; }
      __syncthreads();
      u32x4 a[10];
      read_memf(memf, l, a);
      Side sd = load_side(ws, s, l);
      SCHED_FENCE();
      project2(a, wt, sidx, qk_w);
      __syncthreads();
      STAMPV(0, Rm);
      if (s >= 1 && s < NSEG - 1) schedule(ws, s, l, sd, Rm, Rp, dead);
    }
  } else {
    for (int s = 0; s < NSEG; ++s) {
      const u32 sb = cb + (u32)s * STEP_BYTES;
      if (s == 1) parmask = vote_result(ws, l);
      const u32 par = ((parmask >> (s >> 5)) & 1) * 8192;
      const int sn = s + 1 < NSEG ? s + 1 : s;
      __syncthreads();
      { u32 sp = gather_mem(ws, sb + par, wr, gA, gB, l, dead); COUNT(5, sp); if (sp && l == 0) ctl[2 + (s & 1)] = (u32)s; }
      __syncthreads();
      u32x4 a[10];
      read_memf(memf, l, a);
      SCHED_FENCE();
      u32x4 pk[4];
      f32x4 c0 = {0, 0, 0, 0}, c1 = {0, 0, 0, 0};
#pragma unroll
      for (int ks = 0; ks < 10; ++ks) {
        c0 = smfma(a[ks], wt[0][ks], c0, sidx);
        c1 = smfma(a[ks], wt[1][ks], c1, sidx);
        if (ks == KQPOS) {
          SCHED_FENCE();
#pragma unroll
          for (int i = 0; i < 4; ++i) pk[i] = ld16c(ws, l * 16 + i * 1024, kq_src + (u32)sn * (32 * 8192));
          SCHED_FENCE();
        }
      }
      *(__bf16*)(qk_w) = (__bf16)(c0[0] + c0[1]);
      *(__bf16*)(qk_w + QS) = (__bf16)(c0[2] + c0[3]);
      *(__bf16*)(qk_w + 8) = (__bf16)(c1[0] + c1[1]);
      *(__bf16*)(qk_w + 8 + QS) = (__bf16)(c1[2] + c1[3]);
      __syncthreads();
      unsigned char* kn = kq + ((s + 1) & 1) * 16384 + (w - 4) * 4096 + l * 16;
#pragma unroll
      for (int i = 0; i < 4; ++i) *(u32x4*)(kn + i * 1024) = pk[i];
      const int s2 = s + 2;
      if (s2 < NSEG) {
        if (seg_prod(s2)) { if (seen1 < seg_need(s2)) { __builtin_amdgcn_s_sleep(12); seen1 = wait_prog(ws, OFF_PQK + (u32)h * 128 + 4, 1, seg_need(s2), l, dead, 0); } }
        else { if (seen0 < seg_need(s2)) { __builtin_amdgcn_s_sleep(12); seen0 = wait_prog(ws, OFF_PQK + (u32)h * 128, 1, seg_need(s2), l, dead, 0); } }
      }
    }
  }
  __syncthreads();
  if (tid == 0) st4c(ws, 0u, OFF_PPX + (u32)h * 128, (u32)NSEG);
}

DEV u32x4 finish_mem(f32x4 acc) {
  auto x = __builtin_amdgcn_permlane32_swap(__float_as_uint(acc[0]), __float_as_uint(acc[2]), false, false);
  auto y = __builtin_amdgcn_permlane32_swap(__float_as_uint(acc[1]), __float_as_uint(acc[3]), false, false);
  float t0, t1;
  asm("s_nop 1\n\tv_add_f32_dpp %0, %2, %3 row_ror:8 row_mask:0xf bank_mask:0xf\n\tv_add_f32_dpp %1, %4, %5 row_ror:8 row_mask:0xf bank_mask:0xf"
      : "=&v"(t0), "=&v"(t1) : "v"(x[1]), "v"(x[0]), "v"(y[1]), "v"(y[0]));
  const u32 d = pk2s(t0, t1);
  auto s16 = __builtin_amdgcn_permlane16_swap(d, d, false, false);
  auto sa = __builtin_amdgcn_permlane32_swap(s16[0], s16[0], false, false);
  auto sb = __builtin_amdgcn_permlane32_swap(s16[1], s16[1], false, false);
  return (u32x4){sa[0], sa[1], sb[0], sb[1]};
}

DEV u32 second4(u32 a, u32 b, u32 c, u32 d) {
  const u32 h0 = max(a, b), l0 = min(a, b), h1 = max(c, d), l1 = min(c, d);
  return max(min(h0, h1), h0 > h1 ? l0 : l1);
}
DEV void value_role(const Params& p, rsrc_t ws, u32 cb, int v, unsigned char* lds) {
  const int tid = threadIdx.x, l = tid & 63, c16 = l & 15, g = l >> 4;
  const int w = __builtin_amdgcn_readfirstlane(tid >> 6);
  unsigned char* memf = lds;
  unsigned char* um = lds + 10240;
  u32* ctl = (u32*)(lds + 15360);
  unsigned char* w0s = lds + 16384;
  unsigned char* uxs = lds + 57344;
  const int sidx = (l & 1) ? 0xEEEE : 0x4444;
  const u32 um_w = (u32)((4 * w + (c16 >> 3)) * 128 + (c16 & 7) * 16 + g * 4);
  const int wr = (w + v) & 7;
  unsigned char* gA = memf + wr * 1024 + g * 256 + (l & 7) * 32 + ((l >> 3) & 1) * 16;
  unsigned char* gB = memf + 8192 + wr * 256 + ((l >> 2) & 7) * 32 + (l >> 5) * 16 + (l & 3) * 4;
  bool dead = false;
  b_unit(p, ws, v, lds);
  GSTAMP(4);
  if (w == 0) wait_prog(ws, OFF_BC + (u32)(v / 40) * 128, 1, 64, l, dead, 0);
  if (w == 1) wait_prog(ws, OFF_INITC, 8, 32, l, dead, 0, 64);
  __syncthreads();
  GSTAMP(5);
  if (w == 0) {
#pragma unroll
    for (int i = 0; i < 40; ++i) *(u32x4*)(w0s + i * 1024 + l * 16) = ld16c(ws, l * 16, OFF_BF + (u32)((v * 16 * 20 + i) * 1024));
    __syncthreads();
    GSTAMP(1);
    __syncthreads();
    GSTAMP(2);
    u32 parmask = __builtin_amdgcn_readfirstlane(ctl[4] | (ctl[5] << 1));
    const u32 offX = (u32)((c16 >> 3) * 512 + g * 128 + (c16 & 7) * 16);
    const u32 L = (u32)(v * 128 + (c16 & 7) * 16);
    const u32 pub = (c16 < 8 && !(g & 1)) ? (L >> 13) * 16384 + (u32)(g >> 1) * 8192 + (L & 8191) : 0x80000000u;
    u32 Rg = 0, d3 = 0, Rm = 0, dcur = v == 0 ? ((D1 + D2) << 10) | ((D3 + D4) << 20) : 0u;
    u32 a0 = D1, a1 = D1, a2 = D1, a3 = D1, b0 = D3, b1 = D3, b2 = D3, b3 = D3;
    const bool sidel = (g == 1 && c16 < 8);
    for (int s = 0; s < NSEG; ++s) {
      const u32 sb = cb + (u32)s * STEP_BYTES;
      if (s == 1) parmask = vote_result(ws, l);
      const u32 par = ((parmask >> (s >> 5)) & 1) * 8192;
      if (s >= 1) wait_until(Rg);
      __syncthreads();
      const u32x4 ga = ld16c(ws, (u32)l * 16, sb + par + (u32)wr * 1024);
      const u32 gb = ld4c(ws, (u32)l * 4, sb + par + 16384 + (u32)wr * 256);
      SCHED_FENCE();
      __builtin_amdgcn_s_sleep(1);
      Side sd = load_side(ws, s, l);
      u32 d1s = 0;
      if (v == 0 && s >= 1) d1s = ld4c(ws, (u32)(80 + (l & 31)) * 128, OFF_SIDE + (u32)(s - 1) * SIDE_STEP);
      u32x4 uf[20];
#pragma unroll
      for (int i = 0; i < 16; ++i) uf[i] = *(const u32x4*)(uxs + (((i >> 2) + PROT) & 3) * 4096 + (i & 3) * 1024 + offX);
      SCHED_FENCE();
      *(u32x4*)gA = ga;
      *(u32*)gB = gb;
      if (__builtin_expect(__any(umax3(umax3(ga.x, ga.y, ga.z), ga.w, gb) == SENT), 0)) { u32 sp = gather_mem(ws, sb + par, wr, gA, gB, l, dead); COUNT(5, sp + 1); }
      __syncthreads();
      STAMP(0);
      if (s == NSEG - 1) {
        __syncthreads();
        __syncthreads();
        break;
      }
      u32 so[5];
#pragma unroll
      for (int i = 0; i < 4; ++i) { const u32 qd = (u32)(i + PROT) & 3; so[i] = sb + par + (2 + (qd >> 1)) * 16384 + (qd & 1) * 4096; }
      so[4] = sb + par + 16384 + 2048;
      asm volatile("" : "+s"(so[0]), "+s"(so[1]), "+s"(so[2]), "+s"(so[3]), "+s"(so[4]));
      const u32 pubo = sidel ? OFF_SIDE + (u32)(s + 1) * SIDE_STEP + (u32)(v * 128 + c16 * 16) : pub + sb + STEP_BYTES;
      u32 Rp = 0;
      if (s >= 1) schedule(ws, s, l, sd, Rm, Rp, dead);
      const u32 sy = d3 | dcur;
      const int lead = (int)(Rp - rtime());
      bool ontime = s >= 1 && lead > 0, pfail = false;
      STAMPV(1, Rp);
      STAMP(2);
      SCHED_FENCE();
      if (s >= 1) wait_until(Rp);
      SCHED_FENCE();
      u32x4 pf[20];
#pragma unroll
      for (int i = 0; i < 20; ++i) { pf[i] = ld16c(ws, (u32)l * 16 + (i & 3) * 1024, so[i >> 2]); SCHED_FENCE(); }
      f32x4 acc = {0, 0, 0, 0};
#pragma unroll
      for (int i = 0; i < 20; ++i) {
        if (i == UMPOS) {
          __syncthreads();
#pragma unroll
          for (int q = 0; q < 4; ++q) uf[16 + q] = *(const u32x4*)(um + q * 1024 + l * 16);
          SCHED_FENCE();
        }
        acc = mfma(uf[i], pf[i], acc);
        SCHED_FENCE();
      }
      const u32 tl = rtime();
      u32x4 o = finish_mem(acc);
      if (__builtin_expect(__any(acc[0] != acc[0]), 0)) {
        u32 spins = 0;
#pragma nounroll
        while (!dead) {
          u32 m = 0;
#pragma unroll
          for (int i = 0; i < 20; ++i) m = umax3(umax3(m, pf[i].x, pf[i].y), pf[i].z, pf[i].w);
          if (__all(m != SENT)) break;
          ontime = false; pfail = true;
          if (++spins > SPIN_CAP) dead = true;
          asm volatile("" ::: "memory");
#pragma unroll
          for (int i = 0; i < 20; ++i) pf[i] = ld16c(ws, (u32)l * 16 + (i & 3) * 1024, so[i >> 2]);
        }
        STAMPV(4, spins);
        acc = (f32x4){0, 0, 0, 0};
#pragma unroll
        for (int i = 0; i < 20; ++i) acc = mfma(uf[i], pf[i], acc);
        o = finish_mem(acc);
      }
      u32 sx = 0;
      if ((int)(tl + DLS - Rm) > 0) sx = (tl + DL + 4) | 1u;
      if (__builtin_expect(!ontime, 0)) sx = (rtime() + DL) | 1u;
      if (sx == SENT) sx = 1u;
      o.x = sidel ? sx : o.x;
      o.y = sidel ? sy : o.y;
      st16c(ws, pubo, 0u, o);
      const u32 tp = fine_now();
      __syncthreads();
      d3 = (ontime && lead >= 28) ? tp - Rp : 0u;
      d3 = d3 < 1000u ? d3 : 0u;
      Rg = sx == 0 ? Rm + DV : tp + D4 + DV;
      STAMPV(7, d3);
      if (v == 0) {
        const u32 ma = (u32)wave_max(d1s == SENT ? 0 : (int)d1s), mb = s >= 1 ? (u32)wave_max((int)max(sd.a.y & 1023u, sd.b.y & 1023u)) : 0u;
        if (ma) { a3 = a2; a2 = a1; a1 = a0; a0 = ma; }
        if (pfail && s >= 2) { a0 = a1 = min(second4(a0, a1, a2, a3) + 8, 300u); }
        if (mb) { b3 = b2; b2 = b1; b1 = b0; b0 = mb; }
        dcur = (min(second4(a0, a1, a2, a3) + D2, 1000u) << 10) | (min(second4(b0, b1, b2, b3) + D4, 1000u) << 20);
      }
      STAMP(3);
      STAMPV(6, ((sy >> 10) & 1023u) * 1000 + ((sy >> 20) & 1023u));
    }
  } else {
    u32x8 wb[2][10];
#pragma unroll
    for (int t = 0; t < 2; ++t)
#pragma unroll
      for (int ks = 0; ks < 10; ++ks) {
        u32 o = OFF_BF + (u32)(((v * 16 + 2 * w + t) * 20 + 2 * ks) * 1024);
        wb[t][ks] = cat(ld16c(ws, l * 16, o), ld16c(ws, l * 16 + 1024, o));
      }
    __builtin_amdgcn_s_waitcnt(0x0F70);
    __syncthreads();
    if (w < 3) { u32 r = probe(ws, cb, 32 + v, w - 1, l); vote(p, w - 1, r, l); if (l == 0) ctl[3 + w] = r; }
    const bool pfw = w == 3 || w >= 5;
    const u32 uxo = (u32)((w == 3 ? 0 : w - 4) * 4096 + l * 16);
    u32 seen0 = 0, seen1 = 0;
    if (pfw) {
      seen0 = wait_prog(ws, OFF_PVU, 32, 2, l, dead, 0);
#pragma unroll
      for (int i = 0; i < 4; ++i) *(u32x4*)(uxs + uxo + i * 1024) = ld16c(ws, uxo + i * 1024, OFF_UX + (u32)(v * 16384));
    }
    __syncthreads();
    u32 parmask = __builtin_amdgcn_readfirstlane(ctl[4] | (ctl[5] << 1));
    if (w == 4) {
      for (int s = 0; s < NSEG; ++s) {
        const u32 sb = cb + (u32)s * STEP_BYTES;
        if (s == 1) parmask = vote_result(ws, l);
      const u32 par = ((parmask >> (s >> 5)) & 1) * 8192;
        u32x8 sb0[2], sb1[2];
        sb0[0] = cat(*(const u32x4*)(w0s + l * 16), *(const u32x4*)(w0s + 1024 + l * 16));
        sb1[0] = cat(*(const u32x4*)(w0s + 20480 + l * 16), *(const u32x4*)(w0s + 21504 + l * 16));
        __syncthreads();
        { u32 sp = gather_mem(ws, sb + par, wr, gA, gB, l, dead); COUNT(5, sp); }
        __syncthreads();
        u32x4 a[3];
        a[0] = *(const u32x4*)(memf + l * 16);
        a[1] = *(const u32x4*)(memf + 1024 + l * 16);
        f32x4 a0 = {0, 0, 0, 0}, a1 = {0, 0, 0, 0}, a2 = {0, 0, 0, 0}, a3 = {0, 0, 0, 0};
#pragma unroll
        for (int ks = 0; ks < 10; ++ks) {
          if (ks + 2 < 10) a[(ks + 2) % 3] = *(const u32x4*)(memf + (ks + 2) * 1024 + l * 16);
          if (ks + 1 < 10) {
            const int kn = ks + 1;
            sb0[kn & 1] = cat(*(const u32x4*)(w0s + (2 * kn) * 1024 + l * 16), *(const u32x4*)(w0s + (2 * kn + 1) * 1024 + l * 16));
            sb1[kn & 1] = cat(*(const u32x4*)(w0s + (20 + 2 * kn) * 1024 + l * 16), *(const u32x4*)(w0s + (21 + 2 * kn) * 1024 + l * 16));
          }
          SCHED_FENCE();
          a0 = smfma(a[ks % 3], wb[0][ks], a0, sidx);
          a1 = smfma(a[ks % 3], wb[1][ks], a1, sidx);
          a2 = smfma(a[ks % 3], sb0[ks & 1], a2, sidx);
          a3 = smfma(a[ks % 3], sb1[ks & 1], a3, sidx);
          SCHED_FENCE();
        }
        *(u32*)(um + um_w) = pk2(a0[0] + a0[1], a0[2] + a0[3]);
        *(u32*)(um + um_w + 256) = pk2(a1[0] + a1[1], a1[2] + a1[3]);
        *(u32*)(um + um_w - 2048) = pk2(a2[0] + a2[1], a2[2] + a2[3]);
        *(u32*)(um + um_w - 2048 + 256) = pk2(a3[0] + a3[1], a3[2] + a3[3]);
        __syncthreads();
        __syncthreads();
      }
    } else {
      for (int s = 0; s < NSEG; ++s) {
        const u32 sb = cb + (u32)s * STEP_BYTES;
        if (s == 1) parmask = vote_result(ws, l);
      const u32 par = ((parmask >> (s >> 5)) & 1) * 8192;
        __syncthreads();
        { u32 sp = gather_mem(ws, sb + par, wr, gA, gB, l, dead); COUNT(5, sp); }
        __syncthreads();
        u32x4 a[10];
        read_memf(memf, l, a);
        u32x4 pu[4];
        if (pfw && s + 1 < NSEG) {
#pragma unroll
          for (int i = 0; i < 4; ++i) pu[i] = ld16c(ws, uxo + i * 1024, OFF_UX + (u32)(((s + 1) * 80 + v) * 16384));
        }
        SCHED_FENCE();
        f32x4 a0 = {0, 0, 0, 0}, a1 = {0, 0, 0, 0};
#pragma unroll
        for (int ks = 0; ks < 10; ++ks) {
          a0 = smfma(a[ks], wb[0][ks], a0, sidx);
          a1 = smfma(a[ks], wb[1][ks], a1, sidx);
        }
        *(u32*)(um + um_w) = pk2(a0[0] + a0[1], a0[2] + a0[3]);
        *(u32*)(um + um_w + 256) = pk2(a1[0] + a1[1], a1[2] + a1[3]);
        if (w == 1) st4c(ws, l == 0 ? 0u : 0x80000000u, OFF_PUM + (u32)v * 128, (u32)s);
        __syncthreads();
        __syncthreads();
        if (pfw && s + 1 < NSEG) {
#pragma unroll
          for (int i = 0; i < 4; ++i) *(u32x4*)(uxs + uxo + i * 1024) = pu[i];
          const int sn = s + 2;
          if (sn < NSEG) {
            if (seg_prod(sn)) { if (seen1 < seg_need(sn)) seen1 = wait_prog(ws, OFF_PVU + 128, 32, seg_need(sn), l, dead, 0); }
            else { if (seen0 < seg_need(sn)) seen0 = wait_prog(ws, OFF_PVU, 32, seg_need(sn), l, dead, 0); }
          }
        }
        if (w == 1 || w == 2) {
#pragma unroll
          for (int i = 0; i < 2; ++i) {
            u32 o = (u32)(((w - 1) * 2 + i) * 1024 + l * 16);
            st16c(ws, o, OFF_UM + (u32)((s * 80 + v) * 4096), *(const u32x4*)(um + o));
          }
          VM_DRAIN();
        }
      }
    }
  }
  __syncthreads();
  if (tid == 0) st4c(ws, 0u, OFF_PUM + (u32)v * 128, (u32)NSEG);
}

DEV void glds16(const void* gsrc, u32 lds_dst) {
  u32 keep;
  asm volatile("s_mov_b32 %0, m0\n\ts_mov_b32 m0, %2\n\ts_nop 0\n\tglobal_load_lds_dwordx4 %1, off sc1\n\ts_mov_b32 m0, %0"
               : "=&s"(keep) : "v"(gsrc), "s"(lds_dst) : "memory");
}
template <int NST, bool ROWS, class Off, class Body, class Pub>
DEV void stream_tiles(const unsigned char* src, Off off, int n, int nbody, unsigned char* lds, Body body, Pub pub) {
  const int l = threadIdx.x & 63, w = __builtin_amdgcn_readfirstlane(threadIdx.x >> 6);
  const u32 lbase = (u32)(uintptr_t)lds + (u32)w * 5120;
  constexpr int QS_ = ROWS ? 64 : 1024;
  const unsigned char* g0 = ROWS ? src + (16 * (w >> 2) + (l & 15)) * 1280 + (w & 3) * 320 + (l >> 4) * 16 : src + w * 5120 + l * 16;
#pragma unroll
  for (int q = 0; q < 5; ++q) glds16(g0 + off(0) + q * QS_, lbase + q * 1024);
#pragma nounroll
  for (int i = 0; i < nbody; ++i) {
    if (i <= 3) VM_DRAIN(); else asm volatile("s_waitcnt vmcnt(%0)" :: "n"(NST) : "memory");
    RAW_BARRIER();
    if (i >= 1) pub(i <= 3 ? i : i - 1);
    const unsigned char* g = g0 + off(i + 1 < n ? i + 1 : n - 1);
    const u32 lb = lbase + (u32)((i + 1) & 1) * 40960;
    body(i, lds + (i & 1) * 40960, [&](int q) { glds16(g + q * QS_, lb + q * 1024); });
  }
  VM_DRAIN();
  RAW_BARRIER();
  pub(nbody);
}
template <int W, class Dma, class Use>
DEV void sweep40(const unsigned char* t, Dma dma, Use use) {
  u32x4 xb[W];
#pragma unroll
  for (int i = 0; i < W; ++i) xb[i] = *(const u32x4*)(t + ((i & 1) * 20 + (i >> 1)) * 1024);
  SCHED_FENCE();
#pragma unroll
  for (int i = 0; i < 40; ++i) {
    use(i, xb[i % W]);
    if (i + W < 40) xb[i % W] = *(const u32x4*)(t + (((i + W) & 1) * 20 + ((i + W) >> 1)) * 1024);
    if (i % 2 == 1 && i < 10) dma(i >> 1);
    SCHED_FENCE();
  }
}

DEV void qk_role(const Params& p, rsrc_t ws, int h, int par, unsigned char* lds, bool sig) {
  const int tid = threadIdx.x, l = tid & 63;
  const int w = __builtin_amdgcn_readfirstlane(tid >> 6);
  const int mat = w >> 2;
  u32x4 wt[2][20];
  load_qk(p, h, w, l, lds, wt, sig);
  GSTAMP(1);
  const u32 dst = (mat ? OFF_KX : OFF_QX) + (u32)((h * 8 + (w & 3)) * 1024);
  const int n = par ? NHELP : NSEG - NHELP;
  wait_init(ws);
  stream_tiles<2, false>(p.ws + OFF_XF, [&](int i) { return tile_seg(par, i) * 40960; }, n, n, lds,
    [&](int i, const unsigned char* t, auto dma) {
      f32x4 acc[2][2] = {};
      sweep40<8>(t + l * 16, dma, [&](int f, u32x4 xb) {
        acc[0][f & 1] = mfma(wt[0][f >> 1], xb, acc[0][f & 1]);
        acc[1][f & 1] = mfma(wt[1][f >> 1], xb, acc[1][f & 1]);
      });
#pragma unroll
      for (int tt = 0; tt < 2; ++tt) {
        u32x4 o = {pk2(acc[0][tt][0], acc[0][tt][1]), pk2(acc[0][tt][2], acc[0][tt][3]), pk2(acc[1][tt][0], acc[1][tt][1]), pk2(acc[1][tt][2], acc[1][tt][3])};
        st16c(ws, l * 16 + tt * 4096, dst + (u32)tile_seg(par, i) * (32 * 8192), o);
      }
    },
    [&](int k) {
      if (w == 0) {
        if (k == 2) { bool dead = false; wait_prog(ws, OFF_XFC + 64, 1, 16, l, dead, 0); }
        st4c(ws, l == 0 ? 0u : 0x80000000u, OFF_PQK + (u32)(h * 128 + par * 4), (u32)k);
      }
    });
}

DEV void vu_role(const Params& p, rsrc_t ws, int h, int par, unsigned char* lds, bool sig) {
  const int tid = threadIdx.x, l = tid & 63, c16 = l & 15, g = l >> 4;
  const int w = __builtin_amdgcn_readfirstlane(tid >> 6);
  u32x4 wv[20], wo[5][4];
  load_vu(p, h, w, l, lds, wv, wo, sig);
  GSTAMP(1);
  unsigned char* vs = lds + 81920;
  const u32 uoff = (u32)(((c16 >> 1) & 1) * 16384 + h * 512 + g * 128 + (2 * (c16 >> 2) + (c16 & 1)) * 16);
  const int n = par ? NHELP : NSEG - NHELP;
  wait_init(ws);
  stream_tiles<5, false>(p.ws + OFF_XF, [&](int i) { return tile_seg(par, i) * 40960; }, n, n + 1, lds,
    [&](int i, const unsigned char* t, auto dma) {
      u32x4 vf[2][4];
#pragma unroll
      for (int tt = 0; tt < 2; ++tt)
#pragma unroll
        for (int ks = 0; ks < 4; ++ks) vf[tt][ks] = *(const u32x4*)(vs + ((i + 1) & 1) * 8192 + ((tt * 4 + ks) * 64 + l) * 16);
      const u32 uo = i ? uoff + OFF_UX + (u32)((tile_seg(par, i - 1) * 80 + 10 * w) * 16384) : 0x80000000u;
      f32x4 av[2] = {}, cu[2] = {};
      u32x4 ou[2];
      sweep40<6>(t + l * 16, dma, [&](int f, u32x4 xb) {
        av[f & 1] = mfma(wv[f >> 1], xb, av[f & 1]);
        cu[f & 1] = mfma(vf[f & 1][(f >> 1) & 3], wo[f >> 3][(f >> 1) & 3], cu[f & 1]);
        if ((f & 7) == 7) {
          const int ct = f >> 3;
          u32x4 o = {pk2(cu[0][0], cu[0][1]), pk2(cu[0][2], cu[0][3]), pk2(cu[1][0], cu[1][1]), pk2(cu[1][2], cu[1][3])};
          cu[0] = (f32x4){0, 0, 0, 0}; cu[1] = (f32x4){0, 0, 0, 0};
          if (ct == 0) ou[0] = o;
          else { if (ct == 1) st16c(ws, uo, 0u, ou[0]); st16c(ws, uo, (u32)ct * 32768, o); }
        }
      });
#pragma unroll
      for (int tt = 0; tt < 2; ++tt) {
        u32x2 o = {pk2(av[tt][0], av[tt][1]), pk2(av[tt][2], av[tt][3])};
        *(u32x2*)(vs + (i & 1) * 8192 + ((tt * 4 + (w >> 1)) * 64 + l) * 16 + (w & 1) * 8) = o;
      }
    },
    [&](int k) {
      if (w == 0 && k >= 2) {
        st4c(ws, l == 0 ? 0u : 0x80000000u, OFF_PVU + (u32)(par * 128 + h * 4), (u32)(k - 1));
        if (k == 3) { bool dead = false; wait_prog(ws, OFF_XFC + 64, 1, 16, l, dead, 0); }
      }
    });
}

DEV f32x2 cmul(f32x2 a, f32x2 b) { return (f32x2){a.x * b.x - a.y * b.y, a.x * b.y + a.y * b.x}; }
DEV f32x2 cmulc(f32x2 a, f32x2 b) { return (f32x2){a.x * b.x + a.y * b.y, a.y * b.x - a.x * b.y}; }
template <bool INV> DEV f32x2 rot90(f32x2 a) { return INV ? (f32x2){-a.y, a.x} : (f32x2){a.y, -a.x}; }
template <bool INV> DEV void fft4(f32x2& a, f32x2& b, f32x2& c, f32x2& d) {
  f32x2 s0 = a + c, d0 = a - c, s1 = b + d, d1 = rot90<INV>(b - d);
  a = s0 + s1; b = d0 + d1; c = s0 - s1; d = d0 - d1;
}
template <bool INV> DEV f32x2 twc(f32x2 a, float c, float s) {
  return INV ? (f32x2){a.x * c - a.y * s, a.y * c + a.x * s} : (f32x2){a.x * c + a.y * s, a.y * c - a.x * s};
}
template <bool INV> DEV void fft16(f32x2 (&v)[16]) {
  constexpr float C1 = 0.92387953251128674f, S1 = 0.38268343236508977f, R = 0.70710678118654752f;
#pragma unroll
  for (int i = 0; i < 4; ++i) fft4<INV>(v[i], v[i + 4], v[i + 8], v[i + 12]);
  v[5] = twc<INV>(v[5], C1, S1);  v[9] = twc<INV>(v[9], R, R);     v[13] = twc<INV>(v[13], S1, C1);
  v[6] = twc<INV>(v[6], R, R);    v[10] = rot90<INV>(v[10]);       v[14] = twc<INV>(v[14], -R, R);
  v[7] = twc<INV>(v[7], S1, C1);  v[11] = twc<INV>(v[11], -R, R);  v[15] = twc<INV>(v[15], -C1, -S1);
#pragma unroll
  for (int k1 = 0; k1 < 4; ++k1) fft4<INV>(v[4 * k1], v[4 * k1 + 1], v[4 * k1 + 2], v[4 * k1 + 3]);
  f32x2 t;
#define SWP(a, b) t = v[a]; v[a] = v[b]; v[b] = t;
  SWP(1, 4) SWP(2, 8) SWP(3, 12) SWP(6, 9) SWP(7, 13) SWP(11, 14)
#undef SWP
}
template <bool INV> DEV void fft8(f32x2 (&v)[8]) {
  constexpr float R = 0.70710678118654752f;
  fft4<INV>(v[0], v[2], v[4], v[6]);
  fft4<INV>(v[1], v[3], v[5], v[7]);
  f32x2 o1 = twc<INV>(v[3], R, R), o2 = rot90<INV>(v[5]), o3 = twc<INV>(v[7], -R, R), o0 = v[1];
  f32x2 e0 = v[0], e1 = v[2], e2 = v[4], e3 = v[6];
  v[0] = e0 + o0; v[4] = e0 - o0; v[1] = e1 + o1; v[5] = e1 - o1; v[2] = e2 + o2; v[6] = e2 - o2; v[3] = e3 + o3; v[7] = e3 - o3;
}

DEV void fft_cols(const Params& p, rsrc_t ws, int q, unsigned char* lds) {
  const int tid = threadIdx.x, c = tid >> 7, t = tid & 127;
  f32x2* tw = (f32x2*)lds;
  f32x2* buf = (f32x2*)(lds + 16384) + c * (16 * 136);
  f32x2* fin = (f32x2*)(lds + 16384);
  __syncthreads();
  for (int m = tid; m < 2048; m += 512) { float sn, cs; sincospif((float)m * (1.0f / 1024.0f), &sn, &cs); tw[m] = (f32x2){cs, -sn}; }
  f32x2 keep[16];
  const int npass = (q == 0) ? 2 : 1;
  for (int pass = npass - 1; pass >= 0; --pass) {
    f32x2 v[16];
#pragma unroll
    for (int a = 0; a < 16; ++a)
      v[a] = __builtin_bit_cast(f32x2, ld8c(ws, (u32)(t * 16 + (c & 1) * 8 + (c >> 1) * 32768), OFF_FX + (u32)(2 * q * 32768 + a * 2048)));
    if (q == 0 && c == 0) {
#pragma unroll
      for (int a = 0; a < 16; ++a) v[a] = (f32x2){pass ? v[a].y : v[a].x, 0.f};
    }
    u32 wreg[16];
#pragma unroll
    for (int i = 0; i < 2; ++i)
#pragma unroll
      for (int kc = 0; kc < 8; ++kc) {
        const int pp = t + 128 * i, k = (pp >> 4) + 16 * (pp & 15) + 256 * kc;
        wreg[i * 8 + kc] = ld4c(ws, (u32)(k * 16 + c * 4), OFF_WT + (u32)(pass ? 80 : q) * 32768);
      }
    __syncthreads();
    fft16<false>(v);
#pragma unroll
    for (int ka = 0; ka < 16; ++ka) buf[ka * 136 + t] = ka ? cmul(v[ka], tw[t * ka]) : v[ka];
    __syncthreads();
    const int ka2 = t >> 3, c2 = t & 7;
#pragma unroll
    for (int b = 0; b < 16; ++b) v[b] = buf[ka2 * 136 + 8 * b + c2];
    fft16<false>(v);
#pragma unroll
    for (int kb = 0; kb < 16; ++kb) buf[ka2 * 136 + 8 * kb + c2] = kb ? cmul(v[kb], tw[16 * c2 * kb]) : v[kb];
    __syncthreads();
#pragma unroll
    for (int i = 0; i < 2; ++i) {
      const int pp = t + 128 * i;
      f32x2* e = buf + (pp >> 4) * 136 + 8 * (pp & 15);
      f32x2 u[8];
#pragma unroll
      for (int j = 0; j < 8; ++j) u[j] = e[j];
      fft8<false>(u);
#pragma unroll
      for (int kc = 0; kc < 8; ++kc) {
        const u32 wv = wreg[i * 8 + kc];
        const f32x2 wf = {__uint_as_float(wv << 16) * (1.0f / (2048.0f * 640.0f)), __uint_as_float(wv & 0xFFFF0000u) * (1.0f / (2048.0f * 640.0f))};
        u[kc] = cmul(u[kc], wf);
      }
      fft8<true>(u);
#pragma unroll
      for (int j = 0; j < 8; ++j) e[j] = u[j];
    }
    __syncthreads();
#pragma unroll
    for (int kb = 0; kb < 16; ++kb) { f32x2 x = buf[ka2 * 136 + 8 * kb + c2]; v[kb] = kb ? cmulc(x, tw[16 * c2 * kb]) : x; }
    fft16<true>(v);
#pragma unroll
    for (int b = 0; b < 16; ++b) buf[ka2 * 136 + 8 * b + c2] = v[b];
    __syncthreads();
#pragma unroll
    for (int ka = 0; ka < 16; ++ka) { f32x2 x = buf[ka * 136 + t]; v[ka] = ka ? cmulc(x, tw[t * ka]) : x; }
    fft16<true>(v);
    if (pass == 1) {
#pragma unroll
      for (int a = 0; a < 16; ++a) keep[a] = v[a];
    } else {
      if (q == 0 && c == 0) {
#pragma unroll
        for (int a = 0; a < 16; ++a) v[a].y = keep[a].x;
      }
      __syncthreads();
#pragma unroll
      for (int a = 0; a < 16; ++a) fin[(128 * a + t) * 4 + c] = v[a];
      __syncthreads();
    }
  }
#pragma unroll
  for (int i = 0; i < 4; ++i) {
    const int n = tid + 512 * i;
    const f32x4 x0 = *(const f32x4*)(fin + n * 4), x1 = *(const f32x4*)(fin + n * 4 + 2);
    u32x4 hi = {pk2(x0[0], x0[1]), pk2(x0[2], x0[3]), pk2(x1[0], x1[1]), pk2(x1[2], x1[3])};
    u32x4 lo = {pk2(x0[0] - __uint_as_float(hi[0] << 16), x0[1] - __uint_as_float(hi[0] & 0xFFFF0000u)),
                pk2(x0[2] - __uint_as_float(hi[1] << 16), x0[3] - __uint_as_float(hi[1] & 0xFFFF0000u)),
                pk2(x1[0] - __uint_as_float(hi[2] << 16), x1[1] - __uint_as_float(hi[2] & 0xFFFF0000u)),
                pk2(x1[2] - __uint_as_float(hi[3] << 16), x1[3] - __uint_as_float(hi[3] & 0xFFFF0000u))};
    const u32 o = (u32)((n >> 4) * 40960 + (q >> 2) * 1024 + ((q & 3) * 16 + (n & 15)) * 16);
    st16c(ws, o, OFF_FY, hi);
    st16c(ws, o + 20480, OFF_FY, lo);
  }
}

DEV void fw_task(const Params& p, rsrc_t ws, int k) {
  for (int q = k * 2560 + threadIdx.x; q < (k + 1) * 2560; q += 512) {
    const int which = q / 51200, r = q % 51200, tile = r / 1280, ks = (r % 1280) >> 6, ln = r & 63;
    u32x4 hi, lo;
    float val[8];
#pragma unroll
    for (int j = 0; j < 8; ++j) {
      const int aa = 16 * tile + (ln & 15), bb = 32 * ks + 8 * (ln >> 4) + j;
      const int kp = which ? bb : aa, d = which ? aa : bb;
      const int kk = (kp == 1) ? 320 : (kp >> 1);
      float sn, cs;
      sincospif((float)((kk * d) % 640) * (1.0f / 320.0f), &sn, &cs);
      float x = (kp & 1) && kp != 1 ? -sn : cs;
      if (which && kp > 1) x *= 2.0f;
      val[j] = x;
    }
#pragma unroll
    for (int j = 0; j < 4; ++j) { hi[j] = pk2(val[2 * j], val[2 * j + 1]); lo[j] = pk2lo(val[2 * j], val[2 * j + 1], hi[j]); }
    const u32 o = (which ? OFF_FW3 : OFF_FW1) + (u32)((tile * 40 + ks) * 1024 + ln * 16);
    st16c(ws, o, 0, hi);
    st16c(ws, o + 20480, 0, lo);
  }
  VM_DRAIN();
  __syncthreads();
  if (threadIdx.x == 0) __hip_atomic_fetch_add((u32*)(p.ws + OFF_FWC), 1u, __ATOMIC_RELAXED, __HIP_MEMORY_SCOPE_AGENT);
}
DEV void wt_task(const Params& p, rsrc_t ws, int k, unsigned char* lds) {
  const int tid = threadIdx.x;
  const rsrc_t rr = mk_rsrc(p.wr, 2048 * 642), ri = mk_rsrc(p.wi, 2048 * 642);
  for (int blk = 0; blk < 2; ++blk) {
    const int row0 = k * 128 + blk * 64;
    __syncthreads();
#pragma unroll
    for (int i = 0; i < 6; ++i) {
      const int c = tid + 512 * i;
      if (c < 2568) {
        *(u32x4*)(lds + c * 16) = ld16(rr, (u32)c * 16, (u32)row0 * 642);
        *(u32x4*)(lds + 41088 + c * 16) = ld16(ri, (u32)c * 16, (u32)row0 * 642);
      }
    }
    __syncthreads();
    const int r = tid & 63;
    const u16* a = (const u16*)lds + r * 321;
    for (int q = tid >> 6; q < 81; q += 8) {
      u32x4 o;
#pragma unroll
      for (int j = 0; j < 4; ++j) { const int col = q < 80 ? 4 * q + j : 320; o[j] = (u32)a[col] | ((u32)a[20544 + col] << 16); }
      st16c(ws, (u32)(row0 + r) * 16, OFF_WT + (u32)q * 32768, o);
    }
  }
  VM_DRAIN();
  __syncthreads();
  if (tid == 0) __hip_atomic_fetch_add((u32*)(p.ws + OFF_FWC), 1u, __ATOMIC_RELAXED, __HIP_MEMORY_SCOPE_AGENT);
}
DEV void wait_fw(rsrc_t ws) {
  if (threadIdx.x < 64) { bool dead = false; wait_prog(ws, OFF_FWC, 1, 56, threadIdx.x, dead, 1); }
  __syncthreads();
}
DEV void f1_task(const Params& p, rsrc_t ws, int type, int rg, unsigned char* lds) {
  const int tid = threadIdx.x, l = tid & 63, c16 = l & 15, g = l >> 4;
  const int w = __builtin_amdgcn_readfirstlane(tid >> 6);
  const int tile = type * 8 + w;
  wait_fw(ws);
  u32x4 wt[2][20];
#pragma unroll
  for (int i = 0; i < 2; ++i)
#pragma unroll
    for (int ks = 0; ks < 20; ++ks) { wt[i][ks] = ld16c(ws, l * 16, OFF_FW1 + (u32)((tile * 40 + i * 20 + ks) * 1024)); THROTTLE(ks); }
  __builtin_amdgcn_s_waitcnt(0x0F70);
  stream_tiles<2, true>((const unsigned char*)p.ax + rg * 4 * 40960, [](int i) { return i * 40960; }, 4, 4, lds,
    [&](int i, const unsigned char* t, auto dma) {
      f32x4 acc[2][2] = {};
      sweep40<8>(t + l * 16, dma, [&](int f, u32x4 xb) {
        acc[0][f & 1] = mfma(wt[0][f >> 1], xb, acc[0][f & 1]);
        acc[1][f & 1] = mfma(wt[1][f >> 1], xb, acc[1][f & 1]);
      });
#pragma unroll
      for (int tt = 0; tt < 2; ++tt) {
        f32x4 o = acc[0][tt] + acc[1][tt];
        st16c(ws, (u32)(g * 32768 + c16 * 16 + tt * 256), OFF_FX + (u32)(tile * 4 * 32768 + (rg * 4 + i) * 512), __builtin_bit_cast(u32x4, o));
      }
    },
    [&](int k) { if (k == 4 && tid == 0) __hip_atomic_fetch_add((u32*)(p.ws + OFF_F1C + type * 128), 1u, __ATOMIC_RELAXED, __HIP_MEMORY_SCOPE_AGENT); });
}
DEV void f3_task(const Params& p, rsrc_t ws, int type, int rg, unsigned char* lds) {
  const int tid = threadIdx.x, l = tid & 63, c16 = l & 15, g = l >> 4;
  const int w = __builtin_amdgcn_readfirstlane(tid >> 6);
  const int tile = type * 8 + w;
  wait_fw(ws);
  u32x4 wt[2][20];
#pragma unroll
  for (int i = 0; i < 2; ++i)
#pragma unroll
    for (int ks = 0; ks < 20; ++ks) { wt[i][ks] = ld16c(ws, l * 16, OFF_FW3 + (u32)((tile * 40 + i * 20 + ks) * 1024)); THROTTLE(ks); }
  __builtin_amdgcn_s_waitcnt(0x0F70);
  if (w == 0) {
    bool dead = false;
    wait_prog(ws, OFF_F2C, 1, 80, l, dead, 1);
    __builtin_amdgcn_fence(__ATOMIC_ACQUIRE, "agent");
    VM_DRAIN();
  }
  __syncthreads();
  u16* outp = p.out + (rg * 128 + c16) * 1280 + tile * 16 + 4 * g;
  stream_tiles<1, false>(p.ws + OFF_FY + rg * 8 * 40960, [](int i) { return i * 40960; }, 8, 8, lds,
    [&](int i, const unsigned char* t, auto dma) {
      f32x4 a0 = {0, 0, 0, 0}, a1 = {0, 0, 0, 0}, a2 = {0, 0, 0, 0};
      sweep40<10>(t + l * 16, dma, [&](int f, u32x4 y) {
        if (f & 1) a2 = mfma(wt[0][f >> 1], y, a2);
        else { a0 = mfma(wt[0][f >> 1], y, a0); a1 = mfma(wt[1][f >> 1], y, a1); }
      });
      f32x4 o = a0 + (a1 + a2);
      u32x2 ov = {pk2(o[0], o[1]), pk2(o[2], o[3])};
      *(u32x2*)(outp + i * 16 * 1280) = ov;
    },
    [&](int) {});
}

DEV void out_task(const Params& p, rsrc_t ws, int s, int cg, unsigned char* lds) {
  const int tid = threadIdx.x, l = tid & 63, c16 = l & 15, g = l >> 4;
  const int w = __builtin_amdgcn_readfirstlane(tid >> 6);
  const int G = 8 * cg + w;
  const int pp = (c16 >> 1) & 1, j = 2 * (c16 >> 2) + (c16 & 1);
  const u32 ox = (u32)(pp * 16384 + g * 128 + j * 16), om = (u32)(pp * 4096 + g * 128 + j * 16);
  bool dead = false;
  if (w == 0) wait_prog(ws, OFF_PVU + (u32)seg_prod(s) * 128, 32, seg_need(s), l, dead, 1);
  __syncthreads();
  u32x4 ua[32];
#pragma unroll
  for (int h = 0; h < 32; ++h) { ua[h] = ld16c(ws, ox + (h & 7) * 512, OFF_UX + (u32)((s * 80 + 2 * G) * 16384 + (h >> 3) * 4096)); THROTTLE(h); }
  SCHED_FENCE();
  if (w < 2) {
    wait_prog(ws, OFF_PPX + (u32)(cg + 5 * (s & 3)) * 128, 1, (u32)s, l, dead, 2);
    if (w == 0) wait_prog(ws, OFF_PPX, 32, (u32)s + 1, l, dead, 1, 128);
    else wait_prog(ws, OFF_PUM + (u32)cg * 2048, 16, (u32)s + 1, l, dead, 1, 128);
  }
  __syncthreads();
  u32x4 pr[10], ub[8];
#pragma unroll
  for (int i = 0; i < 8; ++i) pr[i] = ld16c(ws, tid * 16, OFF_PXX + (u32)(s * 65536 + i * 8192));
#pragma unroll
  for (int i = 0; i < 2; ++i) pr[8 + i] = ld16c(ws, tid * 16, OFF_PXM + (u32)(s * 16384 + i * 8192));
#pragma unroll
  for (int q = 0; q < 8; ++q) ub[q] = ld16c(ws, om + q * 512, OFF_UM + (u32)((s * 80 + 2 * G) * 4096));
#pragma unroll
  for (int i = 0; i < 10; ++i) *(u32x4*)(lds + (i * 512 + tid) * 16) = pr[i];
  __syncthreads();
  f32x4 acc[2] = {};
  {
    const unsigned char* px = lds + l * 16;
    const unsigned char* pm = lds + 65536 + (g * 16 + c16) * 16;
    auto rd = [&](int i) { return i < 64 ? *(const u32x4*)(px + i * 1024) : *(const u32x4*)(pm + (i & 1) * 8192 + ((i - 64) >> 1) * 1024); };
    u32x4 pb[12];
#pragma unroll
    for (int i = 0; i < 12; ++i) pb[i] = rd(i);
    SCHED_FENCE();
#pragma unroll
    for (int i = 0; i < 80; ++i) {
      acc[i & 1] = mfma(i < 64 ? ua[i >> 1] : ub[(i - 64) >> 1], pb[i % 12], acc[i & 1]);
      if (i + 12 < 80) pb[i % 12] = rd(i + 12);
      SCHED_FENCE();
    }
  }
#pragma unroll
  for (int tt = 0; tt < 2; ++tt) {
    u32x2 o = {pk2(acc[tt][0], acc[tt][1]), pk2(acc[tt][2], acc[tt][3])};
    *(u32x2*)(p.out + (32 * s + 16 * tt + c16) * 1280 + 640 + 16 * G + 4 * g) = o;
  }
}

DEV void prefetch_role(rsrc_t ws, int x, int k) {
  const int l = threadIdx.x & 63;
  const int w = __builtin_amdgcn_readfirstlane(threadIdx.x >> 6);
  if (w >= PFW) return;
  bool dead = false;
  u32 seen0 = 0, seen1 = 0;
  for (int s = 1; s < NSEG; ++s) {
    const u32 prog = wait_prog(ws, OFF_PUM + (u32)(x * 16 + k) * 128, 1, (u32)(s > PFD ? s - PFD : 0), l, dead, 1);
    if ((u32)s <= prog + 1) { s = (int)prog + 1; continue; }
    const u32 need = seg_need(s);
    if (seg_prod(s)) { if (seen1 < need) seen1 = wait_prog(ws, OFF_PVU + 128, 32, need, l, dead, 1); }
    else { if (seen0 < need) seen0 = wait_prog(ws, OFF_PVU, 32, need, l, dead, 1); }
#pragma nounroll
    for (int i = w; i < 14 * (8192 / (64 * PFSTR)); i += PFW) {
      const int u = k * 14 + i / (8192 / (64 * PFSTR));
      u32 o;
      if (u >= 40) o = ((u & 1) ? OFF_QX : OFF_KX) + (u32)((s * 32 + x * 8 + ((u - 40) >> 1)) * 8192);
      else o = OFF_UX + (u32)((s * 80 + x * 20) * 16384 + u * 8192);
      if (false) {}
      asm volatile("buffer_load_dword v255, %0, %1, %2 offen sc1\n\ts_waitcnt vmcnt(%3)"
                   :: "v"(l * PFSTR), "s"(ws), "s"(o + (u32)(i % (8192 / (64 * PFSTR))) * (64 * PFSTR)), "n"(PFQ) : "memory", "v255");
      __builtin_amdgcn_s_sleep(PFS);
    }
  }
  VM_DRAIN();
}

constexpr int T_SET = 56, T_F1 = 80, T_F2 = 80, T_F3 = 80, T_OUT = 320;
DEV int next_ticket(const Params& p, unsigned char* lds, u32 off) {
  u32* tk = (u32*)(lds + LDS_MAIN - 16);
  __syncthreads();
  if (threadIdx.x == 0) *tk = __hip_atomic_fetch_add((u32*)(p.ws + off), 1u, __ATOMIC_RELAXED, __HIP_MEMORY_SCOPE_AGENT);
  __syncthreads();
  return __builtin_amdgcn_readfirstlane(*tk);
}

DEV u32 claim_role(const Params& p, rsrc_t ws, unsigned char* lds) {
  u32* tk = (u32*)(lds + LDS_MAIN - 16);
  if (threadIdx.x < 64) {
    const int l = threadIdx.x;
    const u32 xcc = (u32)__builtin_amdgcn_s_getreg(20 | (3 << 11)) & 7u;
    u32 t = 0;
    if (l == 0) t = __hip_atomic_fetch_add((u32*)(p.ws + OFF_ROLE + xcc * 128), 1u, __ATOMIC_RELAXED, __HIP_MEMORY_SCOPE_AGENT);
    t = __builtin_amdgcn_readfirstlane(t);
    u32 pos = xcc * 32 + t;
    if (__builtin_expect(t >= 32, 0)) {
      u32 k = 0;
      if (l == 0) k = __hip_atomic_fetch_add((u32*)(p.ws + OFF_ROLE + 1024), 1u, __ATOMIC_RELAXED, __HIP_MEMORY_SCOPE_AGENT);
      k = __builtin_amdgcn_readfirstlane(k);
      u32 c = 0;
#pragma nounroll
      for (u32 spins = 0; spins < SPIN_CAP; ++spins) {
        asm volatile("" ::: "memory");
        c = ld4c(ws, (u32)(l & 7) * 128, OFF_ROLE);
        u32 sum = 0;
        for (int j = 0; j < 8; ++j) sum += (u32)__builtin_amdgcn_readlane(c, j);
        if (sum >= 256) break;
        __builtin_amdgcn_s_sleep(4);
      }
      pos = 255;
      for (int j = 0; j < 8; ++j) {
        const u32 cj = (u32)__builtin_amdgcn_readlane(c, j), fr = cj < 32 ? 32 - cj : 0;
        if (k < fr) { pos = (u32)j * 32 + cj + k; break; }
        k -= fr;
      }
    }
    if (l == 0) *tk = pos;
  }
  __syncthreads();
  return __builtin_amdgcn_readfirstlane(*tk);
}

__global__ __launch_bounds__(512, 2) void k_main(Params p, int mode) {
  extern __shared__ __attribute__((aligned(16))) unsigned char lds[];
  const rsrc_t ws = mk_rsrc(p.ws, WS_BYTES);
  const u32 cb = comm_base(p.ws);
  const int b = blockIdx.x;
  if (mode & 1) GSTAMP(0);
  if (b >= 4) {
    st16c(ws, cb + (u32)(b * 1024 + threadIdx.x) * 16, 0, (u32x4){SENT, SENT, SENT, SENT});
    st16c(ws, cb + (u32)(b * 1024 + 512 + threadIdx.x) * 16, 0, (u32x4){SENT, SENT, SENT, SENT});
  }
  if (b < 112) st16c(ws, OFF_SIDE + (u32)(b * 512 + threadIdx.x) * 16, 0, (u32x4){SENT, SENT, SENT, SENT});
  asm volatile("" ::: "memory");
  SCHED_FENCE();
  const u32 pos = claim_role(p, ws, lds);
  const int x = (int)(pos >> 5), slot = (int)(pos & 31);
  const int cidx = (x >= 4 || slot >= 28) ? 1000 : (slot < 8 ? x * 8 + slot : 32 + x * 20 + slot - 8);
  if (cidx < 112) {
    if (!(mode & 1)) return;
    if (cidx < 32) score_role(p, ws, cb, cidx, lds);
    else value_role(p, ws, cb, cidx - 32, lds);
    GSTAMP(3);
    return;
  }
  if (x < 4) {
    if (mode & 1) {
      init_part(p, ws, cb, x * 4 + slot - 28);
      prefetch_role(ws, x, slot - 28);
    }
    return;
  }
  if (x >= 4 && (mode & 2)) {
    const int h = (x - 4) * 8 + (slot >> 2);
    const bool bw = (slot & 1) && slot < 24;
    if (bw) b_unit(p, ws, 80 + (x - 4) * 12 + (slot >> 1), lds);
    if (slot & 2) vu_role(p, ws, h, slot & 1, lds, !bw);
    else qk_role(p, ws, h, slot & 1, lds, !bw);
    GSTAMP(2);
  }
  if (!(mode & 4)) return;
  const bool outonly = (slot & 3) == 3 && (slot & 4);
  if (!outonly) {
    int t = next_ticket(p, lds, OFF_TICKET) - T_SET;
    while (t < 0) { const int k = t + T_SET; if (k < 40) fw_task(p, ws, k); else wt_task(p, ws, k - 40, lds); t = next_ticket(p, lds, OFF_TICKET) - T_SET; }
    while (t < T_F1) { f1_task(p, ws, t % 5, t / 5, lds); t = next_ticket(p, lds, OFF_TICKET) - T_SET; }
    GSTAMP(4);
    while (t < T_F1 + T_F2) {
      const int q = t - T_F1;
      if (threadIdx.x < 64) { bool dead = false; wait_prog(ws, OFF_F1C + (u32)(q >> 4) * 128, 1, 16, threadIdx.x, dead, 1); }
      wait_fw(ws);
      fft_cols(p, ws, q, lds);
      VM_DRAIN();
      __syncthreads();
      if (threadIdx.x == 0) __hip_atomic_fetch_add((u32*)(p.ws + OFF_F2C), 1u, __ATOMIC_RELAXED, __HIP_MEMORY_SCOPE_AGENT);
      t = next_ticket(p, lds, OFF_TICKET) - T_SET;
    }
    GSTAMP(5);
    while (t < T_F1 + T_F2 + T_F3) { const int q = t - T_F1 - T_F2; f3_task(p, ws, q % 5, q / 5, lds); t = next_ticket(p, lds, OFF_TICKET) - T_SET; }
  }
  GSTAMP(6);
  for (int t = next_ticket(p, lds, OFF_TICKET2); t < T_OUT; t = next_ticket(p, lds, OFF_TICKET2)) out_task(p, ws, t / 5, t % 5, lds);
  GSTAMP(3);
}

__global__ __launch_bounds__(384) void k_zero(unsigned char* ws) {
  const rsrc_t r = mk_rsrc(ws, WS_BYTES);
  st16c(r, OFF_FLAGS + (u32)(blockIdx.x * 384 + threadIdx.x) * 16, 0, (u32x4){0, 0, 0, 0});
}

extern "C" void kernel_launch(void* const* d_in, const int* in_sizes, int n_in, void* d_out, int out_size, void* d_ws, size_t ws_size,
                              hipStream_t stream) {
  Params p;
  p.ax = (const u16*)d_in[0]; p.wr = (const u16*)d_in[1]; p.wi = (const u16*)d_in[2];
  p.x = (const u16*)d_in[3]; p.mem0 = (const u16*)d_in[4]; p.wq = (const u16*)d_in[5]; p.wk = (const u16*)d_in[6];
  p.wv = (const u16*)d_in[7]; p.wo = (const u16*)d_in[8];
  p.out = (u16*)d_out; p.ws = (unsigned char*)d_ws;
  (void)hipFuncSetAttribute((const void*)k_main, hipFuncAttributeMaxDynamicSharedMemorySize, LDS_MAIN);
  k_zero<<<8, 384, 0, stream>>>(p.ws);
  k_main<<<256, 512, LDS_MAIN, stream>>>(p, 7);
}
```

```cpp
#include <hip/hip_runtime.h>
#include <hip/hip_bf16.h>
#include <stdint.h>
#define BAR4
#define BULK1
#define SPREAD

typedef unsigned short u16;
typedef unsigned int u32;
typedef unsigned long long u64;
typedef __bf16 bf16x2 __attribute__((ext_vector_type(2)));
typedef __bf16 bf16x8 __attribute__((ext_vector_type(8)));
typedef __bf16 bf16x16 __attribute__((ext_vector_type(16)));
typedef float f32x2 __attribute__((ext_vector_type(2)));
typedef float f32x4 __attribute__((ext_vector_type(4)));
typedef u32 u32x2 __attribute__((ext_vector_type(2)));
typedef u32 u32x4 __attribute__((ext_vector_type(4)));
typedef u32 u32x8 __attribute__((ext_vector_type(8)));
typedef __amdgpu_buffer_rsrc_t rsrc_t;
#define DEV __device__ __forceinline__

constexpr int NSEG = 64;
constexpr u32 MB = 1u << 20;
constexpr u32 OFF_WF = 6 * MB;
constexpr u32 OFF_WOF = 21 * MB;
constexpr u32 OFF_BF = 26 * MB;
constexpr u32 OFF_XF = 51 * MB;
constexpr u32 OFF_AXF = 54 * MB;
constexpr u32 OFF_KX = 57 * MB;
constexpr u32 OFF_QX = 73 * MB;
constexpr u32 OFF_UX = 89 * MB;
constexpr u32 OFF_UM = 169 * MB;
constexpr u32 OFF_PXX = 189 * MB;
constexpr u32 OFF_PXM = 193 * MB;
constexpr u32 OFF_FX = 194 * MB;
constexpr u32 OFF_FY = 200 * MB;
constexpr u32 OFF_FLAGS = 206 * MB;
constexpr u32 OFF_FW1 = 208 * MB;
constexpr u32 OFF_FW3 = 210 * MB;
constexpr u32 OFF_SIDE = 212 * MB;
constexpr u32 SIDE_STEP = 112 * 128;
constexpr u32 OFF_WT = 213 * MB;
constexpr u32 WS_BYTES = 216 * MB;
constexpr u32 STEP_BYTES = 65536;
constexpr u32 SENT = 0xFFFFFFFFu;
constexpr u32 SPIN_CAP = 1u << 16;
constexpr u32 OFF_PQK = OFF_FLAGS + 0xA000;
constexpr u32 OFF_PVU = OFF_FLAGS + 0xB000;
constexpr u32 OFF_PPX = OFF_FLAGS;
constexpr u32 OFF_PUM = OFF_FLAGS + 0x1000;
constexpr u32 OFF_TICKET = OFF_FLAGS + 0xB400;
constexpr u32 OFF_F1C = OFF_FLAGS + 0xB480;
constexpr u32 OFF_F2C = OFF_FLAGS + 0xB800;
constexpr u32 OFF_FWC = OFF_FLAGS + 0xB880;
constexpr u32 OFF_TICKET2 = OFF_FLAGS + 0xB900;
constexpr u32 OFF_INITC = OFF_FLAGS + 0xBC00;
constexpr u32 OFF_ROLE = OFF_FLAGS + 0x9000;
constexpr u32 OFF_XFC = OFF_FLAGS + 0xBF00;
constexpr u32 OFF_BC = OFF_FLAGS + 0xBE00;
constexpr u32 FLAG_BYTES = 0xC000;
constexpr u32 OFF_VOTE = OFF_FLAGS + 0xBA00;
constexpr int LDS_MAIN = 97 * 1024;
#define HS0 4
#define HS1 20
constexpr int NHELP = (HS1 - HS0) / 2;
__host__ __device__ constexpr int seg_prod(int s) { return (s >= HS0 && s < HS1) ? (s & 1) : 0; }
__host__ __device__ constexpr u32 seg_need(int s) {
  return s < HS0 ? (u32)s + 1 : (s < HS1 ? ((s & 1) ? (u32)((s - HS0) >> 1) + 1 : (u32)(HS0 + ((s - HS0) >> 1)) + 1) : (u32)(s - NHELP) + 1);
}
__host__ __device__ constexpr int tile_seg(int par, int i) {
  return par ? HS0 + 2 * i + 1 : (i < HS0 ? i : (i < HS0 + NHELP ? HS0 + 2 * (i - HS0) : i + NHELP));
}

struct Params {
  const u16* ax; const u16* wr; const u16* wi;
  const u16* x; const u16* mem0; const u16* wq; const u16* wk; const u16* wv; const u16* wo;
  u16* out; unsigned char* ws;
};

DEV rsrc_t mk_rsrc(const void* p, u32 bytes) { return __builtin_amdgcn_make_buffer_rsrc((void*)p, 0, bytes, 0x00020000); }
DEV u32x4 ld16(rsrc_t r, u32 vo, u32 so) { return __builtin_amdgcn_raw_buffer_load_b128(r, vo, so, 0); }
DEV u32x4 ld16c(rsrc_t r, u32 vo, u32 so) { return __builtin_amdgcn_raw_buffer_load_b128(r, vo, so, 16); }
DEV u32x2 ld8c(rsrc_t r, u32 vo, u32 so) { return __builtin_amdgcn_raw_buffer_load_b64(r, vo, so, 16); }
DEV u32 ld4c(rsrc_t r, u32 vo, u32 so) { return __builtin_amdgcn_raw_buffer_load_b32(r, vo, so, 16); }
DEV void st16(rsrc_t r, u32 vo, u32 so, u32x4 v) { __builtin_amdgcn_raw_buffer_store_b128(v, r, vo + so, 0, 0); }
DEV void st16c(rsrc_t r, u32 vo, u32 so, u32x4 v) { __builtin_amdgcn_raw_buffer_store_b128(v, r, vo + so, 0, 16); }
DEV void st8c(rsrc_t r, u32 vo, u32 so, u32x2 v) { __builtin_amdgcn_raw_buffer_store_b64(v, r, vo, so, 16); }
DEV void st4c(rsrc_t r, u32 vo, u32 so, u32 v) { __builtin_amdgcn_raw_buffer_store_b32(v, r, vo, so, 16); }
DEV u32 pk2(float a, float b) { bf16x2 v = {(__bf16)a, (__bf16)b}; return __builtin_bit_cast(u32, v); }
DEV u32 pk2s(float a, float b) { u32 v = pk2(a, b); return v < 0xFFFFFFFEu ? v : 0xFFFFFFFEu; }
DEV u32 pk2lo(float a, float b, u32 hi) { return pk2(a - __uint_as_float(hi << 16), b - __uint_as_float(hi & 0xFFFF0000u)); }
DEV f32x4 mfma(u32x4 a, u32x4 b, f32x4 c) {
  return __builtin_amdgcn_mfma_f32_16x16x32_bf16(__builtin_bit_cast(bf16x8, a), __builtin_bit_cast(bf16x8, b), c, 0, 0, 0);
}
DEV f32x4 smfma(u32x4 a, u32x8 b, f32x4 c, int idx) {
  return __builtin_amdgcn_smfmac_f32_16x16x64_bf16(__builtin_bit_cast(bf16x8, a), __builtin_bit_cast(bf16x16, b), c, idx, 0, 0);
}
DEV u32x8 cat(u32x4 a, u32x4 b) { return __builtin_shufflevector(a, b, 0, 1, 2, 3, 4, 5, 6, 7); }
DEV u32 comm_base(const unsigned char* ws) { return (u32)((2 * MB - ((uintptr_t)ws & (2 * MB - 1))) & (2 * MB - 1)); }
DEV u64 stamp_cyc() { u64 t; asm volatile("s_memtime %0\n\ts_waitcnt lgkmcnt(0)" : "=s"(t) :: "memory"); return t; }
DEV u32 umax3(u32 a, u32 b, u32 c) { u32 m = a > b ? a : b; return m > c ? m : c; }
DEV float fmx(float a, float b) { return __builtin_elementwise_maximum(a, b); }
#define SCHED_FENCE() __builtin_amdgcn_sched_barrier(0)
#define VM_DRAIN() asm volatile("s_waitcnt vmcnt(0)" ::: "memory")
#define RAW_BARRIER() do { asm volatile("s_waitcnt lgkmcnt(0)" ::: "memory"); __builtin_amdgcn_s_barrier(); asm volatile("" ::: "memory"); } while (0)

#define STAMP(ev)
#define STAMPV(ev, val)
#define COUNT(ev, val)
#define GSTAMP(slot)

typedef short s16x4 __attribute__((ext_vector_type(4)));
#define LAS __attribute__((address_space(3)))
DEV u32x4 tr_frag(const unsigned char* a0, const unsigned char* a1) {
  const u32x2 x = __builtin_bit_cast(u32x2, __builtin_amdgcn_ds_read_tr16_b64_v4i16((LAS s16x4*)a0));
  const u32x2 y = __builtin_bit_cast(u32x2, __builtin_amdgcn_ds_read_tr16_b64_v4i16((LAS s16x4*)a1));
  return (u32x4){x.x, x.y, y.x, y.y};
}
DEV float dpp_ror8(float x) { return __uint_as_float(__builtin_amdgcn_update_dpp(0, (int)__float_as_uint(x), 0x128, 0xF, 0xF, true)); }
DEV float red_max_g(float m) {
  auto a = __builtin_amdgcn_permlane16_swap(__float_as_uint(m), __float_as_uint(m), false, false);
  m = fmx(__uint_as_float(a[0]), __uint_as_float(a[1]));
  auto b = __builtin_amdgcn_permlane32_swap(__float_as_uint(m), __float_as_uint(m), false, false);
  return fmx(__uint_as_float(b[0]), __uint_as_float(b[1]));
}
DEV float red_sum_g(float m) {
  auto a = __builtin_amdgcn_permlane16_swap(__float_as_uint(m), __float_as_uint(m), false, false);
  m = __uint_as_float(a[0]) + __uint_as_float(a[1]);
  auto b = __builtin_amdgcn_permlane32_swap(__float_as_uint(m), __float_as_uint(m), false, false);
  return __uint_as_float(b[0]) + __uint_as_float(b[1]);
}
DEV float red_max(float m) { return red_max_g(fmx(m, dpp_ror8(m))); }
DEV float red_sum(float m) { return red_sum_g(m + dpp_ror8(m)); }

#define NPROBE 3
DEV u32 probe(rsrc_t ws, u32 cb, int idx, int page, int l) {
  const u32 vo = (l == 0) ? 0u : 0x80000000u;
  const u32 so = cb + (u32)(page * 32 + 30) * STEP_BYTES + (u32)(2 + (idx >> 6)) * 16384 + (u32)(idx & 63) * 128;
  u32 b0 = ~0u, b1 = ~0u;
#pragma nounroll
  for (int rep = 0; rep < 2 * NPROBE; ++rep) {
    u64 t0 = stamp_cyc();
    st4c(ws, vo, so + (u32)(rep & 1) * 8192, SENT);
    VM_DRAIN();
    u64 t1 = stamp_cyc();
    u32 d = (u32)(t1 - t0);
    if (rep & 1) b1 = d < b1 ? d : b1; else b0 = d < b0 ? d : b0;
  }
  return b1 < b0 ? 1u : 0u;
}

DEV u32 xcc_group() { return ((u32)__builtin_amdgcn_s_getreg(20 | (3 << 11)) >> 2) & 3u; }
DEV void vote(const Params& p, int page, u32 r, int l) {
  if (l == 0) __hip_atomic_fetch_add((u32*)(p.ws + OFF_VOTE + (xcc_group() * 2 + page) * 64), r + 0x10000u, __ATOMIC_RELAXED, __HIP_MEMORY_SCOPE_AGENT);
}
DEV u32 vote_result(rsrc_t ws, int l) {
  const u32 x = ld4c(ws, (u32)(l & 1) * 64, OFF_VOTE + xcc_group() * 128);
  const u32 m = (2 * (x & 0xFFFFu) > (x >> 16)) ? 1u : 0u;
  return (u32)__builtin_amdgcn_readlane(m, 0) | ((u32)__builtin_amdgcn_readlane(m, 1) << 1);
}
#define D1 140
#define D2 12
#define D3 60
#define D4 12
#define DV 15
#define DX 25
#define DL 5
#define PFD 2
#define DLS 16
#define THR 6
#define THR_S2(x) #x
#define THR_S(x) THR_S2(x)
#define THROTTLE(i) asm volatile("s_waitcnt vmcnt(" THR_S(THR) ")" ::: "memory")
#define UMPOS 0
#define SDQ 0
#define SDV 0
#define XPERM 0x76325410u
#define XCCMASK 7u
#define PSLEEP 0
#define PFW 1
#define PFQ 4
#define PFS 2
#define PFSTR 128
#define PROT v
#define KQPOS 0
#define DA 12
DEV u32 rtime() { return (u32)__builtin_amdgcn_s_memrealtime(); }
DEV void wait_until(u32 T) {
  u32 r = rtime();
  if ((int)(T - r) < 28) {
#pragma nounroll
    for (int i = 0; i < 64 && (int)(T - r) > 0; ++i) r = rtime();
    return;
  }
#pragma nounroll
  for (int i = 0; i < 4096 && (int)(T - r) > 35; ++i) r = rtime();
  u32 n;
  asm volatile(
    "s_memrealtime s[80:81]\n\ts_nop 2\n\ts_memrealtime s[82:83]\n\ts_nop 2\n\ts_memrealtime s[84:85]\n\ts_nop 2\n\ts_memrealtime s[86:87]\n\ts_nop 2\n\t"
    "s_memrealtime s[88:89]\n\ts_nop 2\n\ts_memrealtime s[90:91]\n\ts_nop 2\n\ts_memrealtime s[92:93]\n\ts_nop 2\n\ts_memrealtime s[94:95]\n\ts_waitcnt lgkmcnt(0)\n\t"
    "s_mov_b32 s81, 0\n\t"
    "s_cmp_eq_u32 s82, s80\n\ts_addc_u32 s81, s81, 0\n\t"
    "s_cmp_eq_u32 s84, s80\n\ts_addc_u32 s81, s81, 0\n\t"
    "s_cmp_eq_u32 s86, s80\n\ts_addc_u32 s81, s81, 0\n\t"
    "s_cmp_eq_u32 s88, s80\n\ts_addc_u32 s81, s81, 0\n\t"
    "s_cmp_eq_u32 s90, s80\n\ts_addc_u32 s81, s81, 0\n\t"
    "s_cmp_eq_u32 s92, s80\n\ts_addc_u32 s81, s81, 0\n\t"
    "s_cmp_eq_u32 s94, s80\n\ts_addc_u32 s81, s81, 0\n\t"
    "s_sub_u32 %0, %1, s80\n\t"
    "s_mul_i32 %0, %0, 3\n\t"
    "s_lshl1_add_u32 %0, s81, %0\n\t"
    "s_sub_i32 %0, %0, 64\n\t"
    "s_max_i32 %0, %0, 0\n\t"
    "s_min_i32 %0, %0, 63\n\t"
    "s_bitcmp1_b32 %0, 0\n\ts_cbranch_scc0 1f\n\ts_nop 5\n\t1:\n\t"
    "s_bitcmp1_b32 %0, 1\n\ts_cbranch_scc0 2f\n\ts_nop 7\n\t2:\n\t"
    "s_bitcmp1_b32 %0, 2\n\ts_cbranch_scc0 3f\n\ts_nop 11\n\t3:\n\t"
    "s_bitcmp1_b32 %0, 3\n\ts_cbranch_scc0 4f\n\ts_nop 15\n\ts_nop 3\n\t4:\n\t"
    "s_bitcmp1_b32 %0, 4\n\ts_cbranch_scc0 5f\n\ts_nop 15\n\ts_nop 15\n\ts_nop 3\n\t5:\n\t"
    "s_bitcmp1_b32 %0, 5\n\ts_cbranch_scc0 6f\n\ts_nop 15\n\ts_nop 15\n\ts_nop 15\n\ts_nop 15\n\ts_nop 3\n\t6:\n\t"
    : "=&s"(n) : "s"(T)
    : "scc", "memory", "s80", "s81", "s82", "s83", "s84", "s85", "s86", "s87", "s88", "s89", "s90", "s91", "s92", "s93", "s94", "s95");
}
template <int CTRL> DEV int dpp_i(int x) { return __builtin_amdgcn_update_dpp(x, x, CTRL, 0xF, 0xF, false); }
DEV int row_max(int d) {
  d = max(d, dpp_i<0x121>(d));
  d = max(d, dpp_i<0x122>(d));
  d = max(d, dpp_i<0x124>(d));
  return max(d, dpp_i<0x128>(d));
}
DEV int wave_max(int d) {
  d = row_max(d);
  return max(max(__builtin_amdgcn_readlane(d, 0), __builtin_amdgcn_readlane(d, 16)), max(__builtin_amdgcn_readlane(d, 32), __builtin_amdgcn_readlane(d, 48)));
}
DEV u32 fine_now() {
  u32 n;
  asm volatile(
    "s_memrealtime s[80:81]\n\ts_nop 2\n\ts_memrealtime s[82:83]\n\ts_nop 2\n\ts_memrealtime s[84:85]\n\ts_nop 2\n\ts_memrealtime s[86:87]\n\ts_nop 2\n\t"
    "s_memrealtime s[88:89]\n\ts_nop 2\n\ts_memrealtime s[90:91]\n\ts_nop 2\n\ts_memrealtime s[92:93]\n\ts_waitcnt lgkmcnt(0)\n\t"
    "s_mov_b32 s81, 0\n\t"
    "s_cmp_eq_u32 s82, s80\n\ts_addc_u32 s81, s81, 0\n\t"
    "s_cmp_eq_u32 s84, s80\n\ts_addc_u32 s81, s81, 0\n\t"
    "s_cmp_eq_u32 s86, s80\n\ts_addc_u32 s81, s81, 0\n\t"
    "s_cmp_eq_u32 s88, s80\n\ts_addc_u32 s81, s81, 0\n\t"
    "s_cmp_eq_u32 s90, s80\n\ts_addc_u32 s81, s81, 0\n\t"
    "s_cmp_eq_u32 s92, s80\n\ts_addc_u32 s81, s81, 0\n\t"
    "s_mul_i32 s81, s81, 11\n\t"
    "s_lshr_b32 s81, s81, 4\n\t"
    "s_add_u32 %0, s80, 4\n\t"
    "s_sub_u32 %0, %0, s81\n\t"
    : "=&s"(n) :
    : "scc", "memory", "s80", "s81", "s82", "s83", "s84", "s85", "s86", "s87", "s88", "s89", "s90", "s91", "s92", "s93");
  return n;
}
struct Side { u32x2 a, b; };
DEV Side load_side(rsrc_t ws, int s, int l) {
  Side t;
  t.a = ld8c(ws, (u32)l * 128, OFF_SIDE + (u32)s * SIDE_STEP);
  t.b = ld8c(ws, (u32)(64 + (l & 15)) * 128, OFF_SIDE + (u32)s * SIDE_STEP);
  return t;
}
DEV void schedule(rsrc_t ws, int s, int l, Side& t, u32& Rm, u32& Rp, bool& dead) {
  if (__builtin_expect(__any(umax3(t.a.x, t.a.y, t.b.x > t.b.y ? t.b.x : t.b.y) == SENT), 0)) {
#pragma nounroll
    for (u32 spins = 0; !dead; ++spins) {
      asm volatile("" ::: "memory");
      t = load_side(ws, s, l);
      if (__all(umax3(t.a.x, t.a.y, t.b.x > t.b.y ? t.b.x : t.b.y) != SENT)) break;
      if (spins > SPIN_CAP) dead = true;
    }
  }
  u32 B = Rm;
  if (__builtin_expect(__any((t.a.x | t.b.x) != 0), 0)) {
    const u32 ref = rtime();
    const int dx = t.a.x ? (int)(t.a.x - ref) : -(1 << 30), dy = t.b.x ? (int)(t.b.x - ref) : -(1 << 30);
    B = ref + (u32)wave_max(max(dx, dy)) + D4 + DX;
  }
  const u32 d = __builtin_amdgcn_readfirstlane(t.a.y);
  Rp = B + ((d >> 10) & 1023u);
  Rm = Rp + ((d >> 20) & 1023u);
}

DEV u32 gather_mem(rsrc_t ws, u32 sbp, int wr, unsigned char* dA, unsigned char* dB, int l, bool& dead) {
  u32x4 a = ld16c(ws, (u32)l * 16, sbp + (u32)wr * 1024);
  u32 b = ld4c(ws, (u32)l * 4, sbp + 16384 + (u32)wr * 256);
  SCHED_FENCE();
  *(u32x4*)dA = a;
  *(u32*)dB = b;
  u32 m = umax3(umax3(a.x, a.y, a.z), a.w, b);
  u32 spins = 0;
  if (__builtin_expect(__any(m == SENT), 0)) {
#pragma nounroll
    while (!dead) {
      ++spins;
      asm volatile("" ::: "memory");
      a = ld16c(ws, (u32)l * 16, sbp + (u32)wr * 1024);
      b = ld4c(ws, (u32)l * 4, sbp + 16384 + (u32)wr * 256);
      m = umax3(umax3(a.x, a.y, a.z), a.w, b);
      if (__all(m != SENT)) break;
      if (spins > SPIN_CAP) dead = true;
    }
    *(u32x4*)dA = a;
    *(u32*)dB = b;
  }
  return spins;
}
DEV u32 wait_prog(rsrc_t ws, u32 off, int n, u32 need, int l, bool& dead, int slp, u32 stride = 4) {
  u32 mn = 0;
#pragma nounroll
  for (u32 spins = 0;; ++spins) {
    asm volatile("" ::: "memory");
    const u32 x = ld4c(ws, (u32)(l < n ? l : 0) * stride, off);
    int d = -(int)x;
    d = row_max(d);
    mn = (u32)(-max(__builtin_amdgcn_readlane(d, 0), __builtin_amdgcn_readlane(d, 16)));
    if (mn >= need || dead) break;
    if (spins > SPIN_CAP) { dead = true; break; }
    if (slp == 2) __builtin_amdgcn_s_sleep(32); else if (slp) __builtin_amdgcn_s_sleep(6); else __builtin_amdgcn_s_sleep(1);
  }
  asm volatile("" ::: "memory");
  return mn;
}

DEV void init_signal(const Params& p, bool on) {
  if (on && threadIdx.x == 0) __hip_atomic_fetch_add((u32*)(p.ws + OFF_INITC + (blockIdx.x & 7) * 64), 1u, __ATOMIC_RELAXED, __HIP_MEMORY_SCOPE_AGENT);
}
DEV int wrow(int r) { return r * 288 + (r >> 3) * 128; }
DEV void load_qk(const Params& p, int h, int w, int l, unsigned char* lds, u32x4 (&wt)[2][20], bool sig) {
  const rsrc_t src = mk_rsrc((w >> 2) ? p.wk : p.wq, 640 * 8192);
  u32x4 st[40];
#pragma unroll
  for (int i = 0; i < 40; ++i)
    st[i] = ld16(src, (u32)((l >> 4) * 8192 + (l & 15) * 16), (u32)((32 * (w & 3) + (i >> 3) * 128 + (i & 7) * 4) * 8192 + h * 256));
  unsigned char* img = lds + (w >> 2) * 38912;
  unsigned char* wp = img + wrow(32 * (w & 3)) + (l >> 4) * 288 + (l & 15) * 16;
  const unsigned char* rp = img + wrow(8 * (l >> 4)) + ((l >> 2) & 3) * 288 + (w & 3) * 64 + (l & 3) * 8;
#pragma unroll
  for (int r = 0; r < 5; ++r) {
#pragma unroll
    for (int i = 0; i < 8; ++i) *(u32x4*)(wp + wrow(4 * i)) = st[r * 8 + i];
    __syncthreads();
    if (r == 0) init_signal(p, sig);
#pragma unroll
    for (int kk = 0; kk < 4; ++kk)
#pragma unroll
      for (int i = 0; i < 2; ++i) wt[i][4 * r + kk] = tr_frag(rp + wrow(32 * kk) + i * 32, rp + wrow(32 * kk) + 4 * 288 + i * 32);
    __syncthreads();
  }
}
DEV void load_vu(const Params& p, int h, int w, int l, unsigned char* lds, u32x4 (&wv)[20], u32x4 (&wo)[5][4], bool sig) {
  const rsrc_t sv = mk_rsrc(p.wv, 640 * 8192), so = mk_rsrc(p.wo, 640 * 8192);
  u32x4 st[40];
#pragma unroll
  for (int i = 0; i < 20; ++i)
    st[i] = ld16(sv, (u32)((l >> 4) * 8192 + (l & 15) * 16), (u32)((40 * w + (i / 10) * 320 + (i % 10) * 4) * 8192 + h * 256));
#pragma unroll
  for (int i = 0; i < 20; ++i) st[20 + i] = ld16(so, (u32)l * 16, (u32)(h * 163840 + (w * 640 + (i / 10) * 5120 + (i % 10) * 64) * 16));
  unsigned char* wp = lds + wrow(40 * w) + (l >> 4) * 288 + (l & 15) * 16;
  const unsigned char* rp = lds + wrow(8 * (l >> 4)) + ((l >> 2) & 3) * 288 + w * 32 + (l & 3) * 8;
#pragma unroll
  for (int r = 0; r < 2; ++r) {
#pragma unroll
    for (int i = 0; i < 10; ++i) *(u32x4*)(wp + wrow(4 * i)) = st[r * 10 + i];
    __syncthreads();
    if (r == 0) init_signal(p, sig);
#pragma unroll
    for (int kk = 0; kk < 10; ++kk) wv[10 * r + kk] = tr_frag(rp + wrow(32 * kk), rp + wrow(32 * kk) + 4 * 288);
    __syncthreads();
  }
  const unsigned char* ro = lds + (4 * (l >> 4) + ((l >> 2) & 3)) * 1312 + w * 160 + (l & 3) * 8;
#pragma unroll
  for (int r = 0; r < 2; ++r) {
#pragma unroll
    for (int i = 0; i < 10; ++i) {
      const int c = (w * 10 + i) * 64 + l;
      *(u32x4*)(lds + (c / 80) * 1312 + (c % 80) * 16) = st[20 + r * 10 + i];
    }
    __syncthreads();
#pragma unroll
    for (int kk = 0; kk < 2; ++kk)
#pragma unroll
      for (int ct = 0; ct < 5; ++ct) wo[ct][2 * r + kk] = tr_frag(ro + 32 * kk * 1312 + ct * 32, ro + (32 * kk + 16) * 1312 + ct * 32);
    __syncthreads();
  }
}

DEV void count_done(const Params& p, u32 off) {
  VM_DRAIN();
  __syncthreads();
  if (threadIdx.x == 0) __hip_atomic_fetch_add((u32*)(p.ws + off), 1u, __ATOMIC_RELAXED, __HIP_MEMORY_SCOPE_AGENT);
}
DEV void wait_init(rsrc_t ws) {
  if (threadIdx.x < 64) { bool dead = false; wait_prog(ws, OFF_XFC, 1, 16, threadIdx.x, dead, 0); }
  __syncthreads();
}
DEV void b_unit(const Params& p, rsrc_t ws, int u, unsigned char* lds) {
  const int tid = threadIdx.x, l = tid & 63, c16 = l & 15, g = l >> 4;
  const int w = __builtin_amdgcn_readfirstlane(tid >> 6);
  const int h = u >> 2, dh = (u >> 1) & 1, ch = u & 1;
  const rsrc_t rv = mk_rsrc(p.wv, 640 * 8192), ro = mk_rsrc(p.wo, 640 * 8192);
  u32x4 wo_r[10];
#pragma unroll
  for (int i = 0; i < 10; ++i) {
    const int c = tid + 512 * i, e = c / 40, cc = c % 40;
    wo_r[i] = ld16(ro, (u32)(e * 1280 + cc * 16), (u32)(h * 163840 + ch * 640));
  }
  u32x4 a[3][2][4];
#pragma unroll
  for (int i = 0; i < 3; ++i) {
    const int bl = (w & 3) + 4 * i;
    const u32 so = bl < 10 ? (u32)((32 * (10 * dh + bl)) * 8192 + h * 256) : 0x80000000u;
#pragma unroll
    for (int t = 0; t < 2; ++t)
#pragma unroll
      for (int ks = 0; ks < 4; ++ks)
        a[i][t][ks] = ld16(rv, (u32)((8 * (c16 >> 2) + (c16 & 3) + 4 * t) * 8192 + g * 16 + ks * 64), so);
  }
  SCHED_FENCE();
#pragma unroll
  for (int i = 0; i < 10; ++i) {
    const int c = tid + 512 * i, e = c / 40, cc = c % 40;
    *(u32x4*)(lds + e * 672 + (e >> 3) * 128 + cc * 16) = wo_r[i];
  }
  __syncthreads();
  init_signal(p, true);
  const u32 vo = (u32)(((c16 >> 1) & 1) * (16 * 20 * 1024) + (16 * g + 8 * (h & 1) + 2 * (c16 >> 2) + (c16 & 1)) * 16);
#pragma nounroll
  for (int half = 0; half < 2; ++half) {
    u32x4 bf[5][4];
    const unsigned char* rp = lds + (8 * g + ((l >> 2) & 3)) * 672 + g * 128 + half * 320 + (w >> 2) * 160 + (l & 3) * 8;
#pragma unroll
    for (int ct = 0; ct < 5; ++ct)
#pragma unroll
      for (int ks = 0; ks < 4; ++ks) bf[ct][ks] = tr_frag(rp + ks * (32 * 672 + 512) + ct * 32, rp + ks * (32 * 672 + 512) + 4 * 672 + ct * 32);
#pragma unroll
    for (int i = 0; i < 3; ++i) {
      const int bl = (w & 3) + 4 * i;
      if (bl < 10) {
#pragma unroll
        for (int ct = 0; ct < 5; ++ct) {
          f32x4 c0 = {0, 0, 0, 0}, c1 = {0, 0, 0, 0};
#pragma unroll
          for (int ks = 0; ks < 4; ++ks) { c0 = mfma(a[i][0][ks], bf[ct][ks], c0); c1 = mfma(a[i][1][ks], bf[ct][ks], c1); }
          u32x4 o = {pk2(c0[0], c0[1]), pk2(c0[2], c0[3]), pk2(c1[0], c1[1]), pk2(c1[2], c1[3])};
          st16c(ws, vo, OFF_BF + (u32)(((2 * (20 * ch + 10 * half + 5 * (w >> 2) + ct) * 16 + (h >> 1)) * 20 + 10 * dh + bl) * 1024), o);
        }
      }
    }
  }
  count_done(p, OFF_BC + (u32)ch * 128);
}
DEV void init_part(const Params& p, rsrc_t ws, u32 cb, int k) {
  const int tid = threadIdx.x;
  u32x4 xw[20];
#pragma unroll
  for (int i = 0; i < 20; ++i) {
    const int seg = k + 16 * (i / 5), r = (i % 5) * 512 + tid, tt = r / 1280, ks = (r % 1280) >> 6, ln = r & 63;
    xw[i] = *(const u32x4*)(p.x + (32 * seg + 16 * tt + (ln & 15)) * 640 + 32 * ks + 8 * (ln >> 4));
    if (i == 4) SCHED_FENCE();
  }
  if (tid < 256) {
    const u32 o = (u32)(k * 256 + tid) * 16;
    const u32 L = (o >> 14) * 8192 + (o & 8191);
    u32x4 v = {SENT, SENT, SENT, SENT};
    if (L < 10240) {
      const int vv = L >> 7, t = (L >> 4) & 7;
      const u16* m = p.mem0 + t * 640 + 16 * (vv >> 1) + 2 * (vv & 1);
      for (int q = 0; q < 4; ++q) { u32 x = *(const u32*)(m + 4 * q); v[q] = x < 0xFFFFFFFEu ? x : 0xFFFFFFFEu; }
    }
    st16c(ws, cb + o, 0, v);
  }
#pragma unroll
  for (int i = 0; i < 5; ++i) st16c(ws, (u32)(i * 512 + tid) * 16, OFF_XF + (u32)k * 40960, xw[i]);
  count_done(p, OFF_XFC);
  init_signal(p, true);
#pragma unroll
  for (int i = 5; i < 20; ++i) st16c(ws, (u32)((i % 5) * 512 + tid) * 16, OFF_XF + (u32)(k + 16 * (i / 5)) * 40960, xw[i]);
  count_done(p, OFF_XFC + 64);
}

constexpr float CEXP = 0.08838834764831845f * 1.4426950408889634f;
constexpr float NEGS = -1.0e9f / 0.08838834764831845f;
constexpr int QS = 288;

DEV void read_memf(const unsigned char* memf, int l, u32x4 (&a)[10]) {
#pragma unroll
  for (int ks = 0; ks < 10; ++ks) a[ks] = *(const u32x4*)(memf + ks * 1024 + l * 16);
}
DEV void project2(const u32x4 (&a)[10], const u32x8 (&wt)[2][10], int sidx, unsigned char* dst) {
  f32x4 c0 = {0, 0, 0, 0}, c1 = {0, 0, 0, 0};
#pragma unroll
  for (int ks = 0; ks < 10; ++ks) {
    c0 = smfma(a[ks], wt[0][ks], c0, sidx);
    c1 = smfma(a[ks], wt[1][ks], c1, sidx);
  }
  *(__bf16*)(dst) = (__bf16)(c0[0] + c0[1]);
  *(__bf16*)(dst + QS) = (__bf16)(c0[2] + c0[3]);
  *(__bf16*)(dst + 8) = (__bf16)(c1[0] + c1[1]);
  *(__bf16*)(dst + 8 + QS) = (__bf16)(c1[2] + c1[3]);
}

DEV void score_role(const Params& p, rsrc_t ws, u32 cb, int h, unsigned char* lds) {
  const int tid = threadIdx.x, l = tid & 63, c16 = l & 15, g = l >> 4;
  const int w = __builtin_amdgcn_readfirstlane(tid >> 6);
  unsigned char* memf = lds;
  unsigned char* qs = lds + 10240;
  unsigned char* kms = qs + 8 * QS;
  u32* ctl = (u32*)(lds + 15360);
  unsigned char* kq = lds + 16384;
  u32x8 wt[2][10];
  {
    u32x4 wf[2][20];
    load_qk(p, h, w, l, lds, wf, true);
#pragma unroll
    for (int i = 0; i < 2; ++i)
#pragma unroll
      for (int ks = 0; ks < 10; ++ks) wt[i][ks] = cat(wf[i][2 * ks], wf[i][2 * ks + 1]);
  }
  GSTAMP(1);
  bool dead = false;
  if (w == 0) wait_prog(ws, OFF_INITC, 8, 32, l, dead, 0, 64);
  __syncthreads();
  if (tid < 2) ctl[2 + tid] = 0;
  u32 seen0 = 0, seen1 = 0;
  const u32 kq_src = ((w & 2) ? OFF_QX : OFF_KX) + (u32)(h * 8192 + (w & 1) * 4096);
  if (w < 2) { u32 r = probe(ws, cb, h, w, l); vote(p, w, r, l); if (l == 0) ctl[4 + w] = r; }
  else if (w >= 4) {
    seen0 = wait_prog(ws, OFF_PQK + (u32)h * 128, 1, 2, l, dead, 0);
#pragma unroll
    for (int i = 0; i < 4; ++i) *(u32x4*)(kq + (w - 4) * 4096 + i * 1024 + l * 16) = ld16c(ws, l * 16 + i * 1024, kq_src);
  }
  __syncthreads();
  GSTAMP(2);
  u32 parmask = __builtin_amdgcn_readfirstlane(ctl[4] | (ctl[5] << 1));
  const int sidx = (l & 1) ? 0xEEEE : 0x4444;
  unsigned char* qk_w = qs + (w >> 2) * 8 * QS + 2 * g * QS + (32 * (w & 3) + 8 * (c16 >> 2) + (c16 & 3)) * 2;
  const unsigned char* km_r = kms + (2 * (c16 >> 2) + (c16 & 1)) * QS + g * 16;
  const int wr = (w + h) & 7;
  unsigned char* gA = memf + wr * 1024 + g * 256 + (l & 7) * 32 + ((l >> 3) & 1) * 16;
  unsigned char* gB = memf + 8192 + wr * 256 + ((l >> 2) & 7) * 32 + (l >> 5) * 16 + (l & 3) * 4;
  if (w == 0) {
    const unsigned char* q_r = qs + (c16 & 7) * QS + g * 16;
    const bool hb = c16 >= 8;
    const u32 xo = (u32)(((h >> 1) & 7) * 1024 + g * 256 + (h & 1) * 128 + (c16 & 7) * 16 + (c16 >> 3) * 8);
    const u32 mo = (u32)((c16 >> 3) * 8192 + h * 128 + (c16 & 7) * 16 + g * 4);
    for (int s = 0; s < NSEG; ++s) {
      const u32 sb = cb + (u32)s * STEP_BYTES;
      if (s == 1) parmask = vote_result(ws, l);
      const u32 par = ((parmask >> (s >> 5)) & 1) * 8192;
      const unsigned char* kxs = kq + (s & 1) * 16384;
      __syncthreads();
      { u32 sp = gather_mem(ws, sb + par, wr, gA, gB, l, dead); COUNT(5, sp); if (sp && l == 0) ctl[2 + (s & 1)] = (u32)s; }
      __syncthreads();
      { u32x4 a[10]; read_memf(memf, l, a); SCHED_FENCE(); project2(a, wt, sidx, qk_w); }
      u32x4 kx[8];
#pragma unroll
      for (int i = 0; i < 8; ++i) kx[i] = *(const u32x4*)(kxs + i * 1024 + l * 16);
      __syncthreads();
      if (s == NSEG - 1) break;
      u32x4 qf[4], km[4];
#pragma unroll
      for (int ks = 0; ks < 4; ++ks) { qf[ks] = *(const u32x4*)(q_r + ks * 64); km[ks] = *(const u32x4*)(km_r + ks * 64); }
      SCHED_FENCE();
      f32x4 s0 = {0, 0, 0, 0}, s1 = {0, 0, 0, 0}, s2 = {0, 0, 0, 0};
#pragma unroll
      for (int ks = 0; ks < 4; ++ks) {
        s1 = mfma(kx[ks], qf[ks], s1);
        s2 = mfma(kx[4 + ks], qf[ks], s2);
        s0 = mfma(km[ks], qf[ks], s0);
      }
      float v[4];
#pragma unroll
      for (int r = 0; r < 4; ++r) v[r] = hb ? s2[r] : s1[r];
      float m = fmx(fmx(fmx(v[0], v[1]), v[2]), fmx(fmx(v[3], s0[0]), s0[1]));
      m = red_max(m);
      const float mc = -m * CEXP;
#pragma unroll
      for (int r = 0; r < 4; ++r) v[r] = __builtin_amdgcn_exp2f(fmaf(v[r], CEXP, mc));
      const float e0 = __builtin_amdgcn_exp2f(fmaf(s0[0], CEXP, mc)), e1 = __builtin_amdgcn_exp2f(fmaf(s0[1], CEXP, mc));
      float sum = ((v[0] + v[1]) + (v[2] + v[3])) + (hb ? e1 : e0);
      sum = red_sum(sum);
      const float inv = __builtin_amdgcn_rcpf(sum);
      u32x2 px = {pk2s(v[0] * inv, v[1] * inv), pk2s(v[2] * inv, v[3] * inv)};
      const u32 pm = pk2s(e0 * inv, e1 * inv);
      const u32 xs = sb + (u32)(2 + (h >> 4)) * 16384;
      st8c(ws, xo, xs + par, px);
      st4c(ws, mo, sb + 16384 + 2048, pm);
      st8c(ws, xo, xs + (par ^ 8192), px);
      const u32 tnow = fine_now();
      const u32 rm = __builtin_amdgcn_readfirstlane(ctl[s & 1]), bad = __builtin_amdgcn_readfirstlane(ctl[2 + (s & 1)]);
      const u32 d1 = (s >= 2 && bad != (u32)s) ? tnow - rm : 0u;
      st4c(ws, l < 32 ? (u32)l * 4 : 0x80000000u, OFF_SIDE + (u32)s * SIDE_STEP + (u32)(80 + h) * 128, d1 < 1000u ? d1 : 0u);
      STAMPV(7, d1);
      STAMPV(4, tnow);
    }
  } else if (w < 3) {
    const int tx = 16 * (w - 1) + c16;
    for (int s = 0; s < NSEG; ++s) {
      const unsigned char* kxs = kq + (s & 1) * 16384;
      const u32 sb = cb + (u32)s * STEP_BYTES;
      if (s == 1) parmask = vote_result(ws, l);
      const u32 par = ((parmask >> (s >> 5)) & 1) * 8192;
      __syncthreads();
      { u32 sp = gather_mem(ws, sb + par, wr, gA, gB, l, dead); COUNT(5, sp); if (sp && l == 0) ctl[2 + (s & 1)] = (u32)s; }
      __syncthreads();
      if (w == 1) STAMP(1);
      { u32x4 a[10]; read_memf(memf, l, a); SCHED_FENCE(); project2(a, wt, sidx, qk_w); }
      __syncthreads();
      if (w == 1) STAMP(2);
      if (w == 1) st4c(ws, l == 0 ? 0u : 0x80000000u, OFF_PPX + (u32)h * 128, (u32)s);
      f32x4 s0 = {0, 0, 0, 0}, s1 = {0, 0, 0, 0}, s2 = {0, 0, 0, 0};
#pragma unroll
      for (int ks = 0; ks < 4; ++ks) {
        u32x4 qf = *(const u32x4*)(kxs + 8192 + ((w - 1) * 4 + ks) * 1024 + l * 16);
        s0 = mfma(*(const u32x4*)(km_r + ks * 64), qf, s0);
        s1 = mfma(*(const u32x4*)(kxs + ks * 1024 + l * 16), qf, s1);
        s2 = mfma(*(const u32x4*)(kxs + (4 + ks) * 1024 + l * 16), qf, s2);
      }
      float m = fmx(s0[0], s0[1]);
#pragma unroll
      for (int r = 0; r < 4; ++r) {
        s1[r] = (4 * g + r <= tx) ? s1[r] : NEGS;
        s2[r] = (16 + 4 * g + r <= tx) ? s2[r] : NEGS;
        m = fmx(m, fmx(s1[r], s2[r]));
      }
      m = red_max_g(m);
      const float mc = -m * CEXP;
      const float e0 = __builtin_amdgcn_exp2f(fmaf(s0[0], CEXP, mc)), e1 = __builtin_amdgcn_exp2f(fmaf(s0[1], CEXP, mc));
      float sum = e0 + e1;
#pragma unroll
      for (int r = 0; r < 4; ++r) {
        s1[r] = __builtin_amdgcn_exp2f(fmaf(s1[r], CEXP, mc));
        s2[r] = __builtin_amdgcn_exp2f(fmaf(s2[r], CEXP, mc));
        sum += s1[r] + s2[r];
      }
      sum = red_sum_g(sum);
      const float inv = __builtin_amdgcn_rcpf(sum);
      u32x4 px = {pk2(s1[0] * inv, s1[1] * inv), pk2(s1[2] * inv, s1[3] * inv), pk2(s2[0] * inv, s2[1] * inv), pk2(s2[2] * inv, s2[3] * inv)};
      st16c(ws, l * 16, OFF_PXX + (u32)(((s * 32 + h) * 2 + (w - 1)) * 1024), px);
      st4c(ws, c16 * 16 + g * 4, OFF_PXM + (u32)((((s * 2 + (w - 1)) * 32 + h) * 16) * 16), pk2(e0 * inv, e1 * inv));
      VM_DRAIN();
    }
  } else if (w == 3) {
    u32 Rm = 0, Rp;
    for (int s = 0; s < NSEG; ++s) {
      const u32 sb = cb + (u32)s * STEP_BYTES;
      if (s == 1) parmask = vote_result(ws, l);
      const u32 par = ((parmask >> (s >> 5)) & 1) * 8192;
      if (s >= 2) {
        if (l == 0) { ctl[s & 1] = Rm; if ((int)(Rm - rtime()) < 28) ctl[2 + (s & 1)] = (u32)s; }
        wait_until(Rm);
      }
      __syncthreads();
      { u32 sp = gather_mem(ws, sb + par, wr, gA, gB, l, dead); COUNT(5, sp); if (sp && l == 0) ctl[2 + (s & 1)] = (u32)s; }
      __syncthreads();
      u32x4 a[10];
      read_memf(memf, l, a);
      Side sd = load_side(ws, s, l);
      SCHED_FENCE();
      project2(a, wt, sidx, qk_w);
      __syncthreads();
      STAMPV(0, Rm);
      if (s >= 1 && s < NSEG - 1) schedule(ws, s, l, sd, Rm, Rp, dead);
    }
  } else {
    for (int s = 0; s < NSEG; ++s) {
      const u32 sb = cb + (u32)s * STEP_BYTES;
      if (s == 1) parmask = vote_result(ws, l);
      const u32 par = ((parmask >> (s >> 5)) & 1) * 8192;
      const int sn = s + 1 < NSEG ? s + 1 : s;
      __syncthreads();
      { u32 sp = gather_mem(ws, sb + par, wr, gA, gB, l, dead); COUNT(5, sp); if (sp && l == 0) ctl[2 + (s & 1)] = (u32)s; }
      __syncthreads();
      u32x4 a[10];
      read_memf(memf, l, a);
      SCHED_FENCE();
      u32x4 pk[4];
      f32x4 c0 = {0, 0, 0, 0}, c1 = {0, 0, 0, 0};
#pragma unroll
      for (int ks = 0; ks < 10; ++ks) {
        c0 = smfma(a[ks], wt[0][ks], c0, sidx);
        c1 = smfma(a[ks], wt[1][ks], c1, sidx);
        if (ks == KQPOS) {
          SCHED_FENCE();
#pragma unroll
          for (int i = 0; i < 4; ++i) pk[i] = ld16c(ws, l * 16 + i * 1024, kq_src + (u32)sn * (32 * 8192));
          SCHED_FENCE();
        }
      }
      *(__bf16*)(qk_w) = (__bf16)(c0[0] + c0[1]);
      *(__bf16*)(qk_w + QS) = (__bf16)(c0[2] + c0[3]);
      *(__bf16*)(qk_w + 8) = (__bf16)(c1[0] + c1[1]);
      *(__bf16*)(qk_w + 8 + QS) = (__bf16)(c1[2] + c1[3]);
      __syncthreads();
      unsigned char* kn = kq + ((s + 1) & 1) * 16384 + (w - 4) * 4096 + l * 16;
#pragma unroll
      for (int i = 0; i < 4; ++i) *(u32x4*)(kn + i * 1024) = pk[i];
      const int s2 = s + 2;
      if (s2 < NSEG) {
        if (seg_prod(s2)) { if (seen1 < seg_need(s2)) { __builtin_amdgcn_s_sleep(12); seen1 = wait_prog(ws, OFF_PQK + (u32)h * 128 + 4, 1, seg_need(s2), l, dead, 0); } }
        else { if (seen0 < seg_need(s2)) { __builtin_amdgcn_s_sleep(12); seen0 = wait_prog(ws, OFF_PQK + (u32)h * 128, 1, seg_need(s2), l, dead, 0); } }
      }
    }
  }
  __syncthreads();
  if (tid == 0) st4c(ws, 0u, OFF_PPX + (u32)h * 128, (u32)NSEG);
}

DEV u32x4 finish_mem(f32x4 acc) {
  auto x = __builtin_amdgcn_permlane32_swap(__float_as_uint(acc[0]), __float_as_uint(acc[2]), false, false);
  auto y = __builtin_amdgcn_permlane32_swap(__float_as_uint(acc[1]), __float_as_uint(acc[3]), false, false);
  float t0, t1;
  asm("s_nop 1\n\tv_add_f32_dpp %0, %2, %3 row_ror:8 row_mask:0xf bank_mask:0xf\n\tv_add_f32_dpp %1, %4, %5 row_ror:8 row_mask:0xf bank_mask:0xf"
      : "=&v"(t0), "=&v"(t1) : "v"(x[1]), "v"(x[0]), "v"(y[1]), "v"(y[0]));
  const u32 d = pk2s(t0, t1);
  auto s16 = __builtin_amdgcn_permlane16_swap(d, d, false, false);
  auto sa = __builtin_amdgcn_permlane32_swap(s16[0], s16[0], false, false);
  auto sb = __builtin_amdgcn_permlane32_swap(s16[1], s16[1], false, false);
  return (u32x4){sa[0], sa[1], sb[0], sb[1]};
}

DEV u32 second4(u32 a, u32 b, u32 c, u32 d) {
  const u32 h0 = max(a, b), l0 = min(a, b), h1 = max(c, d), l1 = min(c, d);
  return max(min(h0, h1), h0 > h1 ? l0 : l1);
}
DEV void value_role(const Params& p, rsrc_t ws, u32 cb, int v, unsigned char* lds) {
  const int tid = threadIdx.x, l = tid & 63, c16 = l & 15, g = l >> 4;
  const int w = __builtin_amdgcn_readfirstlane(tid >> 6);
  unsigned char* memf = lds;
  unsigned char* um = lds + 10240;
  u32* ctl = (u32*)(lds + 15360);
  unsigned char* w0s = lds + 16384;
  unsigned char* uxs = lds + 57344;
  const int sidx = (l & 1) ? 0xEEEE : 0x4444;
  const u32 um_w = (u32)((4 * w + (c16 >> 3)) * 128 + (c16 & 7) * 16 + g * 4);
  const int wr = (w + v) & 7;
  unsigned char* gA = memf + wr * 1024 + g * 256 + (l & 7) * 32 + ((l >> 3) & 1) * 16;
  unsigned char* gB = memf + 8192 + wr * 256 + ((l >> 2) & 7) * 32 + (l >> 5) * 16 + (l & 3) * 4;
  bool dead = false;
  b_unit(p, ws, v, lds);
  GSTAMP(4);
  if (w == 0) wait_prog(ws, OFF_BC + (u32)(v / 40) * 128, 1, 64, l, dead, 0);
  if (w == 1) wait_prog(ws, OFF_INITC, 8, 32, l, dead, 0, 64);
  __syncthreads();
  GSTAMP(5);
  if (w == 0) {
#pragma unroll
    for (int i = 0; i < 40; ++i) *(u32x4*)(w0s + i * 1024 + l * 16) = ld16c(ws, l * 16, OFF_BF + (u32)((v * 16 * 20 + i) * 1024));
    __syncthreads();
    GSTAMP(1);
    __syncthreads();
    GSTAMP(2);
    u32 parmask = __builtin_amdgcn_readfirstlane(ctl[4] | (ctl[5] << 1));
    const u32 offX = (u32)((c16 >> 3) * 512 + g * 128 + (c16 & 7) * 16);
    const u32 L = (u32)(v * 128 + (c16 & 7) * 16);
    const u32 pub = (c16 < 8 && !(g & 1)) ? (L >> 13) * 16384 + (u32)(g >> 1) * 8192 + (L & 8191) : 0x80000000u;
    u32 Rg = 0, d3 = 0, Rm = 0, dcur = v == 0 ? ((D1 + D2) << 10) | ((D3 + D4) << 20) : 0u;
    u32 a0 = D1, a1 = D1, a2 = D1, a3 = D1, b0 = D3, b1 = D3, b2 = D3, b3 = D3;
    const bool sidel = (g == 1 && c16 < 8);
    for (int s = 0; s < NSEG; ++s) {
      const u32 sb = cb + (u32)s * STEP_BYTES;
      if (s == 1) parmask = vote_result(ws, l);
      const u32 par = ((parmask >> (s >> 5)) & 1) * 8192;
      if (s >= 1) wait_until(Rg);
      __syncthreads();
      const u32x4 ga = ld16c(ws, (u32)l * 16, sb + par + (u32)wr * 1024);
      const u32 gb = ld4c(ws, (u32)l * 4, sb + par + 16384 + (u32)wr * 256);
      SCHED_FENCE();
      __builtin_amdgcn_s_sleep(1);
      Side sd = load_side(ws, s, l);
      u32 d1s = 0;
      if (v == 0 && s >= 1) d1s = ld4c(ws, (u32)(80 + (l & 31)) * 128, OFF_SIDE + (u32)(s - 1) * SIDE_STEP);
      u32x4 uf[20];
#pragma unroll
      for (int i = 0; i < 16; ++i) uf[i] = *(const u32x4*)(uxs + (((i >> 2) + PROT) & 3) * 4096 + (i & 3) * 1024 + offX);
      SCHED_FENCE();
      *(u32x4*)gA = ga;
      *(u32*)gB = gb;
      if (__builtin_expect(__any(umax3(umax3(ga.x, ga.y, ga.z), ga.w, gb) == SENT), 0)) { u32 sp = gather_mem(ws, sb + par, wr, gA, gB, l, dead); COUNT(5, sp + 1); }
      __syncthreads();
      STAMP(0);
      if (s == NSEG - 1) {
        __syncthreads();
        __syncthreads();
        break;
      }
      u32 so[5];
#pragma unroll
      for (int i = 0; i < 4; ++i) { const u32 qd = (u32)(i + PROT) & 3; so[i] = sb + par + (2 + (qd >> 1)) * 16384 + (qd & 1) * 4096; }
      so[4] = sb + par + 16384 + 2048;
      asm volatile("" : "+s"(so[0]), "+s"(so[1]), "+s"(so[2]), "+s"(so[3]), "+s"(so[4]));
      const u32 pubo = sidel ? OFF_SIDE + (u32)(s + 1) * SIDE_STEP + (u32)(v * 128 + c16 * 16) : pub + sb + STEP_BYTES;
      u32 Rp = 0;
      if (s >= 1) schedule(ws, s, l, sd, Rm, Rp, dead);
      const u32 sy = d3 | dcur;
      const int lead = (int)(Rp - rtime());
      bool ontime = s >= 1 && lead > 0, pfail = false;
      STAMPV(1, Rp);
      STAMP(2);
      SCHED_FENCE();
      if (s >= 1) wait_until(Rp);
      SCHED_FENCE();
      u32x4 pf[20];
#pragma unroll
      for (int i = 0; i < 20; ++i) { pf[i] = ld16c(ws, (u32)l * 16 + (i & 3) * 1024, so[i >> 2]); SCHED_FENCE(); }
      f32x4 acc = {0, 0, 0, 0};
#pragma unroll
      for (int i = 0; i < 20; ++i) {
        if (i == UMPOS) {
          __syncthreads();
#pragma unroll
          for (int q = 0; q < 4; ++q) uf[16 + q] = *(const u32x4*)(um + q * 1024 + l * 16);
          SCHED_FENCE();
        }
        acc = mfma(uf[i], pf[i], acc);
        SCHED_FENCE();
      }
      const u32 tl = rtime();
      u32x4 o = finish_mem(acc);
      if (__builtin_expect(__any(acc[0] != acc[0]), 0)) {
        u32 spins = 0;
#pragma nounroll
        while (!dead) {
          u32 m = 0;
#pragma unroll
          for (int i = 0; i < 20; ++i) m = umax3(umax3(m, pf[i].x, pf[i].y), pf[i].z, pf[i].w);
          if (__all(m != SENT)) break;
          ontime = false; pfail = true;
          if (++spins > SPIN_CAP) dead = true;
          asm volatile("" ::: "memory");
#pragma unroll
          for (int i = 0; i < 20; ++i) pf[i] = ld16c(ws, (u32)l * 16 + (i & 3) * 1024, so[i >> 2]);
        }
        STAMPV(4, spins);
        acc = (f32x4){0, 0, 0, 0};
#pragma unroll
        for (int i = 0; i < 20; ++i) acc = mfma(uf[i], pf[i], acc);
        o = finish_mem(acc);
      }
      u32 sx = 0;
      if ((int)(tl + DLS - Rm) > 0) sx = (tl + DL + 4) | 1u;
      if (__builtin_expect(!ontime, 0)) sx = (rtime() + DL) | 1u;
      if (sx == SENT) sx = 1u;
      o.x = sidel ? sx : o.x;
      o.y = sidel ? sy : o.y;
      st16c(ws, pubo, 0u, o);
      const u32 tp = fine_now();
      __syncthreads();
      d3 = (ontime && lead >= 28) ? tp - Rp : 0u;
      d3 = d3 < 1000u ? d3 : 0u;
      Rg = sx == 0 ? Rm + DV : tp + D4 + DV;
      STAMPV(7, d3);
      if (v == 0) {
        const u32 ma = (u32)wave_max(d1s == SENT ? 0 : (int)d1s), mb = s >= 1 ? (u32)wave_max((int)max(sd.a.y & 1023u, sd.b.y & 1023u)) : 0u;
        if (ma) { a3 = a2; a2 = a1; a1 = a0; a0 = ma; }
        if (pfail && s >= 2) { a0 = a1 = min(second4(a0, a1, a2, a3) + 8, 300u); }
        if (mb) { b3 = b2; b2 = b1; b1 = b0; b0 = mb; }
        dcur = (min(second4(a0, a1, a2, a3) + D2, 1000u) << 10) | (min(second4(b0, b1, b2, b3) + D4, 1000u) << 20);
      }
      STAMP(3);
      STAMPV(6, ((sy >> 10) & 1023u) * 1000 + ((sy >> 20) & 1023u));
    }
  } else {
    u32x8 wb[2][10];
#pragma unroll
    for (int t = 0; t < 2; ++t)
#pragma unroll
      for (int ks = 0; ks < 10; ++ks) {
        u32 o = OFF_BF + (u32)(((v * 16 + 2 * w + t) * 20 + 2 * ks) * 1024);
        wb[t][ks] = cat(ld16c(ws, l * 16, o), ld16c(ws, l * 16 + 1024, o));
      }
    __builtin_amdgcn_s_waitcnt(0x0F70);
    __syncthreads();
    if (w < 3) { u32 r = probe(ws, cb, 32 + v, w - 1, l); vote(p, w - 1, r, l); if (l == 0) ctl[3 + w] = r; }
    const bool pfw = w == 3 || w >= 5;
    const u32 uxo = (u32)((w == 3 ? 0 : w - 4) * 4096 + l * 16);
    u32 seen0 = 0, seen1 = 0;
    if (pfw) {
      seen0 = wait_prog(ws, OFF_PVU, 32, 2, l, dead, 0);
#pragma unroll
      for (int i = 0; i < 4; ++i) *(u32x4*)(uxs + uxo + i * 1024) = ld16c(ws, uxo + i * 1024, OFF_UX + (u32)(v * 16384));
    }
    __syncthreads();
    u32 parmask = __builtin_amdgcn_readfirstlane(ctl[4] | (ctl[5] << 1));
    if (w == 4) {
      for (int s = 0; s < NSEG; ++s) {
        const u32 sb = cb + (u32)s * STEP_BYTES;
        if (s == 1) parmask = vote_result(ws, l);
      const u32 par = ((parmask >> (s >> 5)) & 1) * 8192;
        u32x8 sb0[2], sb1[2];
        sb0[0] = cat(*(const u32x4*)(w0s + l * 16), *(const u32x4*)(w0s + 1024 + l * 16));
        sb1[0] = cat(*(const u32x4*)(w0s + 20480 + l * 16), *(const u32x4*)(w0s + 21504 + l * 16));
        __syncthreads();
        { u32 sp = gather_mem(ws, sb + par, wr, gA, gB, l, dead); COUNT(5, sp); }
        __syncthreads();
        u32x4 a[3];
        a[0] = *(const u32x4*)(memf + l * 16);
        a[1] = *(const u32x4*)(memf + 1024 + l * 16);
        f32x4 a0 = {0, 0, 0, 0}, a1 = {0, 0, 0, 0}, a2 = {0, 0, 0, 0}, a3 = {0, 0, 0, 0};
#pragma unroll
        for (int ks = 0; ks < 10; ++ks) {
          if (ks + 2 < 10) a[(ks + 2) % 3] = *(const u32x4*)(memf + (ks + 2) * 1024 + l * 16);
          if (ks + 1 < 10) {
            const int kn = ks + 1;
            sb0[kn & 1] = cat(*(const u32x4*)(w0s + (2 * kn) * 1024 + l * 16), *(const u32x4*)(w0s + (2 * kn + 1) * 1024 + l * 16));
            sb1[kn & 1] = cat(*(const u32x4*)(w0s + (20 + 2 * kn) * 1024 + l * 16), *(const u32x4*)(w0s + (21 + 2 * kn) * 1024 + l * 16));
          }
          SCHED_FENCE();
          a0 = smfma(a[ks % 3], wb[0][ks], a0, sidx);
          a1 = smfma(a[ks % 3], wb[1][ks], a1, sidx);
          a2 = smfma(a[ks % 3], sb0[ks & 1], a2, sidx);
          a3 = smfma(a[ks % 3], sb1[ks & 1], a3, sidx);
          SCHED_FENCE();
        }
        *(u32*)(um + um_w) = pk2(a0[0] + a0[1], a0[2] + a0[3]);
        *(u32*)(um + um_w + 256) = pk2(a1[0] + a1[1], a1[2] + a1[3]);
        *(u32*)(um + um_w - 2048) = pk2(a2[0] + a2[1], a2[2] + a2[3]);
        *(u32*)(um + um_w - 2048 + 256) = pk2(a3[0] + a3[1], a3[2] + a3[3]);
        __syncthreads();
        __syncthreads();
      }
    } else {
      for (int s = 0; s < NSEG; ++s) {
        const u32 sb = cb + (u32)s * STEP_BYTES;
        if (s == 1) parmask = vote_result(ws, l);
      const u32 par = ((parmask >> (s >> 5)) & 1) * 8192;
        __syncthreads();
        { u32 sp = gather_mem(ws, sb + par, wr, gA, gB, l, dead); COUNT(5, sp); }
        __syncthreads();
        u32x4 a[10];
        read_memf(memf, l, a);
        u32x4 pu[4];
        if (pfw && s + 1 < NSEG) {
#pragma unroll
          for (int i = 0; i < 4; ++i) pu[i] = ld16c(ws, uxo + i * 1024, OFF_UX + (u32)(((s + 1) * 80 + v) * 16384));
        }
        SCHED_FENCE();
        f32x4 a0 = {0, 0, 0, 0}, a1 = {0, 0, 0, 0};
#pragma unroll
        for (int ks = 0; ks < 10; ++ks) {
          a0 = smfma(a[ks], wb[0][ks], a0, sidx);
          a1 = smfma(a[ks], wb[1][ks], a1, sidx);
        }
        *(u32*)(um + um_w) = pk2(a0[0] + a0[1], a0[2] + a0[3]);
        *(u32*)(um + um_w + 256) = pk2(a1[0] + a1[1], a1[2] + a1[3]);
        if (w == 1) st4c(ws, l == 0 ? 0u : 0x80000000u, OFF_PUM + (u32)v * 128, (u32)s);
        __syncthreads();
        __syncthreads();
        if (pfw && s + 1 < NSEG) {
#pragma unroll
          for (int i = 0; i < 4; ++i) *(u32x4*)(uxs + uxo + i * 1024) = pu[i];
          const int sn = s + 2;
          if (sn < NSEG) {
            if (seg_prod(sn)) { if (seen1 < seg_need(sn)) seen1 = wait_prog(ws, OFF_PVU + 128, 32, seg_need(sn), l, dead, 0); }
            else { if (seen0 < seg_need(sn)) seen0 = wait_prog(ws, OFF_PVU, 32, seg_need(sn), l, dead, 0); }
          }
        }
        if (w == 1 || w == 2) {
#pragma unroll
          for (int i = 0; i < 2; ++i) {
            u32 o = (u32)(((w - 1) * 2 + i) * 1024 + l * 16);
            st16c(ws, o, OFF_UM + (u32)((s * 80 + v) * 4096), *(const u32x4*)(um + o));
          }
          VM_DRAIN();
        }
      }
    }
  }
  __syncthreads();
  if (tid == 0) st4c(ws, 0u, OFF_PUM + (u32)v * 128, (u32)NSEG);
}

DEV void glds16(const void* gsrc, u32 lds_dst) {
  u32 keep;
  asm volatile("s_mov_b32 %0, m0\n\ts_mov_b32 m0, %2\n\ts_nop 0\n\tglobal_load_lds_dwordx4 %1, off sc1\n\ts_mov_b32 m0, %0"
               : "=&s"(keep) : "v"(gsrc), "s"(lds_dst) : "memory");
}
template <int NST, bool ROWS, class Off, class Body, class Pub>
DEV void stream_tiles(const unsigned char* src, Off off, int n, int nbody, unsigned char* lds, Body body, Pub pub) {
  const int l = threadIdx.x & 63, w = __builtin_amdgcn_readfirstlane(threadIdx.x >> 6);
  const u32 lbase = (u32)(uintptr_t)lds + (u32)w * 5120;
  constexpr int QS_ = ROWS ? 64 : 1024;
  const unsigned char* g0 = ROWS ? src + (16 * (w >> 2) + (l & 15)) * 1280 + (w & 3) * 320 + (l >> 4) * 16 : src + w * 5120 + l * 16;
#pragma unroll
  for (int q = 0; q < 5; ++q) glds16(g0 + off(0) + q * QS_, lbase + q * 1024);
#pragma nounroll
  for (int i = 0; i < nbody; ++i) {
    if (i <= 3) VM_DRAIN(); else asm volatile("s_waitcnt vmcnt(%0)" :: "n"(NST) : "memory");
    RAW_BARRIER();
    if (i >= 1) pub(i <= 3 ? i : i - 1);
    const unsigned char* g = g0 + off(i + 1 < n ? i + 1 : n - 1);
    const u32 lb = lbase + (u32)((i + 1) & 1) * 40960;
    body(i, lds + (i & 1) * 40960, [&](int q) { glds16(g + q * QS_, lb + q * 1024); });
  }
  VM_DRAIN();
  RAW_BARRIER();
  pub(nbody);
}
template <int W, class Dma, class Use>
DEV void sweep40(const unsigned char* t, Dma dma, Use use) {
  u32x4 xb[W];
#pragma unroll
  for (int i = 0; i < W; ++i) xb[i] = *(const u32x4*)(t + ((i & 1) * 20 + (i >> 1)) * 1024);
  SCHED_FENCE();
#pragma unroll
  for (int i = 0; i < 40; ++i) {
    use(i, xb[i % W]);
    if (i + W < 40) xb[i % W] = *(const u32x4*)(t + (((i + W) & 1) * 20 + ((i + W) >> 1)) * 1024);
    if (i % 2 == 1 && i < 10) dma(i >> 1);
    SCHED_FENCE();
  }
}

DEV void qk_role(const Params& p, rsrc_t ws, int h, int par, unsigned char* lds, bool sig) {
  const int tid = threadIdx.x, l = tid & 63;
  const int w = __builtin_amdgcn_readfirstlane(tid >> 6);
  const int mat = w >> 2;
  u32x4 wt[2][20];
  load_qk(p, h, w, l, lds, wt, sig);
  GSTAMP(1);
  const u32 dst = (mat ? OFF_KX : OFF_QX) + (u32)((h * 8 + (w & 3)) * 1024);
  const int n = par ? NHELP : NSEG - NHELP;
  wait_init(ws);
  stream_tiles<2, false>(p.ws + OFF_XF, [&](int i) { return tile_seg(par, i) * 40960; }, n, n, lds,
    [&](int i, const unsigned char* t, auto dma) {
      f32x4 acc[2][2] = {};
      sweep40<8>(t + l * 16, dma, [&](int f, u32x4 xb) {
        acc[0][f & 1] = mfma(wt[0][f >> 1], xb, acc[0][f & 1]);
        acc[1][f & 1] = mfma(wt[1][f >> 1], xb, acc[1][f & 1]);
      });
#pragma unroll
      for (int tt = 0; tt < 2; ++tt) {
        u32x4 o = {pk2(acc[0][tt][0], acc[0][tt][1]), pk2(acc[0][tt][2], acc[0][tt][3]), pk2(acc[1][tt][0], acc[1][tt][1]), pk2(acc[1][tt][2], acc[1][tt][3])};
        st16c(ws, l * 16 + tt * 4096, dst + (u32)tile_seg(par, i) * (32 * 8192), o);
      }
    },
    [&](int k) {
      if (w == 0) {
        if (k == 2) { bool dead = false; wait_prog(ws, OFF_XFC + 64, 1, 16, l, dead, 0); }
        st4c(ws, l == 0 ? 0u : 0x80000000u, OFF_PQK + (u32)(h * 128 + par * 4), (u32)k);
      }
    });
}

DEV void vu_role(const Params& p, rsrc_t ws, int h, int par, unsigned char* lds, bool sig) {
  const int tid = threadIdx.x, l = tid & 63, c16 = l & 15, g = l >> 4;
  const int w = __builtin_amdgcn_readfirstlane(tid >> 6);
  u32x4 wv[20], wo[5][4];
  load_vu(p, h, w, l, lds, wv, wo, sig);
  GSTAMP(1);
  unsigned char* vs = lds + 81920;
  const u32 uoff = (u32)(((c16 >> 1) & 1) * 16384 + h * 512 + g * 128 + (2 * (c16 >> 2) + (c16 & 1)) * 16);
  const int n = par ? NHELP : NSEG - NHELP;
  wait_init(ws);
  stream_tiles<5, false>(p.ws + OFF_XF, [&](int i) { return tile_seg(par, i) * 40960; }, n, n + 1, lds,
    [&](int i, const unsigned char* t, auto dma) {
      u32x4 vf[2][4];
#pragma unroll
      for (int tt = 0; tt < 2; ++tt)
#pragma unroll
        for (int ks = 0; ks < 4; ++ks) vf[tt][ks] = *(const u32x4*)(vs + ((i + 1) & 1) * 8192 + ((tt * 4 + ks) * 64 + l) * 16);
      const u32 uo = i ? uoff + OFF_UX + (u32)((tile_seg(par, i - 1) * 80 + 10 * w) * 16384) : 0x80000000u;
      f32x4 av[2] = {}, cu[2] = {};
      u32x4 ou[2];
      sweep40<6>(t + l * 16, dma, [&](int f, u32x4 xb) {
        av[f & 1] = mfma(wv[f >> 1], xb, av[f & 1]);
        cu[f & 1] = mfma(vf[f & 1][(f >> 1) & 3], wo[f >> 3][(f >> 1) & 3], cu[f & 1]);
        if ((f & 7) == 7) {
          const int ct = f >> 3;
          u32x4 o = {pk2(cu[0][0], cu[0][1]), pk2(cu[0][2], cu[0][3]), pk2(cu[1][0], cu[1][1]), pk2(cu[1][2], cu[1][3])};
          cu[0] = (f32x4){0, 0, 0, 0}; cu[1] = (f32x4){0, 0, 0, 0};
          if (ct == 0) ou[0] = o;
          else { if (ct == 1) st16c(ws, uo, 0u, ou[0]); st16c(ws, uo, (u32)ct * 32768, o); }
        }
      });
#pragma unroll
      for (int tt = 0; tt < 2; ++tt) {
        u32x2 o = {pk2(av[tt][0], av[tt][1]), pk2(av[tt][2], av[tt][3])};
        *(u32x2*)(vs + (i & 1) * 8192 + ((tt * 4 + (w >> 1)) * 64 + l) * 16 + (w & 1) * 8) = o;
      }
    },
    [&](int k) {
      if (w == 0 && k >= 2) {
        st4c(ws, l == 0 ? 0u : 0x80000000u, OFF_PVU + (u32)(par * 128 + h * 4), (u32)(k - 1));
        if (k == 3) { bool dead = false; wait_prog(ws, OFF_XFC + 64, 1, 16, l, dead, 0); }
      }
    });
}

DEV f32x2 cmul(f32x2 a, f32x2 b) { return (f32x2){a.x * b.x - a.y * b.y, a.x * b.y + a.y * b.x}; }
DEV f32x2 cmulc(f32x2 a, f32x2 b) { return (f32x2){a.x * b.x + a.y * b.y, a.y * b.x - a.x * b.y}; }
template <bool INV> DEV f32x2 rot90(f32x2 a) { return INV ? (f32x2){-a.y, a.x} : (f32x2){a.y, -a.x}; }
template <bool INV> DEV void fft4(f32x2& a, f32x2& b, f32x2& c, f32x2& d) {
  f32x2 s0 = a + c, d0 = a - c, s1 = b + d, d1 = rot90<INV>(b - d);
  a = s0 + s1; b = d0 + d1; c = s0 - s1; d = d0 - d1;
}
template <bool INV> DEV f32x2 twc(f32x2 a, float c, float s) {
  return INV ? (f32x2){a.x * c - a.y * s, a.y * c + a.x * s} : (f32x2){a.x * c + a.y * s, a.y * c - a.x * s};
}
template <bool INV> DEV void fft16(f32x2 (&v)[16]) {
  constexpr float C1 = 0.92387953251128674f, S1 = 0.38268343236508977f, R = 0.70710678118654752f;
#pragma unroll
  for (int i = 0; i < 4; ++i) fft4<INV>(v[i], v[i + 4], v[i + 8], v[i + 12]);
  v[5] = twc<INV>(v[5], C1, S1);  v[9] = twc<INV>(v[9], R, R);     v[13] = twc<INV>(v[13], S1, C1);
  v[6] = twc<INV>(v[6], R, R);    v[10] = rot90<INV>(v[10]);       v[14] = twc<INV>(v[14], -R, R);
  v[7] = twc<INV>(v[7], S1, C1);  v[11] = twc<INV>(v[11], -R, R);  v[15] = twc<INV>(v[15], -C1, -S1);
#pragma unroll
  for (int k1 = 0; k1 < 4; ++k1) fft4<INV>(v[4 * k1], v[4 * k1 + 1], v[4 * k1 + 2], v[4 * k1 + 3]);
  f32x2 t;
#define SWP(a, b) t = v[a]; v[a] = v[b]; v[b] = t;
  SWP(1, 4) SWP(2, 8) SWP(3, 12) SWP(6, 9) SWP(7, 13) SWP(11, 14)
#undef SWP
}
template <bool INV> DEV void fft8(f32x2 (&v)[8]) {
  constexpr float R = 0.70710678118654752f;
  fft4<INV>(v[0], v[2], v[4], v[6]);
  fft4<INV>(v[1], v[3], v[5], v[7]);
  f32x2 o1 = twc<INV>(v[3], R, R), o2 = rot90<INV>(v[5]), o3 = twc<INV>(v[7], -R, R), o0 = v[1];
  f32x2 e0 = v[0], e1 = v[2], e2 = v[4], e3 = v[6];
  v[0] = e0 + o0; v[4] = e0 - o0; v[1] = e1 + o1; v[5] = e1 - o1; v[2] = e2 + o2; v[6] = e2 - o2; v[3] = e3 + o3; v[7] = e3 - o3;
}

DEV void fft_cols(const Params& p, rsrc_t ws, int q, unsigned char* lds) {
  const int tid = threadIdx.x, c = tid >> 7, t = tid & 127;
  f32x2* tw = (f32x2*)lds;
  f32x2* buf = (f32x2*)(lds + 16384) + c * (16 * 136);
  f32x2* fin = (f32x2*)(lds + 16384);
  __syncthreads();
  for (int m = tid; m < 2048; m += 512) { float sn, cs; sincospif((float)m * (1.0f / 1024.0f), &sn, &cs); tw[m] = (f32x2){cs, -sn}; }
  f32x2 keep[16];
  const int npass = (q == 0) ? 2 : 1;
  for (int pass = npass - 1; pass >= 0; --pass) {
    f32x2 v[16];
#pragma unroll
    for (int a = 0; a < 16; ++a)
      v[a] = __builtin_bit_cast(f32x2, ld8c(ws, (u32)(t * 16 + (c & 1) * 8 + (c >> 1) * 32768), OFF_FX + (u32)(2 * q * 32768 + a * 2048)));
    if (q == 0 && c == 0) {
#pragma unroll
      for (int a = 0; a < 16; ++a) v[a] = (f32x2){pass ? v[a].y : v[a].x, 0.f};
    }
    u32 wreg[16];
#pragma unroll
    for (int i = 0; i < 2; ++i)
#pragma unroll
      for (int kc = 0; kc < 8; ++kc) {
        const int pp = t + 128 * i, k = (pp >> 4) + 16 * (pp & 15) + 256 * kc;
        wreg[i * 8 + kc] = ld4c(ws, (u32)(k * 16 + c * 4), OFF_WT + (u32)(pass ? 80 : q) * 32768);
      }
    __syncthreads();
    fft16<false>(v);
#pragma unroll
    for (int ka = 0; ka < 16; ++ka) buf[ka * 136 + t] = ka ? cmul(v[ka], tw[t * ka]) : v[ka];
    __syncthreads();
    const int ka2 = t >> 3, c2 = t & 7;
#pragma unroll
    for (int b = 0; b < 16; ++b) v[b] = buf[ka2 * 136 + 8 * b + c2];
    fft16<false>(v);
#pragma unroll
    for (int kb = 0; kb < 16; ++kb) buf[ka2 * 136 + 8 * kb + c2] = kb ? cmul(v[kb], tw[16 * c2 * kb]) : v[kb];
    __syncthreads();
#pragma unroll
    for (int i = 0; i < 2; ++i) {
      const int pp = t + 128 * i;
      f32x2* e = buf + (pp >> 4) * 136 + 8 * (pp & 15);
      f32x2 u[8];
#pragma unroll
      for (int j = 0; j < 8; ++j) u[j] = e[j];
      fft8<false>(u);
#pragma unroll
      for (int kc = 0; kc < 8; ++kc) {
        const u32 wv = wreg[i * 8 + kc];
        const f32x2 wf = {__uint_as_float(wv << 16) * (1.0f / (2048.0f * 640.0f)), __uint_as_float(wv & 0xFFFF0000u) * (1.0f / (2048.0f * 640.0f))};
        u[kc] = cmul(u[kc], wf);
      }
      fft8<true>(u);
#pragma unroll
      for (int j = 0; j < 8; ++j) e[j] = u[j];
    }
    __syncthreads();
#pragma unroll
    for (int kb = 0; kb < 16; ++kb) { f32x2 x = buf[ka2 * 136 + 8 * kb + c2]; v[kb] = kb ? cmulc(x, tw[16 * c2 * kb]) : x; }
    fft16<true>(v);
#pragma unroll
    for (int b = 0; b < 16; ++b) buf[ka2 * 136 + 8 * b + c2] = v[b];
    __syncthreads();
#pragma unroll
    for (int ka = 0; ka < 16; ++ka) { f32x2 x = buf[ka * 136 + t]; v[ka] = ka ? cmulc(x, tw[t * ka]) : x; }
    fft16<true>(v);
    if (pass == 1) {
#pragma unroll
      for (int a = 0; a < 16; ++a) keep[a] = v[a];
    } else {
      if (q == 0 && c == 0) {
#pragma unroll
        for (int a = 0; a < 16; ++a) v[a].y = keep[a].x;
      }
      __syncthreads();
#pragma unroll
      for (int a = 0; a < 16; ++a) fin[(128 * a + t) * 4 + c] = v[a];
      __syncthreads();
    }
  }
#pragma unroll
  for (int i = 0; i < 4; ++i) {
    const int n = tid + 512 * i;
    const f32x4 x0 = *(const f32x4*)(fin + n * 4), x1 = *(const f32x4*)(fin + n * 4 + 2);
    u32x4 hi = {pk2(x0[0], x0[1]), pk2(x0[2], x0[3]), pk2(x1[0], x1[1]), pk2(x1[2], x1[3])};
    u32x4 lo = {pk2(x0[0] - __uint_as_float(hi[0] << 16), x0[1] - __uint_as_float(hi[0] & 0xFFFF0000u)),
                pk2(x0[2] - __uint_as_float(hi[1] << 16), x0[3] - __uint_as_float(hi[1] & 0xFFFF0000u)),
                pk2(x1[0] - __uint_as_float(hi[2] << 16), x1[1] - __uint_as_float(hi[2] & 0xFFFF0000u)),
                pk2(x1[2] - __uint_as_float(hi[3] << 16), x1[3] - __uint_as_float(hi[3] & 0xFFFF0000u))};
    const u32 o = (u32)((n >> 4) * 40960 + (q >> 2) * 1024 + ((q & 3) * 16 + (n & 15)) * 16);
    st16c(ws, o, OFF_FY, hi);
    st16c(ws, o + 20480, OFF_FY, lo);
  }
}

DEV void fw_task(const Params& p, rsrc_t ws, int k) {
  for (int q = k * 2560 + threadIdx.x; q < (k + 1) * 2560; q += 512) {
    const int which = q / 51200, r = q % 51200, tile = r / 1280, ks = (r % 1280) >> 6, ln = r & 63;
    u32x4 hi, lo;
    float val[8];
#pragma unroll
    for (int j = 0; j < 8; ++j) {
      const int aa = 16 * tile + (ln & 15), bb = 32 * ks + 8 * (ln >> 4) + j;
      const int kp = which ? bb : aa, d = which ? aa : bb;
      const int kk = (kp == 1) ? 320 : (kp >> 1);
      float sn, cs;
      sincospif((float)((kk * d) % 640) * (1.0f / 320.0f), &sn, &cs);
      float x = (kp & 1) && kp != 1 ? -sn : cs;
      if (which && kp > 1) x *= 2.0f;
      val[j] = x;
    }
#pragma unroll
    for (int j = 0; j < 4; ++j) { hi[j] = pk2(val[2 * j], val[2 * j + 1]); lo[j] = pk2lo(val[2 * j], val[2 * j + 1], hi[j]); }
    const u32 o = (which ? OFF_FW3 : OFF_FW1) + (u32)((tile * 40 + ks) * 1024 + ln * 16);
    st16c(ws, o, 0, hi);
    st16c(ws, o + 20480, 0, lo);
  }
  VM_DRAIN();
  __syncthreads();
  if (threadIdx.x == 0) __hip_atomic_fetch_add((u32*)(p.ws + OFF_FWC), 1u, __ATOMIC_RELAXED, __HIP_MEMORY_SCOPE_AGENT);
}
DEV void wt_task(const Params& p, rsrc_t ws, int k, unsigned char* lds) {
  const int tid = threadIdx.x;
  const rsrc_t rr = mk_rsrc(p.wr, 2048 * 642), ri = mk_rsrc(p.wi, 2048 * 642);
  for (int blk = 0; blk < 2; ++blk) {
    const int row0 = k * 128 + blk * 64;
    __syncthreads();
#pragma unroll
    for (int i = 0; i < 6; ++i) {
      const int c = tid + 512 * i;
      if (c < 2568) {
        *(u32x4*)(lds + c * 16) = ld16(rr, (u32)c * 16, (u32)row0 * 642);
        *(u32x4*)(lds + 41088 + c * 16) = ld16(ri, (u32)c * 16, (u32)row0 * 642);
      }
    }
    __syncthreads();
    const int r = tid & 63;
    const u16* a = (const u16*)lds + r * 321;
    for (int q = tid >> 6; q < 81; q += 8) {
      u32x4 o;
#pragma unroll
      for (int j = 0; j < 4; ++j) { const int col = q < 80 ? 4 * q + j : 320; o[j] = (u32)a[col] | ((u32)a[20544 + col] << 16); }
      st16c(ws, (u32)(row0 + r) * 16, OFF_WT + (u32)q * 32768, o);
    }
  }
  VM_DRAIN();
  __syncthreads();
  if (tid == 0) __hip_atomic_fetch_add((u32*)(p.ws + OFF_FWC), 1u, __ATOMIC_RELAXED, __HIP_MEMORY_SCOPE_AGENT);
}
DEV void wait_fw(rsrc_t ws) {
  if (threadIdx.x < 64) { bool dead = false; wait_prog(ws, OFF_FWC, 1, 56, threadIdx.x, dead, 1); }
  __syncthreads();
}
DEV void f1_task(const Params& p, rsrc_t ws, int type, int rg, unsigned char* lds) {
  const int tid = threadIdx.x, l = tid & 63, c16 = l & 15, g = l >> 4;
  const int w = __builtin_amdgcn_readfirstlane(tid >> 6);
  const int tile = type * 8 + w;
  wait_fw(ws);
  u32x4 wt[2][20];
#pragma unroll
  for (int i = 0; i < 2; ++i)
#pragma unroll
    for (int ks = 0; ks < 20; ++ks) { wt[i][ks] = ld16c(ws, l * 16, OFF_FW1 + (u32)((tile * 40 + i * 20 + ks) * 1024)); THROTTLE(ks); }
  __builtin_amdgcn_s_waitcnt(0x0F70);
  stream_tiles<2, true>((const unsigned char*)p.ax + rg * 4 * 40960, [](int i) { return i * 40960; }, 4, 4, lds,
    [&](int i, const unsigned char* t, auto dma) {
      f32x4 acc[2][2] = {};
      sweep40<8>(t + l * 16, dma, [&](int f, u32x4 xb) {
        acc[0][f & 1] = mfma(wt[0][f >> 1], xb, acc[0][f & 1]);
        acc[1][f & 1] = mfma(wt[1][f >> 1], xb, acc[1][f & 1]);
      });
#pragma unroll
      for (int tt = 0; tt < 2; ++tt) {
        f32x4 o = acc[0][tt] + acc[1][tt];
        st16c(ws, (u32)(g * 32768 + c16 * 16 + tt * 256), OFF_FX + (u32)(tile * 4 * 32768 + (rg * 4 + i) * 512), __builtin_bit_cast(u32x4, o));
      }
    },
    [&](int k) { if (k == 4 && tid == 0) __hip_atomic_fetch_add((u32*)(p.ws + OFF_F1C + type * 128), 1u, __ATOMIC_RELAXED, __HIP_MEMORY_SCOPE_AGENT); });
}
DEV void f3_task(const Params& p, rsrc_t ws, int type, int rg, unsigned char* lds) {
  const int tid = threadIdx.x, l = tid & 63, c16 = l & 15, g = l >> 4;
  const int w = __builtin_amdgcn_readfirstlane(tid >> 6);
  const int tile = type * 8 + w;
  wait_fw(ws);
  u32x4 wt[2][20];
#pragma unroll
  for (int i = 0; i < 2; ++i)
#pragma unroll
    for (int ks = 0; ks < 20; ++ks) { wt[i][ks] = ld16c(ws, l * 16, OFF_FW3 + (u32)((tile * 40 + i * 20 + ks) * 1024)); THROTTLE(ks); }
  __builtin_amdgcn_s_waitcnt(0x0F70);
  if (w == 0) {
    bool dead = false;
    wait_prog(ws, OFF_F2C, 1, 80, l, dead, 1);
    __builtin_amdgcn_fence(__ATOMIC_ACQUIRE, "agent");
    VM_DRAIN();
  }
  __syncthreads();
  u16* outp = p.out + (rg * 128 + c16) * 1280 + tile * 16 + 4 * g;
  stream_tiles<1, false>(p.ws + OFF_FY + rg * 8 * 40960, [](int i) { return i * 40960; }, 8, 8, lds,
    [&](int i, const unsigned char* t, auto dma) {
      f32x4 a0 = {0, 0, 0, 0}, a1 = {0, 0, 0, 0}, a2 = {0, 0, 0, 0};
      sweep40<10>(t + l * 16, dma, [&](int f, u32x4 y) {
        if (f & 1) a2 = mfma(wt[0][f >> 1], y, a2);
        else { a0 = mfma(wt[0][f >> 1], y, a0); a1 = mfma(wt[1][f >> 1], y, a1); }
      });
      f32x4 o = a0 + (a1 + a2);
      u32x2 ov = {pk2(o[0], o[1]), pk2(o[2], o[3])};
      *(u32x2*)(outp + i * 16 * 1280) = ov;
    },
    [&](int) {});
}

DEV void out_task(const Params& p, rsrc_t ws, int s, int cg, unsigned char* lds) {
  const int tid = threadIdx.x, l = tid & 63, c16 = l & 15, g = l >> 4;
  const int w = __builtin_amdgcn_readfirstlane(tid >> 6);
  const int G = 8 * cg + w;
  const int pp = (c16 >> 1) & 1, j = 2 * (c16 >> 2) + (c16 & 1);
  const u32 ox = (u32)(pp * 16384 + g * 128 + j * 16), om = (u32)(pp * 4096 + g * 128 + j * 16);
  bool dead = false;
  if (w == 0) wait_prog(ws, OFF_PVU + (u32)seg_prod(s) * 128, 32, seg_need(s), l, dead, 1);
  __syncthreads();
  u32x4 ua[32];
#pragma unroll
  for (int h = 0; h < 32; ++h) { ua[h] = ld16c(ws, ox + (h & 7) * 512, OFF_UX + (u32)((s * 80 + 2 * G) * 16384 + (h >> 3) * 4096)); THROTTLE(h); }
  SCHED_FENCE();
  if (w < 2) {
    wait_prog(ws, OFF_PPX + (u32)(cg + 5 * (s & 3)) * 128, 1, (u32)s, l, dead, 2);
    if (w == 0) wait_prog(ws, OFF_PPX, 32, (u32)s + 1, l, dead, 1, 128);
    else wait_prog(ws, OFF_PUM + (u32)cg * 2048, 16, (u32)s + 1, l, dead, 1, 128);
  }
  __syncthreads();
  u32x4 pr[10], ub[8];
#pragma unroll
  for (int i = 0; i < 8; ++i) pr[i] = ld16c(ws, tid * 16, OFF_PXX + (u32)(s * 65536 + i * 8192));
#pragma unroll
  for (int i = 0; i < 2; ++i) pr[8 + i] = ld16c(ws, tid * 16, OFF_PXM + (u32)(s * 16384 + i * 8192));
#pragma unroll
  for (int q = 0; q < 8; ++q) ub[q] = ld16c(ws, om + q * 512, OFF_UM + (u32)((s * 80 + 2 * G) * 4096));
#pragma unroll
  for (int i = 0; i < 10; ++i) *(u32x4*)(lds + (i * 512 + tid) * 16) = pr[i];
  __syncthreads();
  f32x4 acc[2] = {};
  {
    const unsigned char* px = lds + l * 16;
    const unsigned char* pm = lds + 65536 + (g * 16 + c16) * 16;
    auto rd = [&](int i) { return i < 64 ? *(const u32x4*)(px + i * 1024) : *(const u32x4*)(pm + (i & 1) * 8192 + ((i - 64) >> 1) * 1024); };
    u32x4 pb[12];
#pragma unroll
    for (int i = 0; i < 12; ++i) pb[i] = rd(i);
    SCHED_FENCE();
#pragma unroll
    for (int i = 0; i < 80; ++i) {
      acc[i & 1] = mfma(i < 64 ? ua[i >> 1] : ub[(i - 64) >> 1], pb[i % 12], acc[i & 1]);
      if (i + 12 < 80) pb[i % 12] = rd(i + 12);
      SCHED_FENCE();
    }
  }
#pragma unroll
  for (int tt = 0; tt < 2; ++tt) {
    u32x2 o = {pk2(acc[tt][0], acc[tt][1]), pk2(acc[tt][2], acc[tt][3])};
    *(u32x2*)(p.out + (32 * s + 16 * tt + c16) * 1280 + 640 + 16 * G + 4 * g) = o;
  }
}

DEV void prefetch_role(rsrc_t ws, int x, int k) {
  const int l = threadIdx.x & 63;
  const int w = __builtin_amdgcn_readfirstlane(threadIdx.x >> 6);
  if (w >= PFW) return;
  bool dead = false;
  u32 seen0 = 0, seen1 = 0;
  for (int s = 1; s < NSEG; ++s) {
    const u32 prog = wait_prog(ws, OFF_PUM + (u32)(x * 16 + k) * 128, 1, (u32)(s > PFD ? s - PFD : 0), l, dead, 1);
    if ((u32)s <= prog + 1) { s = (int)prog + 1; continue; }
    const u32 need = seg_need(s);
    if (seg_prod(s)) { if (seen1 < need) seen1 = wait_prog(ws, OFF_PVU + 128, 32, need, l, dead, 1); }
    else { if (seen0 < need) seen0 = wait_prog(ws, OFF_PVU, 32, need, l, dead, 1); }
#pragma nounroll
    for (int i = w; i < 14 * (8192 / (64 * PFSTR)); i += PFW) {
      const int u = k * 14 + i / (8192 / (64 * PFSTR));
      u32 o;
      if (u >= 40) o = ((u & 1) ? OFF_QX : OFF_KX) + (u32)((s * 32 + x * 8 + ((u - 40) >> 1)) * 8192);
      else o = OFF_UX + (u32)((s * 80 + x * 20) * 16384 + u * 8192);
      if (false) {}
      asm volatile("buffer_load_dword v255, %0, %1, %2 offen sc1\n\ts_waitcnt vmcnt(%3)"
                   :: "v"(l * PFSTR), "s"(ws), "s"(o + (u32)(i % (8192 / (64 * PFSTR))) * (64 * PFSTR)), "n"(PFQ) : "memory", "v255");
      __builtin_amdgcn_s_sleep(PFS);
    }
  }
  VM_DRAIN();
}

constexpr int T_SET = 56, T_F1 = 80, T_F2 = 80, T_F3 = 80, T_OUT = 320;
DEV int next_ticket(const Params& p, unsigned char* lds, u32 off) {
  u32* tk = (u32*)(lds + LDS_MAIN - 16);
  __syncthreads();
  if (threadIdx.x == 0) *tk = __hip_atomic_fetch_add((u32*)(p.ws + off), 1u, __ATOMIC_RELAXED, __HIP_MEMORY_SCOPE_AGENT);
  __syncthreads();
  return __builtin_amdgcn_readfirstlane(*tk);
}

DEV u32 claim_role(const Params& p, rsrc_t ws, unsigned char* lds) {
  u32* tk = (u32*)(lds + LDS_MAIN - 16);
  if (threadIdx.x < 64) {
    const int l = threadIdx.x;
    const u32 xcc = (XPERM >> (4 * ((u32)__builtin_amdgcn_s_getreg(20 | (3 << 11)) & XCCMASK))) & 7u, sl = blockIdx.x >> 3;
    u32 old = 0;
    if (l == 0) old = __hip_atomic_fetch_or((u32*)(p.ws + OFF_ROLE + xcc * 128), 1u << sl, __ATOMIC_RELAXED, __HIP_MEMORY_SCOPE_AGENT);
    old = __builtin_amdgcn_readfirstlane(old);
    u32 pos = xcc * 32 + sl;
    if (__builtin_expect((old >> sl) & 1u, 0)) {
      u32 k = 0;
      if (l == 0) k = __hip_atomic_fetch_add((u32*)(p.ws + OFF_ROLE + 1024), 1u, __ATOMIC_RELAXED, __HIP_MEMORY_SCOPE_AGENT);
      k = __builtin_amdgcn_readfirstlane(k);
      u32 c = 0;
#pragma nounroll
      for (u32 spins = 0; spins < SPIN_CAP; ++spins) {
        asm volatile("" ::: "memory");
        c = ld4c(ws, (u32)(l < 8 ? l : 8) * 128, OFF_ROLE);
        u32 sum = (u32)__builtin_amdgcn_readlane(c, 8);
        for (int j = 0; j < 8; ++j) sum += (u32)__builtin_popcount((u32)__builtin_amdgcn_readlane(c, j));
        if (sum >= 256) break;
        __builtin_amdgcn_s_sleep(4);
      }
      pos = 255;
#pragma nounroll
      for (u32 q = 0; q < 256; ++q) {
        const u32 m = (u32)__builtin_amdgcn_readlane(c, q >> 5);
        if (!((m >> (q & 31)) & 1u)) { if (k == 0) { pos = q; break; } --k; }
      }
    }
    if (l == 0) *tk = pos;
  }
  __syncthreads();
  return __builtin_amdgcn_readfirstlane(*tk);
}

__global__ __launch_bounds__(512, 2) void k_main(Params p, int mode) {
  extern __shared__ __attribute__((aligned(16))) unsigned char lds[];
  const rsrc_t ws = mk_rsrc(p.ws, WS_BYTES);
  const u32 cb = comm_base(p.ws);
  const int b = blockIdx.x;
  if (mode & 1) GSTAMP(0);
  if (b >= 4) {
    st16c(ws, cb + (u32)(b * 1024 + threadIdx.x) * 16, 0, (u32x4){SENT, SENT, SENT, SENT});
    st16c(ws, cb + (u32)(b * 1024 + 512 + threadIdx.x) * 16, 0, (u32x4){SENT, SENT, SENT, SENT});
  }
  if (b < 112) st16c(ws, OFF_SIDE + (u32)(b * 512 + threadIdx.x) * 16, 0, (u32x4){SENT, SENT, SENT, SENT});
  asm volatile("" ::: "memory");
  SCHED_FENCE();
  const u32 pos = claim_role(p, ws, lds);
  const int x = (int)(pos >> 5), slot = (int)(pos & 31);
  const int cidx = (x >= 4 || slot >= 28) ? 1000 : (slot < 8 ? x * 8 + slot : 32 + x * 20 + slot - 8);
  if (cidx < 112) {
    if (!(mode & 1)) return;
    if (cidx < 32) score_role(p, ws, cb, cidx, lds);
    else value_role(p, ws, cb, cidx - 32, lds);
    GSTAMP(3);
    return;
  }
  if (x < 4) {
    if (mode & 1) {
      init_part(p, ws, cb, x * 4 + slot - 28);
      prefetch_role(ws, x, slot - 28);
    }
    return;
  }
  if (x >= 4 && (mode & 2)) {
    const int h = (x - 4) * 8 + (slot >> 2);
    const bool bw = (slot & 1) && slot < 24;
    if (bw) b_unit(p, ws, 80 + (x - 4) * 12 + (slot >> 1), lds);
    if (slot & 2) vu_role(p, ws, h, slot & 1, lds, !bw);
    else qk_role(p, ws, h, slot & 1, lds, !bw);
    GSTAMP(2);
  }
  if (!(mode & 4)) return;
  const bool outonly = (slot & 3) == 3 && (slot & 4);
  if (!outonly) {
    int t = next_ticket(p, lds, OFF_TICKET) - T_SET;
    while (t < 0) { const int k = t + T_SET; if (k < 40) fw_task(p, ws, k); else wt_task(p, ws, k - 40, lds); t = next_ticket(p, lds, OFF_TICKET) - T_SET; }
    while (t < T_F1) { f1_task(p, ws, t % 5, t / 5, lds); t = next_ticket(p, lds, OFF_TICKET) - T_SET; }
    GSTAMP(4);
    while (t < T_F1 + T_F2) {
      const int q = t - T_F1;
      if (threadIdx.x < 64) { bool dead = false; wait_prog(ws, OFF_F1C + (u32)(q >> 4) * 128, 1, 16, threadIdx.x, dead, 1); }
      wait_fw(ws);
      fft_cols(p, ws, q, lds);
      VM_DRAIN();
      __syncthreads();
      if (threadIdx.x == 0) __hip_atomic_fetch_add((u32*)(p.ws + OFF_F2C), 1u, __ATOMIC_RELAXED, __HIP_MEMORY_SCOPE_AGENT);
      t = next_ticket(p, lds, OFF_TICKET) - T_SET;
    }
    GSTAMP(5);
    while (t < T_F1 + T_F2 + T_F3) { const int q = t - T_F1 - T_F2; f3_task(p, ws, q % 5, q / 5, lds); t = next_ticket(p, lds, OFF_TICKET) - T_SET; }
  }
  GSTAMP(6);
  for (int t = next_ticket(p, lds, OFF_TICKET2); t < T_OUT; t = next_ticket(p, lds, OFF_TICKET2)) out_task(p, ws, t / 5, t % 5, lds);
  GSTAMP(3);
}

__global__ __launch_bounds__(384) void k_zero(unsigned char* ws) {
  const rsrc_t r = mk_rsrc(ws, WS_BYTES);
  st16c(r, OFF_FLAGS + (u32)(blockIdx.x * 384 + threadIdx.x) * 16, 0, (u32x4){0, 0, 0, 0});
}

extern "C" void kernel_launch(void* const* d_in, const int* in_sizes, int n_in, void* d_out, int out_size, void* d_ws, size_t ws_size,
                              hipStream_t stream) {
  Params p;
  p.ax = (const u16*)d_in[0]; p.wr = (const u16*)d_in[1]; p.wi = (const u16*)d_in[2];
  p.x = (const u16*)d_in[3]; p.mem0 = (const u16*)d_in[4]; p.wq = (const u16*)d_in[5]; p.wk = (const u16*)d_in[6];
  p.wv = (const u16*)d_in[7]; p.wo = (const u16*)d_in[8];
  p.out = (u16*)d_out; p.ws = (unsigned char*)d_ws;
  (void)hipFuncSetAttribute((const void*)k_main, hipFuncAttributeMaxDynamicSharedMemorySize, LDS_MAIN);
  k_zero<<<8, 384, 0, stream>>>(p.ws);
  k_main<<<256, 512, LDS_MAIN, stream>>>(p, 7);
}
```

```cpp
#include <hip/hip_runtime.h>
#include <hip/hip_bf16.h>
#include <stdint.h>
#define BAR4
#define BULK1
#define SPREAD

typedef unsigned short u16;
typedef unsigned int u32;
typedef unsigned long long u64;
typedef __bf16 bf16x2 __attribute__((ext_vector_type(2)));
typedef __bf16 bf16x8 __attribute__((ext_vector_type(8)));
typedef __bf16 bf16x16 __attribute__((ext_vector_type(16)));
typedef float f32x2 __attribute__((ext_vector_type(2)));
typedef float f32x4 __attribute__((ext_vector_type(4)));
typedef u32 u32x2 __attribute__((ext_vector_type(2)));
typedef u32 u32x4 __attribute__((ext_vector_type(4)));
typedef u32 u32x8 __attribute__((ext_vector_type(8)));
typedef __amdgpu_buffer_rsrc_t rsrc_t;
#define DEV __device__ __forceinline__

constexpr int NSEG = 64;
constexpr u32 MB = 1u << 20;
constexpr u32 OFF_WF = 6 * MB;
constexpr u32 OFF_WOF = 21 * MB;
constexpr u32 OFF_BF = 26 * MB;
constexpr u32 OFF_XF = 51 * MB;
constexpr u32 OFF_AXF = 54 * MB;
constexpr u32 OFF_KX = 57 * MB;
constexpr u32 OFF_QX = 73 * MB;
constexpr u32 OFF_UX = 89 * MB;
constexpr u32 OFF_UM = 169 * MB;
constexpr u32 OFF_PXX = 189 * MB;
constexpr u32 OFF_PXM = 193 * MB;
constexpr u32 OFF_FX = 194 * MB;
constexpr u32 OFF_FY = 200 * MB;
constexpr u32 OFF_FLAGS = 206 * MB;
constexpr u32 OFF_FW1 = 208 * MB;
constexpr u32 OFF_FW3 = 210 * MB;
constexpr u32 OFF_SIDE = 212 * MB;
constexpr u32 SIDE_STEP = 112 * 128;
constexpr u32 OFF_WT = 213 * MB;
constexpr u32 WS_BYTES = 216 * MB;
constexpr u32 STEP_BYTES = 65536;
constexpr u32 SENT = 0xFFFFFFFFu;
constexpr u32 SPIN_CAP = 1u << 16;
constexpr u32 OFF_PQK = OFF_FLAGS + 0xA000;
constexpr u32 OFF_PVU = OFF_FLAGS + 0xB000;
constexpr u32 OFF_PPX = OFF_FLAGS;
constexpr u32 OFF_PUM = OFF_FLAGS + 0x1000;
constexpr u32 OFF_TICKET = OFF_FLAGS + 0xB400;
constexpr u32 OFF_F1C = OFF_FLAGS + 0xB480;
constexpr u32 OFF_F2C = OFF_FLAGS + 0xB800;
constexpr u32 OFF_FWC = OFF_FLAGS + 0xB880;
constexpr u32 OFF_TICKET2 = OFF_FLAGS + 0xB900;
constexpr u32 OFF_INITC = OFF_FLAGS + 0xBC00;
constexpr u32 OFF_ROLE = OFF_FLAGS + 0x9000;
constexpr u32 OFF_XFC = OFF_FLAGS + 0xBF00;
constexpr u32 OFF_BC = OFF_FLAGS + 0xBE00;
constexpr u32 FLAG_BYTES = 0xC000;
constexpr u32 OFF_VOTE = OFF_FLAGS + 0xBA00;
constexpr int LDS_MAIN = 97 * 1024;
#define HS0 6
#define HS1 16
constexpr int NHELP = (HS1 - HS0) / 2;
__host__ __device__ constexpr int seg_prod(int s) { return (s >= HS0 && s < HS1) ? (s & 1) : 0; }
__host__ __device__ constexpr u32 seg_need(int s) {
  return s < HS0 ? (u32)s + 1 : (s < HS1 ? ((s & 1) ? (u32)((s - HS0) >> 1) + 1 : (u32)(HS0 + ((s - HS0) >> 1)) + 1) : (u32)(s - NHELP) + 1);
}
__host__ __device__ constexpr int tile_seg(int par, int i) {
  return par ? HS0 + 2 * i + 1 : (i < HS0 ? i : (i < HS0 + NHELP ? HS0 + 2 * (i - HS0) : i + NHELP));
}

struct Params {
  const u16* ax; const u16* wr; const u16* wi;
  const u16* x; const u16* mem0; const u16* wq; const u16* wk; const u16* wv; const u16* wo;
  u16* out; unsigned char* ws;
};

DEV rsrc_t mk_rsrc(const void* p, u32 bytes) { return __builtin_amdgcn_make_buffer_rsrc((void*)p, 0, bytes, 0x00020000); }
DEV u32x4 ld16(rsrc_t r, u32 vo, u32 so) { return __builtin_amdgcn_raw_buffer_load_b128(r, vo, so, 0); }
DEV u32x4 ld16c(rsrc_t r, u32 vo, u32 so) { return __builtin_amdgcn_raw_buffer_load_b128(r, vo, so, 16); }
DEV u32x2 ld8c(rsrc_t r, u32 vo, u32 so) { return __builtin_amdgcn_raw_buffer_load_b64(r, vo, so, 16); }
DEV u32 ld4c(rsrc_t r, u32 vo, u32 so) { return __builtin_amdgcn_raw_buffer_load_b32(r, vo, so, 16); }
DEV void st16(rsrc_t r, u32 vo, u32 so, u32x4 v) { __builtin_amdgcn_raw_buffer_store_b128(v, r, vo + so, 0, 0); }
DEV void st16c(rsrc_t r, u32 vo, u32 so, u32x4 v) { __builtin_amdgcn_raw_buffer_store_b128(v, r, vo + so, 0, 16); }
DEV void st8c(rsrc_t r, u32 vo, u32 so, u32x2 v) { __builtin_amdgcn_raw_buffer_store_b64(v, r, vo, so, 16); }
DEV void st4c(rsrc_t r, u32 vo, u32 so, u32 v) { __builtin_amdgcn_raw_buffer_store_b32(v, r, vo, so, 16); }
DEV u32 pk2(float a, float b) { bf16x2 v = {(__bf16)a, (__bf16)b}; return __builtin_bit_cast(u32, v); }
DEV u32 pk2s(float a, float b) { u32 v = pk2(a, b); return v < 0xFFFFFFFEu ? v : 0xFFFFFFFEu; }
DEV u32 pk2lo(float a, float b, u32 hi) { return pk2(a - __uint_as_float(hi << 16), b - __uint_as_float(hi & 0xFFFF0000u)); }
DEV f32x4 mfma(u32x4 a, u32x4 b, f32x4 c) {
  return __builtin_amdgcn_mfma_f32_16x16x32_bf16(__builtin_bit_cast(bf16x8, a), __builtin_bit_cast(bf16x8, b), c, 0, 0, 0);
}
DEV f32x4 smfma(u32x4 a, u32x8 b, f32x4 c, int idx) {
  return __builtin_amdgcn_smfmac_f32_16x16x64_bf16(__builtin_bit_cast(bf16x8, a), __builtin_bit_cast(bf16x16, b), c, idx, 0, 0);
}
DEV u32x8 cat(u32x4 a, u32x4 b) { return __builtin_shufflevector(a, b, 0, 1, 2, 3, 4, 5, 6, 7); }
DEV u32 comm_base(const unsigned char* ws) { return (u32)((2 * MB - ((uintptr_t)ws & (2 * MB - 1))) & (2 * MB - 1)); }
DEV u64 stamp_cyc() { u64 t; asm volatile("s_memtime %0\n\ts_waitcnt lgkmcnt(0)" : "=s"(t) :: "memory"); return t; }
DEV u32 umax3(u32 a, u32 b, u32 c) { u32 m = a > b ? a : b; return m > c ? m : c; }
DEV float fmx(float a, float b) { return __builtin_elementwise_maximum(a, b); }
#define SCHED_FENCE() __builtin_amdgcn_sched_barrier(0)
#define VM_DRAIN() asm volatile("s_waitcnt vmcnt(0)" ::: "memory")
#define RAW_BARRIER() do { asm volatile("s_waitcnt lgkmcnt(0)" ::: "memory"); __builtin_amdgcn_s_barrier(); asm volatile("" ::: "memory"); } while (0)

#define STAMP(ev)
#define STAMPV(ev, val)
#define COUNT(ev, val)
#define GSTAMP(slot)

typedef short s16x4 __attribute__((ext_vector_type(4)));
#define LAS __attribute__((address_space(3)))
DEV u32x4 tr_frag(const unsigned char* a0, const unsigned char* a1) {
  const u32x2 x = __builtin_bit_cast(u32x2, __builtin_amdgcn_ds_read_tr16_b64_v4i16((LAS s16x4*)a0));
  const u32x2 y = __builtin_bit_cast(u32x2, __builtin_amdgcn_ds_read_tr16_b64_v4i16((LAS s16x4*)a1));
  return (u32x4){x.x, x.y, y.x, y.y};
}
DEV float dpp_ror8(float x) { return __uint_as_float(__builtin_amdgcn_update_dpp(0, (int)__float_as_uint(x), 0x128, 0xF, 0xF, true)); }
DEV float red_max_g(float m) {
  auto a = __builtin_amdgcn_permlane16_swap(__float_as_uint(m), __float_as_uint(m), false, false);
  m = fmx(__uint_as_float(a[0]), __uint_as_float(a[1]));
  auto b = __builtin_amdgcn_permlane32_swap(__float_as_uint(m), __float_as_uint(m), false, false);
  return fmx(__uint_as_float(b[0]), __uint_as_float(b[1]));
}
DEV float red_sum_g(float m) {
  auto a = __builtin_amdgcn_permlane16_swap(__float_as_uint(m), __float_as_uint(m), false, false);
  m = __uint_as_float(a[0]) + __uint_as_float(a[1]);
  auto b = __builtin_amdgcn_permlane32_swap(__float_as_uint(m), __float_as_uint(m), false, false);
  return __uint_as_float(b[0]) + __uint_as_float(b[1]);
}
DEV float red_max(float m) { return red_max_g(fmx(m, dpp_ror8(m))); }
DEV float red_sum(float m) { return red_sum_g(m + dpp_ror8(m)); }

#define NPROBE 3
DEV u32 probe(rsrc_t ws, u32 cb, int idx, int page, int l) {
  const u32 vo = (l == 0) ? 0u : 0x80000000u;
  const u32 so = cb + (u32)(page * 32 + 30) * STEP_BYTES + (u32)(2 + (idx >> 6)) * 16384 + (u32)(idx & 63) * 128;
  u32 b0 = ~0u, b1 = ~0u;
#pragma nounroll
  for (int rep = 0; rep < 2 * NPROBE; ++rep) {
    u64 t0 = stamp_cyc();
    st4c(ws, vo, so + (u32)(rep & 1) * 8192, SENT);
    VM_DRAIN();
    u64 t1 = stamp_cyc();
    u32 d = (u32)(t1 - t0);
    if (rep & 1) b1 = d < b1 ? d : b1; else b0 = d < b0 ? d : b0;
  }
  return b1 < b0 ? 1u : 0u;
}

DEV u32 xcc_group() { return ((u32)__builtin_amdgcn_s_getreg(20 | (3 << 11)) >> 2) & 3u; }
DEV void vote(const Params& p, int page, u32 r, int l) {
  if (l == 0) __hip_atomic_fetch_add((u32*)(p.ws + OFF_VOTE + (xcc_group() * 2 + page) * 64), r + 0x10000u, __ATOMIC_RELAXED, __HIP_MEMORY_SCOPE_AGENT);
}
DEV u32 vote_result(rsrc_t ws, int l) {
  const u32 x = ld4c(ws, (u32)(l & 1) * 64, OFF_VOTE + xcc_group() * 128);
  const u32 m = (2 * (x & 0xFFFFu) > (x >> 16)) ? 1u : 0u;
  return (u32)__builtin_amdgcn_readlane(m, 0) | ((u32)__builtin_amdgcn_readlane(m, 1) << 1);
}
#define D1 130
#define D2 14
#define D3 56
#define D4 12
#define DV 15
#define DX 25
#define DL 5
#define PFD 2
#define DLS 16
#define THR 6
#define THR_S2(x) #x
#define THR_S(x) THR_S2(x)
#define THROTTLE(i) asm volatile("s_waitcnt vmcnt(" THR_S(THR) ")" ::: "memory")
#define UMPOS 0
#define SDQ 0
#define SDV 0
#define XPERM 0x76325410u
#define XCCMASK 7u
#define PSLEEP 0
#define PFW 1
#define PFQ 4
#define PFS 2
#define PFSTR 128
#define PROT v
#define KQPOS 0
#define DA 12
DEV u32 rtime() { return (u32)__builtin_amdgcn_s_memrealtime(); }
DEV void wait_until(u32 T) {
  u32 r = rtime();
  if ((int)(T - r) < 28) {
#pragma nounroll
    for (int i = 0; i < 64 && (int)(T - r) > 0; ++i) r = rtime();
    return;
  }
#pragma nounroll
  for (int i = 0; i < 4096 && (int)(T - r) > 35; ++i) r = rtime();
  u32 n;
  asm volatile(
    "s_memrealtime s[80:81]\n\ts_nop 2\n\ts_memrealtime s[82:83]\n\ts_nop 2\n\ts_memrealtime s[84:85]\n\ts_nop 2\n\ts_memrealtime s[86:87]\n\ts_nop 2\n\t"
    "s_memrealtime s[88:89]\n\ts_nop 2\n\ts_memrealtime s[90:91]\n\ts_nop 2\n\ts_memrealtime s[92:93]\n\ts_nop 2\n\ts_memrealtime s[94:95]\n\ts_waitcnt lgkmcnt(0)\n\t"
    "s_mov_b32 s81, 0\n\t"
    "s_cmp_eq_u32 s82, s80\n\ts_addc_u32 s81, s81, 0\n\t"
    "s_cmp_eq_u32 s84, s80\n\ts_addc_u32 s81, s81, 0\n\t"
    "s_cmp_eq_u32 s86, s80\n\ts_addc_u32 s81, s81, 0\n\t"
    "s_cmp_eq_u32 s88, s80\n\ts_addc_u32 s81, s81, 0\n\t"
    "s_cmp_eq_u32 s90, s80\n\ts_addc_u32 s81, s81, 0\n\t"
    "s_cmp_eq_u32 s92, s80\n\ts_addc_u32 s81, s81, 0\n\t"
    "s_cmp_eq_u32 s94, s80\n\ts_addc_u32 s81, s81, 0\n\t"
    "s_sub_u32 %0, %1, s80\n\t"
    "s_mul_i32 %0, %0, 3\n\t"
    "s_lshl1_add_u32 %0, s81, %0\n\t"
    "s_sub_i32 %0, %0, 64\n\t"
    "s_max_i32 %0, %0, 0\n\t"
    "s_min_i32 %0, %0, 63\n\t"
    "s_bitcmp1_b32 %0, 0\n\ts_cbranch_scc0 1f\n\ts_nop 5\n\t1:\n\t"
    "s_bitcmp1_b32 %0, 1\n\ts_cbranch_scc0 2f\n\ts_nop 7\n\t2:\n\t"
    "s_bitcmp1_b32 %0, 2\n\ts_cbranch_scc0 3f\n\ts_nop 11\n\t3:\n\t"
    "s_bitcmp1_b32 %0, 3\n\ts_cbranch_scc0 4f\n\ts_nop 15\n\ts_nop 3\n\t4:\n\t"
    "s_bitcmp1_b32 %0, 4\n\ts_cbranch_scc0 5f\n\ts_nop 15\n\ts_nop 15\n\ts_nop 3\n\t5:\n\t"
    "s_bitcmp1_b32 %0, 5\n\ts_cbranch_scc0 6f\n\ts_nop 15\n\ts_nop 15\n\ts_nop 15\n\ts_nop 15\n\ts_nop 3\n\t6:\n\t"
    : "=&s"(n) : "s"(T)
    : "scc", "memory", "s80", "s81", "s82", "s83", "s84", "s85", "s86", "s87", "s88", "s89", "s90", "s91", "s92", "s93", "s94", "s95");
}
template <int CTRL> DEV int dpp_i(int x) { return __builtin_amdgcn_update_dpp(x, x, CTRL, 0xF, 0xF, false); }
DEV int row_max(int d) {
  d = max(d, dpp_i<0x121>(d));
  d = max(d, dpp_i<0x122>(d));
  d = max(d, dpp_i<0x124>(d));
  return max(d, dpp_i<0x128>(d));
}
DEV int wave_max(int d) {
  d = row_max(d);
  return max(max(__builtin_amdgcn_readlane(d, 0), __builtin_amdgcn_readlane(d, 16)), max(__builtin_amdgcn_readlane(d, 32), __builtin_amdgcn_readlane(d, 48)));
}
DEV u32 fine_now() {
  u32 n;
  asm volatile(
    "s_memrealtime s[80:81]\n\ts_nop 2\n\ts_memrealtime s[82:83]\n\ts_nop 2\n\ts_memrealtime s[84:85]\n\ts_nop 2\n\ts_memrealtime s[86:87]\n\ts_nop 2\n\t"
    "s_memrealtime s[88:89]\n\ts_nop 2\n\ts_memrealtime s[90:91]\n\ts_nop 2\n\ts_memrealtime s[92:93]\n\ts_waitcnt lgkmcnt(0)\n\t"
    "s_mov_b32 s81, 0\n\t"
    "s_cmp_eq_u32 s82, s80\n\ts_addc_u32 s81, s81, 0\n\t"
    "s_cmp_eq_u32 s84, s80\n\ts_addc_u32 s81, s81, 0\n\t"
    "s_cmp_eq_u32 s86, s80\n\ts_addc_u32 s81, s81, 0\n\t"
    "s_cmp_eq_u32 s88, s80\n\ts_addc_u32 s81, s81, 0\n\t"
    "s_cmp_eq_u32 s90, s80\n\ts_addc_u32 s81, s81, 0\n\t"
    "s_cmp_eq_u32 s92, s80\n\ts_addc_u32 s81, s81, 0\n\t"
    "s_mul_i32 s81, s81, 11\n\t"
    "s_lshr_b32 s81, s81, 4\n\t"
    "s_add_u32 %0, s80, 4\n\t"
    "s_sub_u32 %0, %0, s81\n\t"
    : "=&s"(n) :
    : "scc", "memory", "s80", "s81", "s82", "s83", "s84", "s85", "s86", "s87", "s88", "s89", "s90", "s91", "s92", "s93");
  return n;
}
struct Side { u32x2 a, b; };
DEV Side load_side(rsrc_t ws, int s, int l) {
  Side t;
  t.a = ld8c(ws, (u32)l * 128, OFF_SIDE + (u32)s * SIDE_STEP);
  t.b = ld8c(ws, (u32)(64 + (l & 15)) * 128, OFF_SIDE + (u32)s * SIDE_STEP);
  return t;
}
DEV void schedule(rsrc_t ws, int s, int l, Side& t, u32& Rm, u32& Rp, bool& dead) {
  if (__builtin_expect(__any(umax3(t.a.x, t.a.y, t.b.x > t.b.y ? t.b.x : t.b.y) == SENT), 0)) {
#pragma nounroll
    for (u32 spins = 0; !dead; ++spins) {
      asm volatile("" ::: "memory");
      t = load_side(ws, s, l);
      if (__all(umax3(t.a.x, t.a.y, t.b.x > t.b.y ? t.b.x : t.b.y) != SENT)) break;
      if (spins > SPIN_CAP) dead = true;
    }
  }
  u32 B = Rm;
  if (__builtin_expect(__any((t.a.x | t.b.x) != 0), 0)) {
    const u32 ref = rtime();
    const int dx = t.a.x ? (int)(t.a.x - ref) : -(1 << 30), dy = t.b.x ? (int)(t.b.x - ref) : -(1 << 30);
    B = ref + (u32)wave_max(max(dx, dy)) + D4 + DX;
  }
  const u32 d = __builtin_amdgcn_readfirstlane(t.a.y);
  Rp = B + ((d >> 10) & 1023u);
  Rm = Rp + ((d >> 20) & 1023u);
}

DEV u32 gather_mem(rsrc_t ws, u32 sbp, int wr, unsigned char* dA, unsigned char* dB, int l, bool& dead) {
  u32x4 a = ld16c(ws, (u32)l * 16, sbp + (u32)wr * 1024);
  u32 b = ld4c(ws, (u32)l * 4, sbp + 16384 + (u32)wr * 256);
  SCHED_FENCE();
  *(u32x4*)dA = a;
  *(u32*)dB = b;
  u32 m = umax3(umax3(a.x, a.y, a.z), a.w, b);
  u32 spins = 0;
  if (__builtin_expect(__any(m == SENT), 0)) {
#pragma nounroll
    while (!dead) {
      ++spins;
      asm volatile("" ::: "memory");
      a = ld16c(ws, (u32)l * 16, sbp + (u32)wr * 1024);
      b = ld4c(ws, (u32)l * 4, sbp + 16384 + (u32)wr * 256);
      m = umax3(umax3(a.x, a.y, a.z), a.w, b);
      if (__all(m != SENT)) break;
      if (spins > SPIN_CAP) dead = true;
    }
    *(u32x4*)dA = a;
    *(u32*)dB = b;
  }
  return spins;
}
DEV u32 wait_prog(rsrc_t ws, u32 off, int n, u32 need, int l, bool& dead, int slp, u32 stride = 4) {
  u32 mn = 0;
#pragma nounroll
  for (u32 spins = 0;; ++spins) {
    asm volatile("" ::: "memory");
    const u32 x = ld4c(ws, (u32)(l < n ? l : 0) * stride, off);
    int d = -(int)x;
    d = row_max(d);
    mn = (u32)(-max(__builtin_amdgcn_readlane(d, 0), __builtin_amdgcn_readlane(d, 16)));
    if (mn >= need || dead) break;
    if (spins > SPIN_CAP) { dead = true; break; }
    if (slp == 2) __builtin_amdgcn_s_sleep(32); else if (slp) __builtin_amdgcn_s_sleep(6); else __builtin_amdgcn_s_sleep(1);
  }
  asm volatile("" ::: "memory");
  return mn;
}

DEV void init_signal(const Params& p, bool on) {
  if (on && threadIdx.x == 0) __hip_atomic_fetch_add((u32*)(p.ws + OFF_INITC + (blockIdx.x & 7) * 64), 1u, __ATOMIC_RELAXED, __HIP_MEMORY_SCOPE_AGENT);
}
DEV int wrow(int r) { return r * 288 + (r >> 3) * 128; }
DEV void load_qk(const Params& p, int h, int w, int l, unsigned char* lds, u32x4 (&wt)[2][20], bool sig) {
  const rsrc_t src = mk_rsrc((w >> 2) ? p.wk : p.wq, 640 * 8192);
  u32x4 st[40];
#pragma unroll
  for (int i = 0; i < 40; ++i)
    st[i] = ld16(src, (u32)((l >> 4) * 8192 + (l & 15) * 16), (u32)((32 * (w & 3) + (i >> 3) * 128 + (i & 7) * 4) * 8192 + h * 256));
  unsigned char* img = lds + (w >> 2) * 38912;
  unsigned char* wp = img + wrow(32 * (w & 3)) + (l >> 4) * 288 + (l & 15) * 16;
  const unsigned char* rp = img + wrow(8 * (l >> 4)) + ((l >> 2) & 3) * 288 + (w & 3) * 64 + (l & 3) * 8;
#pragma unroll
  for (int r = 0; r < 5; ++r) {
#pragma unroll
    for (int i = 0; i < 8; ++i) *(u32x4*)(wp + wrow(4 * i)) = st[r * 8 + i];
    __syncthreads();
    if (r == 0) init_signal(p, sig);
#pragma unroll
    for (int kk = 0; kk < 4; ++kk)
#pragma unroll
      for (int i = 0; i < 2; ++i) wt[i][4 * r + kk] = tr_frag(rp + wrow(32 * kk) + i * 32, rp + wrow(32 * kk) + 4 * 288 + i * 32);
    __syncthreads();
  }
}
DEV void load_vu(const Params& p, int h, int w, int l, unsigned char* lds, u32x4 (&wv)[20], u32x4 (&wo)[5][4], bool sig) {
  const rsrc_t sv = mk_rsrc(p.wv, 640 * 8192), so = mk_rsrc(p.wo, 640 * 8192);
  u32x4 st[40];
#pragma unroll
  for (int i = 0; i < 20; ++i)
    st[i] = ld16(sv, (u32)((l >> 4) * 8192 + (l & 15) * 16), (u32)((40 * w + (i / 10) * 320 + (i % 10) * 4) * 8192 + h * 256));
#pragma unroll
  for (int i = 0; i < 20; ++i) st[20 + i] = ld16(so, (u32)l * 16, (u32)(h * 163840 + (w * 640 + (i / 10) * 5120 + (i % 10) * 64) * 16));
  unsigned char* wp = lds + wrow(40 * w) + (l >> 4) * 288 + (l & 15) * 16;
  const unsigned char* rp = lds + wrow(8 * (l >> 4)) + ((l >> 2) & 3) * 288 + w * 32 + (l & 3) * 8;
#pragma unroll
  for (int r = 0; r < 2; ++r) {
#pragma unroll
    for (int i = 0; i < 10; ++i) *(u32x4*)(wp + wrow(4 * i)) = st[r * 10 + i];
    __syncthreads();
    if (r == 0) init_signal(p, sig);
#pragma unroll
    for (int kk = 0; kk < 10; ++kk) wv[10 * r + kk] = tr_frag(rp + wrow(32 * kk), rp + wrow(32 * kk) + 4 * 288);
    __syncthreads();
  }
  const unsigned char* ro = lds + (4 * (l >> 4) + ((l >> 2) & 3)) * 1312 + w * 160 + (l & 3) * 8;
#pragma unroll
  for (int r = 0; r < 2; ++r) {
#pragma unroll
    for (int i = 0; i < 10; ++i) {
      const int c = (w * 10 + i) * 64 + l;
      *(u32x4*)(lds + (c / 80) * 1312 + (c % 80) * 16) = st[20 + r * 10 + i];
    }
    __syncthreads();
#pragma unroll
    for (int kk = 0; kk < 2; ++kk)
#pragma unroll
      for (int ct = 0; ct < 5; ++ct) wo[ct][2 * r + kk] = tr_frag(ro + 32 * kk * 1312 + ct * 32, ro + (32 * kk + 16) * 1312 + ct * 32);
    __syncthreads();
  }
}

DEV void count_done(const Params& p, u32 off) {
  VM_DRAIN();
  __syncthreads();
  if (threadIdx.x == 0) __hip_atomic_fetch_add((u32*)(p.ws + off), 1u, __ATOMIC_RELAXED, __HIP_MEMORY_SCOPE_AGENT);
}
DEV void wait_init(rsrc_t ws) {
  if (threadIdx.x < 64) { bool dead = false; wait_prog(ws, OFF_XFC, 1, 16, threadIdx.x, dead, 0); }
  __syncthreads();
}
DEV void b_unit(const Params& p, rsrc_t ws, int u, unsigned char* lds) {
  const int tid = threadIdx.x, l = tid & 63, c16 = l & 15, g = l >> 4;
  const int w = __builtin_amdgcn_readfirstlane(tid >> 6);
  const int h = u >> 2, dh = (u >> 1) & 1, ch = u & 1;
  const rsrc_t rv = mk_rsrc(p.wv, 640 * 8192), ro = mk_rsrc(p.wo, 640 * 8192);
  u32x4 wo_r[10];
#pragma unroll
  for (int i = 0; i < 10; ++i) {
    const int c = tid + 512 * i, e = c / 40, cc = c % 40;
    wo_r[i] = ld16(ro, (u32)(e * 1280 + cc * 16), (u32)(h * 163840 + ch * 640));
  }
  u32x4 a[3][2][4];
#pragma unroll
  for (int i = 0; i < 3; ++i) {
    const int bl = (w & 3) + 4 * i;
    const u32 so = bl < 10 ? (u32)((32 * (10 * dh + bl)) * 8192 + h * 256) : 0x80000000u;
#pragma unroll
    for (int t = 0; t < 2; ++t)
#pragma unroll
      for (int ks = 0; ks < 4; ++ks)
        a[i][t][ks] = ld16(rv, (u32)((8 * (c16 >> 2) + (c16 & 3) + 4 * t) * 8192 + g * 16 + ks * 64), so);
  }
  SCHED_FENCE();
#pragma unroll
  for (int i = 0; i < 10; ++i) {
    const int c = tid + 512 * i, e = c / 40, cc = c % 40;
    *(u32x4*)(lds + e * 672 + (e >> 3) * 128 + cc * 16) = wo_r[i];
  }
  __syncthreads();
  init_signal(p, true);
  const u32 vo = (u32)(((c16 >> 1) & 1) * (16 * 20 * 1024) + (16 * g + 8 * (h & 1) + 2 * (c16 >> 2) + (c16 & 1)) * 16);
#pragma nounroll
  for (int half = 0; half < 2; ++half) {
    u32x4 bf[5][4];
    const unsigned char* rp = lds + (8 * g + ((l >> 2) & 3)) * 672 + g * 128 + half * 320 + (w >> 2) * 160 + (l & 3) * 8;
#pragma unroll
    for (int ct = 0; ct < 5; ++ct)
#pragma unroll
      for (int ks = 0; ks < 4; ++ks) bf[ct][ks] = tr_frag(rp + ks * (32 * 672 + 512) + ct * 32, rp + ks * (32 * 672 + 512) + 4 * 672 + ct * 32);
#pragma unroll
    for (int i = 0; i < 3; ++i) {
      const int bl = (w & 3) + 4 * i;
      if (bl < 10) {
#pragma unroll
        for (int ct = 0; ct < 5; ++ct) {
          f32x4 c0 = {0, 0, 0, 0}, c1 = {0, 0, 0, 0};
#pragma unroll
          for (int ks = 0; ks < 4; ++ks) { c0 = mfma(a[i][0][ks], bf[ct][ks], c0); c1 = mfma(a[i][1][ks], bf[ct][ks], c1); }
          u32x4 o = {pk2(c0[0], c0[1]), pk2(c0[2], c0[3]), pk2(c1[0], c1[1]), pk2(c1[2], c1[3])};
          st16c(ws, vo, OFF_BF + (u32)(((2 * (20 * ch + 10 * half + 5 * (w >> 2) + ct) * 16 + (h >> 1)) * 20 + 10 * dh + bl) * 1024), o);
        }
      }
    }
  }
  count_done(p, OFF_BC + (u32)ch * 128);
}
DEV void init_part(const Params& p, rsrc_t ws, u32 cb, int k) {
  const int tid = threadIdx.x;
  u32x4 xw[20];
#pragma unroll
  for (int i = 0; i < 20; ++i) {
    const int seg = k + 16 * (i / 5), r = (i % 5) * 512 + tid, tt = r / 1280, ks = (r % 1280) >> 6, ln = r & 63;
    xw[i] = *(const u32x4*)(p.x + (32 * seg + 16 * tt + (ln & 15)) * 640 + 32 * ks + 8 * (ln >> 4));
    if (i == 4) SCHED_FENCE();
  }
  if (tid < 256) {
    const u32 o = (u32)(k * 256 + tid) * 16;
    const u32 L = (o >> 14) * 8192 + (o & 8191);
    u32x4 v = {SENT, SENT, SENT, SENT};
    if (L < 10240) {
      const int vv = L >> 7, t = (L >> 4) & 7;
      const u16* m = p.mem0 + t * 640 + 16 * (vv >> 1) + 2 * (vv & 1);
      for (int q = 0; q < 4; ++q) { u32 x = *(const u32*)(m + 4 * q); v[q] = x < 0xFFFFFFFEu ? x : 0xFFFFFFFEu; }
    }
    st16c(ws, cb + o, 0, v);
  }
#pragma unroll
  for (int i = 0; i < 5; ++i) st16c(ws, (u32)(i * 512 + tid) * 16, OFF_XF + (u32)k * 40960, xw[i]);
  count_done(p, OFF_XFC);
  init_signal(p, true);
#pragma unroll
  for (int i = 5; i < 20; ++i) st16c(ws, (u32)((i % 5) * 512 + tid) * 16, OFF_XF + (u32)(k + 16 * (i / 5)) * 40960, xw[i]);
  count_done(p, OFF_XFC + 64);
}

constexpr float CEXP = 0.08838834764831845f * 1.4426950408889634f;
constexpr float NEGS = -1.0e9f / 0.08838834764831845f;
constexpr int QS = 288;

DEV void read_memf(const unsigned char* memf, int l, u32x4 (&a)[10]) {
#pragma unroll
  for (int ks = 0; ks < 10; ++ks) a[ks] = *(const u32x4*)(memf + ks * 1024 + l * 16);
}
DEV void project2(const u32x4 (&a)[10], const u32x8 (&wt)[2][10], int sidx, unsigned char* dst) {
  f32x4 c0 = {0, 0, 0, 0}, c1 = {0, 0, 0, 0};
#pragma unroll
  for (int ks = 0; ks < 10; ++ks) {
    c0 = smfma(a[ks], wt[0][ks], c0, sidx);
    c1 = smfma(a[ks], wt[1][ks], c1, sidx);
  }
  *(__bf16*)(dst) = (__bf16)(c0[0] + c0[1]);
  *(__bf16*)(dst + QS) = (__bf16)(c0[2] + c0[3]);
  *(__bf16*)(dst + 8) = (__bf16)(c1[0] + c1[1]);
  *(__bf16*)(dst + 8 + QS) = (__bf16)(c1[2] + c1[3]);
}

DEV void score_role(const Params& p, rsrc_t ws, u32 cb, int h, unsigned char* lds) {
  const int tid = threadIdx.x, l = tid & 63, c16 = l & 15, g = l >> 4;
  const int w = __builtin_amdgcn_readfirstlane(tid >> 6);
  unsigned char* memf = lds;
  unsigned char* qs = lds + 10240;
  unsigned char* kms = qs + 8 * QS;
  u32* ctl = (u32*)(lds + 15360);
  unsigned char* kq = lds + 16384;
  u32x8 wt[2][10];
  {
    u32x4 wf[2][20];
    load_qk(p, h, w, l, lds, wf, true);
#pragma unroll
    for (int i = 0; i < 2; ++i)
#pragma unroll
      for (int ks = 0; ks < 10; ++ks) wt[i][ks] = cat(wf[i][2 * ks], wf[i][2 * ks + 1]);
  }
  GSTAMP(1);
  bool dead = false;
  if (w == 0) wait_prog(ws, OFF_INITC, 8, 32, l, dead, 0, 64);
  __syncthreads();
  if (tid < 2) ctl[2 + tid] = 0;
  u32 seen0 = 0, seen1 = 0;
  const u32 kq_src = ((w & 2) ? OFF_QX : OFF_KX) + (u32)(h * 8192 + (w & 1) * 4096);
  if (w < 2) { u32 r = probe(ws, cb, h, w, l); vote(p, w, r, l); if (l == 0) ctl[4 + w] = r; }
  else if (w >= 4) {
    seen0 = wait_prog(ws, OFF_PQK + (u32)h * 128, 1, 1, l, dead, 0);
#pragma unroll
    for (int i = 0; i < 4; ++i) *(u32x4*)(kq + (w - 4) * 4096 + i * 1024 + l * 16) = ld16c(ws, l * 16 + i * 1024, kq_src);
  }
  __syncthreads();
  GSTAMP(2);
  u32 parmask = __builtin_amdgcn_readfirstlane(ctl[4] | (ctl[5] << 1));
  const int sidx = (l & 1) ? 0xEEEE : 0x4444;
  unsigned char* qk_w = qs + (w >> 2) * 8 * QS + 2 * g * QS + (32 * (w & 3) + 8 * (c16 >> 2) + (c16 & 3)) * 2;
  const unsigned char* km_r = kms + (2 * (c16 >> 2) + (c16 & 1)) * QS + g * 16;
  const int wr = (w + h) & 7;
  unsigned char* gA = memf + wr * 1024 + g * 256 + (l & 7) * 32 + ((l >> 3) & 1) * 16;
  unsigned char* gB = memf + 8192 + wr * 256 + ((l >> 2) & 7) * 32 + (l >> 5) * 16 + (l & 3) * 4;
  if (w == 0) {
    const unsigned char* q_r = qs + (c16 & 7) * QS + g * 16;
    const bool hb = c16 >= 8;
    const u32 xo = (u32)(((h >> 1) & 7) * 1024 + g * 256 + (h & 1) * 128 + (c16 & 7) * 16 + (c16 >> 3) * 8);
    const u32 mo = (u32)((c16 >> 3) * 8192 + h * 128 + (c16 & 7) * 16 + g * 4);
    for (int s = 0; s < NSEG; ++s) {
      const u32 sb = cb + (u32)s * STEP_BYTES;
      if (s == 1) parmask = vote_result(ws, l);
      const u32 par = ((parmask >> (s >> 5)) & 1) * 8192;
      const unsigned char* kxs = kq + (s & 1) * 16384;
      __syncthreads();
      { u32 sp = gather_mem(ws, sb + par, wr, gA, gB, l, dead); COUNT(5, sp); if (sp && l == 0) ctl[2 + (s & 1)] = (u32)s; }
      __syncthreads();
      { u32x4 a[10]; read_memf(memf, l, a); SCHED_FENCE(); project2(a, wt, sidx, qk_w); }
      u32x4 kx[8];
#pragma unroll
      for (int i = 0; i < 8; ++i) kx[i] = *(const u32x4*)(kxs + i * 1024 + l * 16);
      __syncthreads();
      if (s == NSEG - 1) break;
      u32x4 qf[4], km[4];
#pragma unroll
      for (int ks = 0; ks < 4; ++ks) { qf[ks] = *(const u32x4*)(q_r + ks * 64); km[ks] = *(const u32x4*)(km_r + ks * 64); }
      SCHED_FENCE();
      f32x4 s0 = {0, 0, 0, 0}, s1 = {0, 0, 0, 0}, s2 = {0, 0, 0, 0};
#pragma unroll
      for (int ks = 0; ks < 4; ++ks) {
        s1 = mfma(kx[ks], qf[ks], s1);
        s2 = mfma(kx[4 + ks], qf[ks], s2);
        s0 = mfma(km[ks], qf[ks], s0);
      }
      float v[4];
#pragma unroll
      for (int r = 0; r < 4; ++r) v[r] = hb ? s2[r] : s1[r];
      float m = fmx(fmx(fmx(v[0], v[1]), v[2]), fmx(fmx(v[3], s0[0]), s0[1]));
      m = red_max(m);
      const float mc = -m * CEXP;
#pragma unroll
      for (int r = 0; r < 4; ++r) v[r] = __builtin_amdgcn_exp2f(fmaf(v[r], CEXP, mc));
      const float e0 = __builtin_amdgcn_exp2f(fmaf(s0[0], CEXP, mc)), e1 = __builtin_amdgcn_exp2f(fmaf(s0[1], CEXP, mc));
      float sum = ((v[0] + v[1]) + (v[2] + v[3])) + (hb ? e1 : e0);
      sum = red_sum(sum);
      const float inv = __builtin_amdgcn_rcpf(sum);
      u32x2 px = {pk2s(v[0] * inv, v[1] * inv), pk2s(v[2] * inv, v[3] * inv)};
      const u32 pm = pk2s(e0 * inv, e1 * inv);
      const u32 xs = sb + (u32)(2 + (h >> 4)) * 16384;
      st8c(ws, xo, xs + (par ^ 8192), px);
      st4c(ws, mo, sb + 16384 + 2048, pm);
      st8c(ws, xo, xs + par, px);
      const u32 tnow = fine_now();
      const u32 rm = __builtin_amdgcn_readfirstlane(ctl[s & 1]), bad = __builtin_amdgcn_readfirstlane(ctl[2 + (s & 1)]);
      const u32 d1 = (s >= 2 && bad != (u32)s) ? tnow - rm : 0u;
      st4c(ws, l < 32 ? (u32)l * 4 : 0x80000000u, OFF_SIDE + (u32)s * SIDE_STEP + (u32)(80 + h) * 128, d1 < 1000u ? d1 : 0u);
      STAMPV(7, d1);
      STAMPV(4, tnow);
    }
  } else if (w < 3) {
    const int tx = 16 * (w - 1) + c16;
    for (int s = 0; s < NSEG; ++s) {
      const unsigned char* kxs = kq + (s & 1) * 16384;
      const u32 sb = cb + (u32)s * STEP_BYTES;
      if (s == 1) parmask = vote_result(ws, l);
      const u32 par = ((parmask >> (s >> 5)) & 1) * 8192;
      __syncthreads();
      { u32 sp = gather_mem(ws, sb + par, wr, gA, gB, l, dead); COUNT(5, sp); if (sp && l == 0) ctl[2 + (s & 1)] = (u32)s; }
      __syncthreads();
      if (w == 1) STAMP(1);
      { u32x4 a[10]; read_memf(memf, l, a); SCHED_FENCE(); project2(a, wt, sidx, qk_w); }
      __syncthreads();
      if (w == 1) STAMP(2);
      if (w == 1) st4c(ws, l == 0 ? 0u : 0x80000000u, OFF_PPX + (u32)h * 128, (u32)s);
      f32x4 s0 = {0, 0, 0, 0}, s1 = {0, 0, 0, 0}, s2 = {0, 0, 0, 0};
#pragma unroll
      for (int ks = 0; ks < 4; ++ks) {
        u32x4 qf = *(const u32x4*)(kxs + 8192 + ((w - 1) * 4 + ks) * 1024 + l * 16);
        s0 = mfma(*(const u32x4*)(km_r + ks * 64), qf, s0);
        s1 = mfma(*(const u32x4*)(kxs + ks * 1024 + l * 16), qf, s1);
        s2 = mfma(*(const u32x4*)(kxs + (4 + ks) * 1024 + l * 16), qf, s2);
      }
      float m = fmx(s0[0], s0[1]);
#pragma unroll
      for (int r = 0; r < 4; ++r) {
        s1[r] = (4 * g + r <= tx) ? s1[r] : NEGS;
        s2[r] = (16 + 4 * g + r <= tx) ? s2[r] : NEGS;
        m = fmx(m, fmx(s1[r], s2[r]));
      }
      m = red_max_g(m);
      const float mc = -m * CEXP;
      const float e0 = __builtin_amdgcn_exp2f(fmaf(s0[0], CEXP, mc)), e1 = __builtin_amdgcn_exp2f(fmaf(s0[1], CEXP, mc));
      float sum = e0 + e1;
#pragma unroll
      for (int r = 0; r < 4; ++r) {
        s1[r] = __builtin_amdgcn_exp2f(fmaf(s1[r], CEXP, mc));
        s2[r] = __builtin_amdgcn_exp2f(fmaf(s2[r], CEXP, mc));
        sum += s1[r] + s2[r];
      }
      sum = red_sum_g(sum);
      const float inv = __builtin_amdgcn_rcpf(sum);
      u32x4 px = {pk2(s1[0] * inv, s1[1] * inv), pk2(s1[2] * inv, s1[3] * inv), pk2(s2[0] * inv, s2[1] * inv), pk2(s2[2] * inv, s2[3] * inv)};
      st16c(ws, l * 16, OFF_PXX + (u32)(((s * 32 + h) * 2 + (w - 1)) * 1024), px);
      st4c(ws, c16 * 16 + g * 4, OFF_PXM + (u32)((((s * 2 + (w - 1)) * 32 + h) * 16) * 16), pk2(e0 * inv, e1 * inv));
      VM_DRAIN();
    }
  } else if (w == 3) {
    u32 Rm = 0, Rp;
    for (int s = 0; s < NSEG; ++s) {
      const u32 sb = cb + (u32)s * STEP_BYTES;
      if (s == 1) parmask = vote_result(ws, l);
      const u32 par = ((parmask >> (s >> 5)) & 1) * 8192;
      if (s >= 2) {
        if (l == 0) { ctl[s & 1] = Rm; if ((int)(Rm - rtime()) < 28) ctl[2 + (s & 1)] = (u32)s; }
        wait_until(Rm);
      }
      __syncthreads();
      { u32 sp = gather_mem(ws, sb + par, wr, gA, gB, l, dead); COUNT(5, sp); if (sp && l == 0) ctl[2 + (s & 1)] = (u32)s; }
      __syncthreads();
      u32x4 a[10];
      read_memf(memf, l, a);
      Side sd = load_side(ws, s, l);
      SCHED_FENCE();
      project2(a, wt, sidx, qk_w);
      __syncthreads();
      STAMPV(0, Rm);
      if (s >= 1 && s < NSEG - 1) schedule(ws, s, l, sd, Rm, Rp, dead);
    }
  } else {
    for (int s = 0; s < NSEG; ++s) {
      const u32 sb = cb + (u32)s * STEP_BYTES;
      if (s == 1) parmask = vote_result(ws, l);
      const u32 par = ((parmask >> (s >> 5)) & 1) * 8192;
      const int sn = s + 1 < NSEG ? s + 1 : s;
      __syncthreads();
      { u32 sp = gather_mem(ws, sb + par, wr, gA, gB, l, dead); COUNT(5, sp); if (sp && l == 0) ctl[2 + (s & 1)] = (u32)s; }
      __syncthreads();
      u32x4 a[10];
      read_memf(memf, l, a);
      SCHED_FENCE();
      u32x4 pk[4];
      f32x4 c0 = {0, 0, 0, 0}, c1 = {0, 0, 0, 0};
#pragma unroll
      for (int ks = 0; ks < 10; ++ks) {
        c0 = smfma(a[ks], wt[0][ks], c0, sidx);
        c1 = smfma(a[ks], wt[1][ks], c1, sidx);
        if (ks == KQPOS) {
          SCHED_FENCE();
          if (s == 0 && seen0 < 2) continue;
#pragma unroll
          for (int i = 0; i < 4; ++i) pk[i] = ld16c(ws, l * 16 + i * 1024, kq_src + (u32)sn * (32 * 8192));
          SCHED_FENCE();
        }
      }
      *(__bf16*)(qk_w) = (__bf16)(c0[0] + c0[1]);
      *(__bf16*)(qk_w + QS) = (__bf16)(c0[2] + c0[3]);
      *(__bf16*)(qk_w + 8) = (__bf16)(c1[0] + c1[1]);
      *(__bf16*)(qk_w + 8 + QS) = (__bf16)(c1[2] + c1[3]);
      __syncthreads();
      if (s == 0 && seen0 < 2) {
        seen0 = wait_prog(ws, OFF_PQK + (u32)h * 128, 1, 2, l, dead, 0);
#pragma unroll
        for (int i = 0; i < 4; ++i) pk[i] = ld16c(ws, l * 16 + i * 1024, kq_src + (u32)sn * (32 * 8192));
      }
      unsigned char* kn = kq + ((s + 1) & 1) * 16384 + (w - 4) * 4096 + l * 16;
#pragma unroll
      for (int i = 0; i < 4; ++i) *(u32x4*)(kn + i * 1024) = pk[i];
      const int s2 = s + 2;
      if (s2 < NSEG) {
        if (seen0 < (u32)s2 + 1) { __builtin_amdgcn_s_sleep(12); seen0 = wait_prog(ws, OFF_PQK + (u32)h * 128, 1, (u32)s2 + 1, l, dead, 0); }
      }
    }
  }
  __syncthreads();
  if (tid == 0) st4c(ws, 0u, OFF_PPX + (u32)h * 128, (u32)NSEG);
}

DEV u32x4 finish_mem(f32x4 acc) {
  auto x = __builtin_amdgcn_permlane32_swap(__float_as_uint(acc[0]), __float_as_uint(acc[2]), false, false);
  auto y = __builtin_amdgcn_permlane32_swap(__float_as_uint(acc[1]), __float_as_uint(acc[3]), false, false);
  float t0, t1;
  asm("s_nop 1\n\tv_add_f32_dpp %0, %2, %3 row_ror:8 row_mask:0xf bank_mask:0xf\n\tv_add_f32_dpp %1, %4, %5 row_ror:8 row_mask:0xf bank_mask:0xf"
      : "=&v"(t0), "=&v"(t1) : "v"(x[1]), "v"(x[0]), "v"(y[1]), "v"(y[0]));
  const u32 d = pk2s(t0, t1);
  auto s16 = __builtin_amdgcn_permlane16_swap(d, d, false, false);
  auto sa = __builtin_amdgcn_permlane32_swap(s16[0], s16[0], false, false);
  auto sb = __builtin_amdgcn_permlane32_swap(s16[1], s16[1], false, false);
  return (u32x4){sa[0], sa[1], sb[0], sb[1]};
}

DEV u32 second4(u32 a, u32 b, u32 c, u32 d) {
  const u32 h0 = max(a, b), l0 = min(a, b), h1 = max(c, d), l1 = min(c, d);
  return max(min(h0, h1), h0 > h1 ? l0 : l1);
}
DEV void value_role(const Params& p, rsrc_t ws, u32 cb, int v, unsigned char* lds) {
  const int tid = threadIdx.x, l = tid & 63, c16 = l & 15, g = l >> 4;
  const int w = __builtin_amdgcn_readfirstlane(tid >> 6);
  unsigned char* memf = lds;
  unsigned char* um = lds + 10240;
  u32* ctl = (u32*)(lds + 15360);
  unsigned char* w0s = lds + 16384;
  unsigned char* uxs = lds + 57344;
  const int sidx = (l & 1) ? 0xEEEE : 0x4444;
  const u32 um_w = (u32)((4 * w + (c16 >> 3)) * 128 + (c16 & 7) * 16 + g * 4);
  const int wr = (w + v) & 7;
  unsigned char* gA = memf + wr * 1024 + g * 256 + (l & 7) * 32 + ((l >> 3) & 1) * 16;
  unsigned char* gB = memf + 8192 + wr * 256 + ((l >> 2) & 7) * 32 + (l >> 5) * 16 + (l & 3) * 4;
  bool dead = false;
  b_unit(p, ws, v, lds);
  GSTAMP(4);
  if (w == 2 || w == 3) { u32 r = probe(ws, cb, 32 + v, w - 2, l); vote(p, w - 2, r, l); if (l == 0) ctl[2 + w] = r; }
  if (w == 0) wait_prog(ws, OFF_BC + (u32)(v / 40) * 128, 1, 64, l, dead, 0);
  if (w == 1) wait_prog(ws, OFF_INITC, 8, 32, l, dead, 0, 64);
  __syncthreads();
  GSTAMP(5);
  if (w == 0) {
#pragma unroll
    for (int i = 0; i < 40; ++i) *(u32x4*)(w0s + i * 1024 + l * 16) = ld16c(ws, l * 16, OFF_BF + (u32)((v * 16 * 20 + i) * 1024));
    __syncthreads();
    GSTAMP(1);
    __syncthreads();
    GSTAMP(2);
    u32 parmask = __builtin_amdgcn_readfirstlane(ctl[4] | (ctl[5] << 1));
    const u32 offX = (u32)((c16 >> 3) * 512 + g * 128 + (c16 & 7) * 16);
    const u32 L = (u32)(v * 128 + (c16 & 7) * 16);
    const u32 pub = (c16 < 8 && !(g & 1)) ? (L >> 13) * 16384 + (u32)(g >> 1) * 8192 + (L & 8191) : 0x80000000u;
    u32 Rg = 0, d3 = 0, Rm = 0, dcur = v == 0 ? ((D1 + D2) << 10) | ((D3 + D4) << 20) : 0u;
    u32 a0 = D1, a1 = D1, a2 = D1, a3 = D1, b0 = D3, b1 = D3, b2 = D3, b3 = D3;
    const bool sidel = (g == 1 && c16 < 8);
    for (int s = 0; s < NSEG; ++s) {
      const u32 sb = cb + (u32)s * STEP_BYTES;
      if (s == 1) parmask = vote_result(ws, l);
      const u32 par = ((parmask >> (s >> 5)) & 1) * 8192;
      if (s >= 1) wait_until(Rg);
      __syncthreads();
      const u32x4 ga = ld16c(ws, (u32)l * 16, sb + par + (u32)wr * 1024);
      const u32 gb = ld4c(ws, (u32)l * 4, sb + par + 16384 + (u32)wr * 256);
      SCHED_FENCE();
      __builtin_amdgcn_s_sleep(1);
      Side sd = load_side(ws, s, l);
      u32 d1s = 0;
      if (v == 0 && s >= 1) d1s = ld4c(ws, (u32)(80 + (l & 31)) * 128, OFF_SIDE + (u32)(s - 1) * SIDE_STEP);
      u32x4 uf[20];
#pragma unroll
      for (int i = 0; i < 16; ++i) uf[i] = *(const u32x4*)(uxs + (((i >> 2) + PROT) & 3) * 4096 + (i & 3) * 1024 + offX);
      SCHED_FENCE();
      *(u32x4*)gA = ga;
      *(u32*)gB = gb;
      if (__builtin_expect(__any(umax3(umax3(ga.x, ga.y, ga.z), ga.w, gb) == SENT), 0)) { u32 sp = gather_mem(ws, sb + par, wr, gA, gB, l, dead); COUNT(5, sp + 1); }
      __syncthreads();
      STAMP(0);
      if (s == NSEG - 1) {
        __syncthreads();
        __syncthreads();
        break;
      }
      u32 so[5];
#pragma unroll
      for (int i = 0; i < 4; ++i) { const u32 qd = (u32)(i + PROT) & 3; so[i] = sb + par + (2 + (qd >> 1)) * 16384 + (qd & 1) * 4096; }
      so[4] = sb + par + 16384 + 2048;
      asm volatile("" : "+s"(so[0]), "+s"(so[1]), "+s"(so[2]), "+s"(so[3]), "+s"(so[4]));
      const u32 pubo = sidel ? OFF_SIDE + (u32)(s + 1) * SIDE_STEP + (u32)(v * 128 + c16 * 16) : pub + sb + STEP_BYTES;
      u32 Rp = 0;
      if (s >= 1) schedule(ws, s, l, sd, Rm, Rp, dead);
      const u32 sy = d3 | dcur;
      const int lead = (int)(Rp - rtime());
      bool ontime = s >= 1 && lead > 0, pfail = false;
      STAMPV(1, Rp);
      STAMP(2);
      SCHED_FENCE();
      if (s >= 1) wait_until(Rp);
      SCHED_FENCE();
      u32x4 pf[20];
#pragma unroll
      for (int i = 0; i < 20; ++i) { pf[i] = ld16c(ws, (u32)l * 16 + (i & 3) * 1024, so[i >> 2]); SCHED_FENCE(); }
      f32x4 acc = {0, 0, 0, 0};
#pragma unroll
      for (int i = 0; i < 20; ++i) {
        if (i == UMPOS) {
          __syncthreads();
#pragma unroll
          for (int q = 0; q < 4; ++q) uf[16 + q] = *(const u32x4*)(um + q * 1024 + l * 16);
          SCHED_FENCE();
        }
        acc = mfma(uf[i], pf[i], acc);
        SCHED_FENCE();
      }
      const u32 tl = rtime();
      u32x4 o = finish_mem(acc);
      if (__builtin_expect(__any(acc[0] != acc[0]), 0)) {
        u32 spins = 0;
#pragma nounroll
        while (!dead) {
          u32 m = 0;
#pragma unroll
          for (int i = 0; i < 20; ++i) m = umax3(umax3(m, pf[i].x, pf[i].y), pf[i].z, pf[i].w);
          if (__all(m != SENT)) break;
          ontime = false; pfail = true;
          if (++spins > SPIN_CAP) dead = true;
          asm volatile("" ::: "memory");
#pragma unroll
          for (int i = 0; i < 20; ++i) pf[i] = ld16c(ws, (u32)l * 16 + (i & 3) * 1024, so[i >> 2]);
        }
        STAMPV(4, spins);
        acc = (f32x4){0, 0, 0, 0};
#pragma unroll
        for (int i = 0; i < 20; ++i) acc = mfma(uf[i], pf[i], acc);
        o = finish_mem(acc);
      }
      u32 sx = 0;
      if ((int)(tl + DLS - Rm) > 0) sx = (tl + DL + 4) | 1u;
      if (__builtin_expect(!ontime, 0)) sx = (rtime() + DL) | 1u;
      if (sx == SENT) sx = 1u;
      o.x = sidel ? sx : o.x;
      o.y = sidel ? sy : o.y;
      st16c(ws, pubo, 0u, o);
      const u32 tp = fine_now();
      __syncthreads();
      d3 = (ontime && lead >= 28) ? tp - Rp : 0u;
      d3 = d3 < 1000u ? d3 : 0u;
      Rg = sx == 0 ? Rm + DV : tp + D4 + DV;
      STAMPV(7, d3);
      if (v == 0) {
        const u32 ma = (u32)wave_max(d1s == SENT ? 0 : (int)d1s), mb = s >= 1 ? (u32)wave_max((int)max(sd.a.y & 1023u, sd.b.y & 1023u)) : 0u;
        if (ma) { a3 = a2; a2 = a1; a1 = a0; a0 = ma; }
        if (pfail && s >= 2) { a0 = a1 = min(second4(a0, a1, a2, a3) + 8, 300u); }
        if (mb) { b3 = b2; b2 = b1; b1 = b0; b0 = mb; }
        dcur = (min(second4(a0, a1, a2, a3) + D2, 1000u) << 10) | (min(second4(b0, b1, b2, b3) + D4, 1000u) << 20);
      }
      STAMP(3);
      STAMPV(6, ((sy >> 10) & 1023u) * 1000 + ((sy >> 20) & 1023u));
    }
  } else {
    u32x8 wb[2][10];
#pragma unroll
    for (int t = 0; t < 2; ++t)
#pragma unroll
      for (int ks = 0; ks < 10; ++ks) {
        u32 o = OFF_BF + (u32)(((v * 16 + 2 * w + t) * 20 + 2 * ks) * 1024);
        wb[t][ks] = cat(ld16c(ws, l * 16, o), ld16c(ws, l * 16 + 1024, o));
      }
    __builtin_amdgcn_s_waitcnt(0x0F70);
    __syncthreads();
    const bool pfw = w == 3 || w >= 5;
    const u32 uxo = (u32)((w == 3 ? 0 : w - 4) * 4096 + l * 16);
    u32 seen0 = 0, seen1 = 0;
    if (pfw) {
      seen0 = wait_prog(ws, OFF_PVU, 32, 2, l, dead, 0);
#pragma unroll
      for (int i = 0; i < 4; ++i) *(u32x4*)(uxs + uxo + i * 1024) = ld16c(ws, uxo + i * 1024, OFF_UX + (u32)(v * 16384));
    }
    __syncthreads();
    u32 parmask = __builtin_amdgcn_readfirstlane(ctl[4] | (ctl[5] << 1));
    if (w == 4) {
      for (int s = 0; s < NSEG; ++s) {
        const u32 sb = cb + (u32)s * STEP_BYTES;
        if (s == 1) parmask = vote_result(ws, l);
      const u32 par = ((parmask >> (s >> 5)) & 1) * 8192;
        u32x8 sb0[2], sb1[2];
        sb0[0] = cat(*(const u32x4*)(w0s + l * 16), *(const u32x4*)(w0s + 1024 + l * 16));
        sb1[0] = cat(*(const u32x4*)(w0s + 20480 + l * 16), *(const u32x4*)(w0s + 21504 + l * 16));
        __syncthreads();
        { u32 sp = gather_mem(ws, sb + par, wr, gA, gB, l, dead); COUNT(5, sp); }
        __syncthreads();
        u32x4 a[3];
        a[0] = *(const u32x4*)(memf + l * 16);
        a[1] = *(const u32x4*)(memf + 1024 + l * 16);
        f32x4 a0 = {0, 0, 0, 0}, a1 = {0, 0, 0, 0}, a2 = {0, 0, 0, 0}, a3 = {0, 0, 0, 0};
#pragma unroll
        for (int ks = 0; ks < 10; ++ks) {
          if (ks + 2 < 10) a[(ks + 2) % 3] = *(const u32x4*)(memf + (ks + 2) * 1024 + l * 16);
          if (ks + 1 < 10) {
            const int kn = ks + 1;
            sb0[kn & 1] = cat(*(const u32x4*)(w0s + (2 * kn) * 1024 + l * 16), *(const u32x4*)(w0s + (2 * kn + 1) * 1024 + l * 16));
            sb1[kn & 1] = cat(*(const u32x4*)(w0s + (20 + 2 * kn) * 1024 + l * 16), *(const u32x4*)(w0s + (21 + 2 * kn) * 1024 + l * 16));
          }
          SCHED_FENCE();
          a0 = smfma(a[ks % 3], wb[0][ks], a0, sidx);
          a1 = smfma(a[ks % 3], wb[1][ks], a1, sidx);
          a2 = smfma(a[ks % 3], sb0[ks & 1], a2, sidx);
          a3 = smfma(a[ks % 3], sb1[ks & 1], a3, sidx);
          SCHED_FENCE();
        }
        *(u32*)(um + um_w) = pk2(a0[0] + a0[1], a0[2] + a0[3]);
        *(u32*)(um + um_w + 256) = pk2(a1[0] + a1[1], a1[2] + a1[3]);
        *(u32*)(um + um_w - 2048) = pk2(a2[0] + a2[1], a2[2] + a2[3]);
        *(u32*)(um + um_w - 2048 + 256) = pk2(a3[0] + a3[1], a3[2] + a3[3]);
        __syncthreads();
        __syncthreads();
      }
    } else {
      for (int s = 0; s < NSEG; ++s) {
        const u32 sb = cb + (u32)s * STEP_BYTES;
        if (s == 1) parmask = vote_result(ws, l);
      const u32 par = ((parmask >> (s >> 5)) & 1) * 8192;
        __syncthreads();
        { u32 sp = gather_mem(ws, sb + par, wr, gA, gB, l, dead); COUNT(5, sp); }
        __syncthreads();
        u32x4 a[10];
        read_memf(memf, l, a);
        u32x4 pu[4];
        if (pfw && s + 1 < NSEG) {
#pragma unroll
          for (int i = 0; i < 4; ++i) pu[i] = ld16c(ws, uxo + i * 1024, OFF_UX + (u32)(((s + 1) * 80 + v) * 16384));
        }
        SCHED_FENCE();
        f32x4 a0 = {0, 0, 0, 0}, a1 = {0, 0, 0, 0};
#pragma unroll
        for (int ks = 0; ks < 10; ++ks) {
          a0 = smfma(a[ks], wb[0][ks], a0, sidx);
          a1 = smfma(a[ks], wb[1][ks], a1, sidx);
        }
        *(u32*)(um + um_w) = pk2(a0[0] + a0[1], a0[2] + a0[3]);
        *(u32*)(um + um_w + 256) = pk2(a1[0] + a1[1], a1[2] + a1[3]);
        if (w == 1) st4c(ws, l == 0 ? 0u : 0x80000000u, OFF_PUM + (u32)v * 128, (u32)s);
        __syncthreads();
        __syncthreads();
        if (pfw && s + 1 < NSEG) {
#pragma unroll
          for (int i = 0; i < 4; ++i) *(u32x4*)(uxs + uxo + i * 1024) = pu[i];
          const int sn = s + 2;
          if (sn < NSEG) {
            if (seg_prod(sn)) { if (seen1 < seg_need(sn)) seen1 = wait_prog(ws, OFF_PVU + 128, 32, seg_need(sn), l, dead, 0); }
            else { if (seen0 < seg_need(sn)) seen0 = wait_prog(ws, OFF_PVU, 32, seg_need(sn), l, dead, 0); }
          }
        }
        if (w == 1 || w == 2) {
#pragma unroll
          for (int i = 0; i < 2; ++i) {
            u32 o = (u32)(((w - 1) * 2 + i) * 1024 + l * 16);
            st16c(ws, o, OFF_UM + (u32)((s * 80 + v) * 4096), *(const u32x4*)(um + o));
          }
          VM_DRAIN();
        }
      }
    }
  }
  __syncthreads();
  if (tid == 0) st4c(ws, 0u, OFF_PUM + (u32)v * 128, (u32)NSEG);
}

template <bool SC1> DEV void glds16(const void* gsrc, u32 lds_dst) {
  u32 keep;
  if (!SC1) {
    asm volatile("s_mov_b32 %0, m0\n\ts_mov_b32 m0, %2\n\ts_nop 0\n\tglobal_load_lds_dwordx4 %1, off\n\ts_mov_b32 m0, %0"
                 : "=&s"(keep) : "v"(gsrc), "s"(lds_dst) : "memory");
    return;
  }
  asm volatile("s_mov_b32 %0, m0\n\ts_mov_b32 m0, %2\n\ts_nop 0\n\tglobal_load_lds_dwordx4 %1, off sc1\n\ts_mov_b32 m0, %0"
               : "=&s"(keep) : "v"(gsrc), "s"(lds_dst) : "memory");
}
template <int NST, bool ROWS, class Off, class Body, class Pub>
DEV void stream_tiles(const unsigned char* src, Off off, int n, int nbody, unsigned char* lds, Body body, Pub pub) {
  const int l = threadIdx.x & 63, w = __builtin_amdgcn_readfirstlane(threadIdx.x >> 6);
  const u32 lbase = (u32)(uintptr_t)lds + (u32)w * 5120;
  constexpr int QS_ = ROWS ? 64 : 1024;
  const unsigned char* g0 = ROWS ? src + (16 * (w >> 2) + (l & 15)) * 1280 + (w & 3) * 320 + (l >> 4) * 16 : src + w * 5120 + l * 16;
#pragma unroll
  for (int q = 0; q < 5; ++q) glds16<!ROWS>(g0 + off(0) + q * QS_, lbase + q * 1024);
#pragma nounroll
  for (int i = 0; i < nbody; ++i) {
    if (i <= 3) VM_DRAIN(); else asm volatile("s_waitcnt vmcnt(%0)" :: "n"(NST) : "memory");
    RAW_BARRIER();
    if (i >= 1) pub(i <= 3 ? i : i - 1);
    const unsigned char* g = g0 + off(i + 1 < n ? i + 1 : n - 1);
    const u32 lb = lbase + (u32)((i + 1) & 1) * 40960;
    body(i, lds + (i & 1) * 40960, [&](int q) { glds16<!ROWS>(g + q * QS_, lb + q * 1024); });
  }
  VM_DRAIN();
  RAW_BARRIER();
  pub(nbody);
}
template <int W, class Dma, class Use>
DEV void sweep40(const unsigned char* t, Dma dma, Use use) {
  u32x4 xb[W];
#pragma unroll
  for (int i = 0; i < W; ++i) xb[i] = *(const u32x4*)(t + ((i & 1) * 20 + (i >> 1)) * 1024);
  SCHED_FENCE();
#pragma unroll
  for (int i = 0; i < 40; ++i) {
    use(i, xb[i % W]);
    if (i + W < 40) xb[i % W] = *(const u32x4*)(t + (((i + W) & 1) * 20 + ((i + W) >> 1)) * 1024);
    if (i % 2 == 1 && i < 10) dma(i >> 1);
    SCHED_FENCE();
  }
}

DEV void qk_role(const Params& p, rsrc_t ws, int h, int par, unsigned char* lds, bool sig) {
  const int tid = threadIdx.x, l = tid & 63;
  const int w = __builtin_amdgcn_readfirstlane(tid >> 6);
  const int mat = w >> 2;
  u32x4 wt[2][20];
  load_qk(p, h, w, l, lds, wt, sig);
  GSTAMP(1);
  const u32 dst = (mat ? OFF_KX : OFF_QX) + (u32)((h * 8 + (w & 3)) * 1024);
  const int n = NSEG;
  wait_init(ws);
  stream_tiles<2, false>(p.ws + OFF_XF, [&](int i) { return i * 40960; }, n, n, lds,
    [&](int i, const unsigned char* t, auto dma) {
      f32x4 acc[2][2] = {};
      sweep40<8>(t + l * 16, dma, [&](int f, u32x4 xb) {
        acc[0][f & 1] = mfma(wt[0][f >> 1], xb, acc[0][f & 1]);
        acc[1][f & 1] = mfma(wt[1][f >> 1], xb, acc[1][f & 1]);
      });
#pragma unroll
      for (int tt = 0; tt < 2; ++tt) {
        u32x4 o = {pk2(acc[0][tt][0], acc[0][tt][1]), pk2(acc[0][tt][2], acc[0][tt][3]), pk2(acc[1][tt][0], acc[1][tt][1]), pk2(acc[1][tt][2], acc[1][tt][3])};
        st16c(ws, l * 16 + tt * 4096, dst + (u32)i * (32 * 8192), o);
      }
    },
    [&](int k) {
      if (w == 0) {
        if (k == 2) { bool dead = false; wait_prog(ws, OFF_XFC + 64, 1, 16, l, dead, 0); }
        st4c(ws, l == 0 ? 0u : 0x80000000u, OFF_PQK + (u32)(h * 128 + par * 4), (u32)k);
      }
    });
}

DEV void vu_role(const Params& p, rsrc_t ws, int h, int par, unsigned char* lds, bool sig) {
  const int tid = threadIdx.x, l = tid & 63, c16 = l & 15, g = l >> 4;
  const int w = __builtin_amdgcn_readfirstlane(tid >> 6);
  u32x4 wv[20], wo[5][4];
  load_vu(p, h, w, l, lds, wv, wo, sig);
  GSTAMP(1);
  unsigned char* vs = lds + 81920;
  const u32 uoff = (u32)(((c16 >> 1) & 1) * 16384 + h * 512 + g * 128 + (2 * (c16 >> 2) + (c16 & 1)) * 16);
  const int n = par ? NHELP : NSEG - NHELP;
  wait_init(ws);
  stream_tiles<5, false>(p.ws + OFF_XF, [&](int i) { return tile_seg(par, i) * 40960; }, n, n + 1, lds,
    [&](int i, const unsigned char* t, auto dma) {
      u32x4 vf[2][4];
#pragma unroll
      for (int tt = 0; tt < 2; ++tt)
#pragma unroll
        for (int ks = 0; ks < 4; ++ks) vf[tt][ks] = *(const u32x4*)(vs + ((i + 1) & 1) * 8192 + ((tt * 4 + ks) * 64 + l) * 16);
      const u32 uo = i ? uoff + OFF_UX + (u32)((tile_seg(par, i - 1) * 80 + 10 * w) * 16384) : 0x80000000u;
      f32x4 av[2] = {}, cu[2] = {};
      u32x4 ou[2];
      sweep40<6>(t + l * 16, dma, [&](int f, u32x4 xb) {
        av[f & 1] = mfma(wv[f >> 1], xb, av[f & 1]);
        cu[f & 1] = mfma(vf[f & 1][(f >> 1) & 3], wo[f >> 3][(f >> 1) & 3], cu[f & 1]);
        if ((f & 7) == 7) {
          const int ct = f >> 3;
          u32x4 o = {pk2(cu[0][0], cu[0][1]), pk2(cu[0][2], cu[0][3]), pk2(cu[1][0], cu[1][1]), pk2(cu[1][2], cu[1][3])};
          cu[0] = (f32x4){0, 0, 0, 0}; cu[1] = (f32x4){0, 0, 0, 0};
          if (ct == 0) ou[0] = o;
          else { if (ct == 1) st16c(ws, uo, 0u, ou[0]); st16c(ws, uo, (u32)ct * 32768, o); }
        }
      });
#pragma unroll
      for (int tt = 0; tt < 2; ++tt) {
        u32x2 o = {pk2(av[tt][0], av[tt][1]), pk2(av[tt][2], av[tt][3])};
        *(u32x2*)(vs + (i & 1) * 8192 + ((tt * 4 + (w >> 1)) * 64 + l) * 16 + (w & 1) * 8) = o;
      }
    },
    [&](int k) {
      if (w == 0 && k >= 2) {
        st4c(ws, l == 0 ? 0u : 0x80000000u, OFF_PVU + (u32)(par * 128 + h * 4), (u32)(k - 1));
        if (k == 3) { bool dead = false; wait_prog(ws, OFF_XFC + 64, 1, 16, l, dead, 0); }
      }
    });
}

DEV f32x2 cmul(f32x2 a, f32x2 b) { return (f32x2){a.x * b.x - a.y * b.y, a.x * b.y + a.y * b.x}; }
DEV f32x2 cmulc(f32x2 a, f32x2 b) { return (f32x2){a.x * b.x + a.y * b.y, a.y * b.x - a.x * b.y}; }
template <bool INV> DEV f32x2 rot90(f32x2 a) { return INV ? (f32x2){-a.y, a.x} : (f32x2){a.y, -a.x}; }
template <bool INV> DEV void fft4(f32x2& a, f32x2& b, f32x2& c, f32x2& d) {
  f32x2 s0 = a + c, d0 = a - c, s1 = b + d, d1 = rot90<INV>(b - d);
  a = s0 + s1; b = d0 + d1; c = s0 - s1; d = d0 - d1;
}
template <bool INV> DEV f32x2 twc(f32x2 a, float c, float s) {
  return INV ? (f32x2){a.x * c - a.y * s, a.y * c + a.x * s} : (f32x2){a.x * c + a.y * s, a.y * c - a.x * s};
}
template <bool INV> DEV void fft16(f32x2 (&v)[16]) {
  constexpr float C1 = 0.92387953251128674f, S1 = 0.38268343236508977f, R = 0.70710678118654752f;
#pragma unroll
  for (int i = 0; i < 4; ++i) fft4<INV>(v[i], v[i + 4], v[i + 8], v[i + 12]);
  v[5] = twc<INV>(v[5], C1, S1);  v[9] = twc<INV>(v[9], R, R);     v[13] = twc<INV>(v[13], S1, C1);
  v[6] = twc<INV>(v[6], R, R);    v[10] = rot90<INV>(v[10]);       v[14] = twc<INV>(v[14], -R, R);
  v[7] = twc<INV>(v[7], S1, C1);  v[11] = twc<INV>(v[11], -R, R);  v[15] = twc<INV>(v[15], -C1, -S1);
#pragma unroll
  for (int k1 = 0; k1 < 4; ++k1) fft4<INV>(v[4 * k1], v[4 * k1 + 1], v[4 * k1 + 2], v[4 * k1 + 3]);
  f32x2 t;
#define SWP(a, b) t = v[a]; v[a] = v[b]; v[b] = t;
  SWP(1, 4) SWP(2, 8) SWP(3, 12) SWP(6, 9) SWP(7, 13) SWP(11, 14)
#undef SWP
}
template <bool INV> DEV void fft8(f32x2 (&v)[8]) {
  constexpr float R = 0.70710678118654752f;
  fft4<INV>(v[0], v[2], v[4], v[6]);
  fft4<INV>(v[1], v[3], v[5], v[7]);
  f32x2 o1 = twc<INV>(v[3], R, R), o2 = rot90<INV>(v[5]), o3 = twc<INV>(v[7], -R, R), o0 = v[1];
  f32x2 e0 = v[0], e1 = v[2], e2 = v[4], e3 = v[6];
  v[0] = e0 + o0; v[4] = e0 - o0; v[1] = e1 + o1; v[5] = e1 - o1; v[2] = e2 + o2; v[6] = e2 - o2; v[3] = e3 + o3; v[7] = e3 - o3;
}

DEV void fft_cols(const Params& p, rsrc_t ws, int q, unsigned char* lds) {
  const int tid = threadIdx.x, c = tid >> 7, t = tid & 127;
  f32x2* tw = (f32x2*)lds;
  f32x2* buf = (f32x2*)(lds + 16384) + c * (16 * 136);
  f32x2* fin = (f32x2*)(lds + 16384);
  __syncthreads();
  for (int m = tid; m < 2048; m += 512) { float sn, cs; sincospif((float)m * (1.0f / 1024.0f), &sn, &cs); tw[m] = (f32x2){cs, -sn}; }
  f32x2 keep[16];
  const int npass = (q == 0) ? 2 : 1;
  for (int pass = npass - 1; pass >= 0; --pass) {
    f32x2 v[16];
#pragma unroll
    for (int a = 0; a < 16; ++a)
      v[a] = __builtin_bit_cast(f32x2, ld8c(ws, (u32)(t * 16 + (c & 1) * 8 + (c >> 1) * 32768), OFF_FX + (u32)(2 * q * 32768 + a * 2048)));
    if (q == 0 && c == 0) {
#pragma unroll
      for (int a = 0; a < 16; ++a) v[a] = (f32x2){pass ? v[a].y : v[a].x, 0.f};
    }
    u32 wreg[16];
#pragma unroll
    for (int i = 0; i < 2; ++i)
#pragma unroll
      for (int kc = 0; kc < 8; ++kc) {
        const int pp = t + 128 * i, k = (pp >> 4) + 16 * (pp & 15) + 256 * kc;
        wreg[i * 8 + kc] = ld4c(ws, (u32)(k * 16 + c * 4), OFF_WT + (u32)(pass ? 80 : q) * 32768);
      }
    __syncthreads();
    fft16<false>(v);
#pragma unroll
    for (int ka = 0; ka < 16; ++ka) buf[ka * 136 + t] = ka ? cmul(v[ka], tw[t * ka]) : v[ka];
    __syncthreads();
    const int ka2 = t >> 3, c2 = t & 7;
#pragma unroll
    for (int b = 0; b < 16; ++b) v[b] = buf[ka2 * 136 + 8 * b + c2];
    fft16<false>(v);
#pragma unroll
    for (int kb = 0; kb < 16; ++kb) buf[ka2 * 136 + 8 * kb + c2] = kb ? cmul(v[kb], tw[16 * c2 * kb]) : v[kb];
    __syncthreads();
#pragma unroll
    for (int i = 0; i < 2; ++i) {
      const int pp = t + 128 * i;
      f32x2* e = buf + (pp >> 4) * 136 + 8 * (pp & 15);
      f32x2 u[8];
#pragma unroll
      for (int j = 0; j < 8; ++j) u[j] = e[j];
      fft8<false>(u);
#pragma unroll
      for (int kc = 0; kc < 8; ++kc) {
        const u32 wv = wreg[i * 8 + kc];
        const f32x2 wf = {__uint_as_float(wv << 16) * (1.0f / (2048.0f * 640.0f)), __uint_as_float(wv & 0xFFFF0000u) * (1.0f / (2048.0f * 640.0f))};
        u[kc] = cmul(u[kc], wf);
      }
      fft8<true>(u);
#pragma unroll
      for (int j = 0; j < 8; ++j) e[j] = u[j];
    }
    __syncthreads();
#pragma unroll
    for (int kb = 0; kb < 16; ++kb) { f32x2 x = buf[ka2 * 136 + 8 * kb + c2]; v[kb] = kb ? cmulc(x, tw[16 * c2 * kb]) : x; }
    fft16<true>(v);
#pragma unroll
    for (int b = 0; b < 16; ++b) buf[ka2 * 136 + 8 * b + c2] = v[b];
    __syncthreads();
#pragma unroll
    for (int ka = 0; ka < 16; ++ka) { f32x2 x = buf[ka * 136 + t]; v[ka] = ka ? cmulc(x, tw[t * ka]) : x; }
    fft16<true>(v);
    if (pass == 1) {
#pragma unroll
      for (int a = 0; a < 16; ++a) keep[a] = v[a];
    } else {
      if (q == 0 && c == 0) {
#pragma unroll
        for (int a = 0; a < 16; ++a) v[a].y = keep[a].x;
      }
      __syncthreads();
#pragma unroll
      for (int a = 0; a < 16; ++a) fin[(128 * a + t) * 4 + c] = v[a];
      __syncthreads();
    }
  }
#pragma unroll
  for (int i = 0; i < 4; ++i) {
    const int n = tid + 512 * i;
    const f32x4 x0 = *(const f32x4*)(fin + n * 4), x1 = *(const f32x4*)(fin + n * 4 + 2);
    u32x4 hi = {pk2(x0[0], x0[1]), pk2(x0[2], x0[3]), pk2(x1[0], x1[1]), pk2(x1[2], x1[3])};
    u32x4 lo = {pk2(x0[0] - __uint_as_float(hi[0] << 16), x0[1] - __uint_as_float(hi[0] & 0xFFFF0000u)),
                pk2(x0[2] - __uint_as_float(hi[1] << 16), x0[3] - __uint_as_float(hi[1] & 0xFFFF0000u)),
                pk2(x1[0] - __uint_as_float(hi[2] << 16), x1[1] - __uint_as_float(hi[2] & 0xFFFF0000u)),
                pk2(x1[2] - __uint_as_float(hi[3] << 16), x1[3] - __uint_as_float(hi[3] & 0xFFFF0000u))};
    const u32 o = (u32)((n >> 4) * 40960 + (q >> 2) * 1024 + ((q & 3) * 16 + (n & 15)) * 16);
    st16c(ws, o, OFF_FY, hi);
    st16c(ws, o + 20480, OFF_FY, lo);
  }
}

DEV void fw_task(const Params& p, rsrc_t ws, int k, unsigned char* lds) {
  float* tab = (float*)lds;
  for (int m = threadIdx.x; m < 640; m += 512) { float sn, cs; sincospif((float)m * (1.0f / 320.0f), &sn, &cs); tab[m] = cs; tab[640 + m] = -sn; }
  __syncthreads();
  for (int q = k * 5120 + threadIdx.x; q < (k + 1) * 5120; q += 512) {
    const int which = q / 51200, r = q % 51200, tile = r / 1280, ks = (r % 1280) >> 6, ln = r & 63;
    u32x4 hi, lo;
    float val[8];
#pragma unroll
    for (int j = 0; j < 8; ++j) {
      const int aa = 16 * tile + (ln & 15), bb = 32 * ks + 8 * (ln >> 4) + j;
      const int kp = which ? bb : aa, d = which ? aa : bb;
      const int kk = (kp == 1) ? 320 : (kp >> 1);
      float x = tab[(kk * d) % 640 + (((kp & 1) && kp != 1) ? 640 : 0)];
      if (which && kp > 1) x *= 2.0f;
      val[j] = x;
    }
#pragma unroll
    for (int j = 0; j < 4; ++j) { hi[j] = pk2(val[2 * j], val[2 * j + 1]); lo[j] = pk2lo(val[2 * j], val[2 * j + 1], hi[j]); }
    const u32 o = (which ? OFF_FW3 : OFF_FW1) + (u32)((tile * 40 + ks) * 1024 + ln * 16);
    st16c(ws, o, 0, hi);
    st16c(ws, o + 20480, 0, lo);
  }
  VM_DRAIN();
  __syncthreads();
  if (threadIdx.x == 0) __hip_atomic_fetch_add((u32*)(p.ws + OFF_FWC), 1u, __ATOMIC_RELAXED, __HIP_MEMORY_SCOPE_AGENT);
}
DEV void wt_task(const Params& p, rsrc_t ws, int k, unsigned char* lds) {
  const int tid = threadIdx.x;
  const rsrc_t rr = mk_rsrc(p.wr, 2048 * 642), ri = mk_rsrc(p.wi, 2048 * 642);
  {
    const rsrc_t ra = mk_rsrc(p.ax, 2048 * 1280);
    u32 t[3];
#pragma unroll
    for (int i = 0; i < 3; ++i) t[i] = __builtin_amdgcn_raw_buffer_load_b32(ra, (i * 512 + tid) < 1280 ? (u32)(i * 512 + tid) * 128 : 0x80000000u, (u32)k * 163840, 0);
#pragma unroll
    for (int i = 0; i < 3; ++i) asm volatile("" :: "v"(t[i]));
  }
  for (int blk = 0; blk < 2; ++blk) {
    const int row0 = k * 128 + blk * 64;
    __syncthreads();
#pragma unroll
    for (int i = 0; i < 6; ++i) {
      const int c = tid + 512 * i;
      if (c < 2568) {
        *(u32x4*)(lds + c * 16) = ld16(rr, (u32)c * 16, (u32)row0 * 642);
        *(u32x4*)(lds + 41088 + c * 16) = ld16(ri, (u32)c * 16, (u32)row0 * 642);
      }
    }
    __syncthreads();
    const int r = tid & 63;
    const u16* a = (const u16*)lds + r * 321;
    for (int q = tid >> 6; q < 81; q += 8) {
      u32x4 o;
#pragma unroll
      for (int j = 0; j < 4; ++j) { const int col = q < 80 ? 4 * q + j : 320; o[j] = (u32)a[col] | ((u32)a[20544 + col] << 16); }
      st16c(ws, (u32)(row0 + r) * 16, OFF_WT + (u32)q * 32768, o);
    }
  }
  VM_DRAIN();
  __syncthreads();
  if (tid == 0) __hip_atomic_fetch_add((u32*)(p.ws + OFF_FWC), 1u, __ATOMIC_RELAXED, __HIP_MEMORY_SCOPE_AGENT);
}
DEV void wait_fw(rsrc_t ws) {
  if (threadIdx.x < 64) { bool dead = false; wait_prog(ws, OFF_FWC, 1, 36, threadIdx.x, dead, 1); }
  __syncthreads();
}
DEV void f1_task(const Params& p, rsrc_t ws, int type, int rg, unsigned char* lds) {
  const int tid = threadIdx.x, l = tid & 63, c16 = l & 15, g = l >> 4;
  const int w = __builtin_amdgcn_readfirstlane(tid >> 6);
  const int tile = type * 8 + w;
  wait_fw(ws);
  u32x4 wt[2][20];
#pragma unroll
  for (int i = 0; i < 2; ++i)
#pragma unroll
    for (int ks = 0; ks < 20; ++ks) { wt[i][ks] = ld16c(ws, l * 16, OFF_FW1 + (u32)((tile * 40 + i * 20 + ks) * 1024)); THROTTLE(ks); }
  __builtin_amdgcn_s_waitcnt(0x0F70);
  stream_tiles<2, true>((const unsigned char*)p.ax + rg * 8 * 40960, [](int i) { return i * 40960; }, 8, 8, lds,
    [&](int i, const unsigned char* t, auto dma) {
      f32x4 acc[2][2] = {};
      sweep40<8>(t + l * 16, dma, [&](int f, u32x4 xb) {
        acc[0][f & 1] = mfma(wt[0][f >> 1], xb, acc[0][f & 1]);
        acc[1][f & 1] = mfma(wt[1][f >> 1], xb, acc[1][f & 1]);
      });
#pragma unroll
      for (int tt = 0; tt < 2; ++tt) {
        f32x4 o = acc[0][tt] + acc[1][tt];
        st16c(ws, (u32)(g * 32768 + c16 * 16 + tt * 256), OFF_FX + (u32)(tile * 4 * 32768 + (rg * 8 + i) * 512), __builtin_bit_cast(u32x4, o));
      }
    },
    [&](int k) { if (k == 8 && tid == 0) __hip_atomic_fetch_add((u32*)(p.ws + OFF_F1C + type * 128), 1u, __ATOMIC_RELAXED, __HIP_MEMORY_SCOPE_AGENT); });
}
DEV void f3_task(const Params& p, rsrc_t ws, int type, int rg, unsigned char* lds) {
  const int tid = threadIdx.x, l = tid & 63, c16 = l & 15, g = l >> 4;
  const int w = __builtin_amdgcn_readfirstlane(tid >> 6);
  const int tile = type * 8 + w;
  wait_fw(ws);
  u32x4 wt[2][20];
#pragma unroll
  for (int i = 0; i < 2; ++i)
#pragma unroll
    for (int ks = 0; ks < 20; ++ks) { wt[i][ks] = ld16c(ws, l * 16, OFF_FW3 + (u32)((tile * 40 + i * 20 + ks) * 1024)); THROTTLE(ks); }
  __builtin_amdgcn_s_waitcnt(0x0F70);
  if (w == 0) {
    bool dead = false;
    wait_prog(ws, OFF_F2C, 1, 80, l, dead, 1);
    __builtin_amdgcn_fence(__ATOMIC_ACQUIRE, "agent");
    VM_DRAIN();
  }
  __syncthreads();
  u16* outp = p.out + (rg * 256 + c16) * 1280 + tile * 16 + 4 * g;
  stream_tiles<1, false>(p.ws + OFF_FY + rg * 16 * 40960, [](int i) { return i * 40960; }, 16, 16, lds,
    [&](int i, const unsigned char* t, auto dma) {
      f32x4 a0 = {0, 0, 0, 0}, a1 = {0, 0, 0, 0}, a2 = {0, 0, 0, 0};
      sweep40<10>(t + l * 16, dma, [&](int f, u32x4 y) {
        if (f & 1) a2 = mfma(wt[0][f >> 1], y, a2);
        else { a0 = mfma(wt[0][f >> 1], y, a0); a1 = mfma(wt[1][f >> 1], y, a1); }
      });
      f32x4 o = a0 + (a1 + a2);
      u32x2 ov = {pk2(o[0], o[1]), pk2(o[2], o[3])};
      *(u32x2*)(outp + i * 16 * 1280) = ov;
    },
    [&](int) {});
}

DEV void out_task(const Params& p, rsrc_t ws, int s, int cg, unsigned char* lds) {
  const int tid = threadIdx.x, l = tid & 63, c16 = l & 15, g = l >> 4;
  const int w = __builtin_amdgcn_readfirstlane(tid >> 6);
  const int G = 8 * cg + w;
  const int pp = (c16 >> 1) & 1, j = 2 * (c16 >> 2) + (c16 & 1);
  const u32 ox = (u32)(pp * 16384 + g * 128 + j * 16), om = (u32)(pp * 4096 + g * 128 + j * 16);
  bool dead = false;
  if (w == 0) wait_prog(ws, OFF_PVU + (u32)seg_prod(s) * 128, 32, seg_need(s), l, dead, 1);
  __syncthreads();
  u32x4 ua[32];
#pragma unroll
  for (int h = 0; h < 32; ++h) { ua[h] = ld16c(ws, ox + (h & 7) * 512, OFF_UX + (u32)((s * 80 + 2 * G) * 16384 + (h >> 3) * 4096)); THROTTLE(h); }
  SCHED_FENCE();
  if (w < 2) {
    wait_prog(ws, OFF_PPX + (u32)(cg + 5 * (s & 3)) * 128, 1, (u32)s, l, dead, 2);
    if (w == 0) wait_prog(ws, OFF_PPX, 32, (u32)s + 1, l, dead, 1, 128);
    else wait_prog(ws, OFF_PUM + (u32)cg * 2048, 16, (u32)s + 1, l, dead, 1, 128);
  }
  __syncthreads();
  u32x4 pr[10], ub[8];
#pragma unroll
  for (int i = 0; i < 8; ++i) pr[i] = ld16c(ws, tid * 16, OFF_PXX + (u32)(s * 65536 + i * 8192));
#pragma unroll
  for (int i = 0; i < 2; ++i) pr[8 + i] = ld16c(ws, tid * 16, OFF_PXM + (u32)(s * 16384 + i * 8192));
#pragma unroll
  for (int q = 0; q < 8; ++q) ub[q] = ld16c(ws, om + q * 512, OFF_UM + (u32)((s * 80 + 2 * G) * 4096));
#pragma unroll
  for (int i = 0; i < 10; ++i) *(u32x4*)(lds + (i * 512 + tid) * 16) = pr[i];
  __syncthreads();
  f32x4 acc[2] = {};
  {
    const unsigned char* px = lds + l * 16;
    const unsigned char* pm = lds + 65536 + (g * 16 + c16) * 16;
    auto rd = [&](int i) { return i < 64 ? *(const u32x4*)(px + i * 1024) : *(const u32x4*)(pm + (i & 1) * 8192 + ((i - 64) >> 1) * 1024); };
    u32x4 pb[12];
#pragma unroll
    for (int i = 0; i < 12; ++i) pb[i] = rd(i);
    SCHED_FENCE();
#pragma unroll
    for (int i = 0; i < 80; ++i) {
      acc[i & 1] = mfma(i < 64 ? ua[i >> 1] : ub[(i - 64) >> 1], pb[i % 12], acc[i & 1]);
      if (i + 12 < 80) pb[i % 12] = rd(i + 12);
      SCHED_FENCE();
    }
  }
#pragma unroll
  for (int tt = 0; tt < 2; ++tt) {
    u32x2 o = {pk2(acc[tt][0], acc[tt][1]), pk2(acc[tt][2], acc[tt][3])};
    *(u32x2*)(p.out + (32 * s + 16 * tt + c16) * 1280 + 640 + 16 * G + 4 * g) = o;
  }
}

DEV void out_fast(const Params& p, rsrc_t ws, int s, int G, unsigned char* lds) {
  const int tid = threadIdx.x, l = tid & 63, c16 = l & 15, g = l >> 4;
  const int w = __builtin_amdgcn_readfirstlane(tid >> 6);
  const int pp = (c16 >> 1) & 1, j = 2 * (c16 >> 2) + (c16 & 1);
  const u32 ox = (u32)(pp * 16384 + g * 128 + j * 16), om = (u32)(pp * 4096 + g * 128 + j * 16);
  bool dead = false;
  if (w == 0) wait_prog(ws, OFF_PVU + (u32)seg_prod(s) * 128, 32, seg_need(s), l, dead, 1);
  __syncthreads();
  u32x4 ua[4];
#pragma unroll
  for (int h = 0; h < 4; ++h) ua[h] = ld16c(ws, ox + h * 512, OFF_UX + (u32)((s * 80 + 2 * G) * 16384 + w * 2048));
  SCHED_FENCE();
  wait_prog(ws, OFF_PPX + (u32)(G & 31) * 128, 1, (u32)s, l, dead, 2);
  wait_prog(ws, OFF_PUM + (u32)G * 256, 2, (u32)s + 1, l, dead, 0, 128);
  const u32x4 ub = ld16c(ws, om, OFF_UM + (u32)((s * 80 + 2 * G) * 4096 + w * 512));
  wait_prog(ws, OFF_PPX + (u32)w * 512, 4, (u32)s + 1, l, dead, 0, 128);
  u32x4 pf[10];
#pragma unroll
  for (int i = 0; i < 8; ++i) pf[i] = ld16c(ws, l * 16 + (i & 3) * 1024, OFF_PXX + (u32)(s * 65536 + w * 8192 + (i >> 2) * 4096));
#pragma unroll
  for (int tt = 0; tt < 2; ++tt) pf[8 + tt] = ld16c(ws, l * 16, OFF_PXM + (u32)(s * 16384 + tt * 8192 + w * 1024));
  f32x4 acc[2] = {};
#pragma unroll
  for (int i = 0; i < 10; ++i) acc[i & 1] = mfma(i < 8 ? ua[i >> 1] : ub, pf[i], acc[i & 1]);
  *(f32x4*)(lds + (w * 2) * 1024 + l * 16) = acc[0];
  *(f32x4*)(lds + (w * 2 + 1) * 1024 + l * 16) = acc[1];
  __syncthreads();
  if (w < 2) {
    f32x4 t = *(const f32x4*)(lds + w * 1024 + l * 16);
#pragma unroll
    for (int k = 1; k < 8; ++k) t += *(const f32x4*)(lds + (k * 2 + w) * 1024 + l * 16);
    u32x2 o = {pk2(t[0], t[1]), pk2(t[2], t[3])};
    *(u32x2*)(p.out + (32 * s + 16 * w + c16) * 1280 + 640 + 16 * G + 4 * g) = o;
  }
}

DEV void prefetch_role(rsrc_t ws, int x, int k) {
  const int l = threadIdx.x & 63;
  const int w = __builtin_amdgcn_readfirstlane(threadIdx.x >> 6);
  if (w >= PFW) return;
  bool dead = false;
  u32 seen0 = 0, seen1 = 0;
  for (int s = 1; s < NSEG; ++s) {
    const u32 prog = wait_prog(ws, OFF_PUM + (u32)(x * 16 + k) * 128, 1, (u32)(s > PFD ? s - PFD : 0), l, dead, 1);
    if ((u32)s <= prog + 1) { s = (int)prog + 1; continue; }
    const u32 need = seg_need(s);
    if (seg_prod(s)) { if (seen1 < need) seen1 = wait_prog(ws, OFF_PVU + 128, 32, need, l, dead, 1); }
    else { if (seen0 < need) seen0 = wait_prog(ws, OFF_PVU, 32, need, l, dead, 1); }
#pragma nounroll
    for (int i = w; i < 14 * (8192 / (64 * PFSTR)); i += PFW) {
      const int u = k * 14 + i / (8192 / (64 * PFSTR));
      u32 o;
      if (u >= 40) o = ((u & 1) ? OFF_QX : OFF_KX) + (u32)((s * 32 + x * 8 + ((u - 40) >> 1)) * 8192);
      else o = OFF_UX + (u32)((s * 80 + x * 20) * 16384 + u * 8192);
      if (false) {}
      asm volatile("buffer_load_dword v255, %0, %1, %2 offen sc1\n\ts_waitcnt vmcnt(%3)"
                   :: "v"(l * PFSTR), "s"(ws), "s"(o + (u32)(i % (8192 / (64 * PFSTR))) * (64 * PFSTR)), "n"(PFQ) : "memory", "v255");
      __builtin_amdgcn_s_sleep(PFS);
    }
  }
  VM_DRAIN();
}

constexpr int T_SET = 36, T_F1 = 40, T_F2 = 80, T_F3 = 40, T_OUT = 310, T_FAST = 80;
DEV int next_ticket(const Params& p, unsigned char* lds, u32 off) {
  u32* tk = (u32*)(lds + LDS_MAIN - 16);
  __syncthreads();
  if (threadIdx.x == 0) *tk = __hip_atomic_fetch_add((u32*)(p.ws + off), 1u, __ATOMIC_RELAXED, __HIP_MEMORY_SCOPE_AGENT);
  __syncthreads();
  return __builtin_amdgcn_readfirstlane(*tk);
}

DEV u32 claim_role(const Params& p, rsrc_t ws, unsigned char* lds) {
  u32* tk = (u32*)(lds + LDS_MAIN - 16);
  if (threadIdx.x < 64) {
    const int l = threadIdx.x;
    const u32 xcc = (XPERM >> (4 * ((u32)__builtin_amdgcn_s_getreg(20 | (3 << 11)) & XCCMASK))) & 7u, sl = blockIdx.x >> 3;
    u32 old = 0;
    if (l == 0) old = __hip_atomic_fetch_or((u32*)(p.ws + OFF_ROLE + xcc * 128), 1u << sl, __ATOMIC_RELAXED, __HIP_MEMORY_SCOPE_AGENT);
    old = __builtin_amdgcn_readfirstlane(old);
    u32 pos = xcc * 32 + sl;
    if (__builtin_expect((old >> sl) & 1u, 0)) {
      u32 k = 0;
      if (l == 0) k = __hip_atomic_fetch_add((u32*)(p.ws + OFF_ROLE + 1024), 1u, __ATOMIC_RELAXED, __HIP_MEMORY_SCOPE_AGENT);
      k = __builtin_amdgcn_readfirstlane(k);
      u32 c = 0;
#pragma nounroll
      for (u32 spins = 0; spins < SPIN_CAP; ++spins) {
        asm volatile("" ::: "memory");
        c = ld4c(ws, (u32)(l < 8 ? l : 8) * 128, OFF_ROLE);
        u32 sum = (u32)__builtin_amdgcn_readlane(c, 8);
        for (int j = 0; j < 8; ++j) sum += (u32)__builtin_popcount((u32)__builtin_amdgcn_readlane(c, j));
        if (sum >= 256) break;
        __builtin_amdgcn_s_sleep(4);
      }
      pos = 255;
#pragma nounroll
      for (u32 q = 0; q < 256; ++q) {
        const u32 m = (u32)__builtin_amdgcn_readlane(c, q >> 5);
        if (!((m >> (q & 31)) & 1u)) { if (k == 0) { pos = q; break; } --k; }
      }
    }
    if (l == 0) *tk = pos;
  }
  __syncthreads();
  return __builtin_amdgcn_readfirstlane(*tk);
}

__global__ __launch_bounds__(512, 2) void k_main(Params p, int mode) {
  extern __shared__ __attribute__((aligned(16))) unsigned char lds[];
  const rsrc_t ws = mk_rsrc(p.ws, WS_BYTES);
  const u32 cb = comm_base(p.ws);
  const int b = blockIdx.x;
  if (mode & 1) GSTAMP(0);
  if (b >= 4) {
    st16c(ws, cb + (u32)(b * 1024 + threadIdx.x) * 16, 0, (u32x4){SENT, SENT, SENT, SENT});
    st16c(ws, cb + (u32)(b * 1024 + 512 + threadIdx.x) * 16, 0, (u32x4){SENT, SENT, SENT, SENT});
  }
  if (b < 112) st16c(ws, OFF_SIDE + (u32)(b * 512 + threadIdx.x) * 16, 0, (u32x4){SENT, SENT, SENT, SENT});
  asm volatile("" ::: "memory");
  SCHED_FENCE();
  const u32 pos = claim_role(p, ws, lds);
  const int x = (int)(pos >> 5), slot = (int)(pos & 31);
  const int cidx = (x >= 4 || slot >= 28) ? 1000 : (slot < 8 ? x * 8 + slot : 32 + x * 20 + slot - 8);
  if (cidx < 112) {
    if (!(mode & 1)) return;
    if (cidx < 32) score_role(p, ws, cb, cidx, lds);
    else value_role(p, ws, cb, cidx - 32, lds);
    GSTAMP(3);
    return;
  }
  if (x < 4) {
    if (mode & 1) {
      init_part(p, ws, cb, x * 4 + slot - 28);
      prefetch_role(ws, x, slot - 28);
    }
    return;
  }
  if (x >= 4 && (mode & 2)) {
    const int h = (x - 4) * 8 + (slot >> 2);
    const bool bw = (slot & 3) == 1 || (slot & 7) == 7;
    if (bw) b_unit(p, ws, 80 + (x - 4) * 12 + ((slot & 2) ? 8 + (slot >> 3) : (slot >> 2)), lds);
    if (slot & 2) vu_role(p, ws, h, slot & 1, lds, !bw);
    else if (!(slot & 1)) qk_role(p, ws, h, 0, lds, true);
    else if (!bw) { VM_DRAIN(); __syncthreads(); init_signal(p, true); }
    GSTAMP(2);
  }
  if (!(mode & 4)) return;
  const bool outonly = (slot & 3) == 3 && (slot & 4);
  if (!outonly) {
    int t = next_ticket(p, lds, OFF_TICKET) - T_SET;
    while (t < 0) { const int k = t + T_SET; if (k < 20) fw_task(p, ws, k, lds); else wt_task(p, ws, k - 20, lds); t = next_ticket(p, lds, OFF_TICKET) - T_SET; }
    while (t < T_F1) { f1_task(p, ws, t % 5, t / 5, lds); t = next_ticket(p, lds, OFF_TICKET) - T_SET; }
    GSTAMP(4);
    while (t < T_F1 + T_F2) {
      const int q = t - T_F1;
      if (threadIdx.x < 64) { bool dead = false; wait_prog(ws, OFF_F1C + (u32)(q >> 4) * 128, 1, 8, threadIdx.x, dead, 1); }
      wait_fw(ws);
      fft_cols(p, ws, q, lds);
      VM_DRAIN();
      __syncthreads();
      if (threadIdx.x == 0) __hip_atomic_fetch_add((u32*)(p.ws + OFF_F2C), 1u, __ATOMIC_RELAXED, __HIP_MEMORY_SCOPE_AGENT);
      t = next_ticket(p, lds, OFF_TICKET) - T_SET;
    }
    GSTAMP(5);
    while (t < T_F1 + T_F2 + T_F3) { const int q = t - T_F1 - T_F2; f3_task(p, ws, q % 5, q / 5, lds); t = next_ticket(p, lds, OFF_TICKET) - T_SET; }
  }
  GSTAMP(6);
  int t2 = next_ticket(p, lds, OFF_TICKET2);
  for (; t2 < T_OUT; t2 = next_ticket(p, lds, OFF_TICKET2)) out_task(p, ws, t2 / 5, t2 % 5, lds);
  for (; t2 < T_OUT + T_FAST; t2 = next_ticket(p, lds, OFF_TICKET2)) out_fast(p, ws, NSEG - 2 + (t2 - T_OUT) / 40, (t2 - T_OUT) % 40, lds);
  GSTAMP(3);
}

__global__ __launch_bounds__(384) void k_zero(unsigned char* ws) {
  const rsrc_t r = mk_rsrc(ws, WS_BYTES);
  st16c(r, OFF_FLAGS + (u32)(blockIdx.x * 384 + threadIdx.x) * 16, 0, (u32x4){0, 0, 0, 0});
}

extern "C" void kernel_launch(void* const* d_in, const int* in_sizes, int n_in, void* d_out, int out_size, void* d_ws, size_t ws_size,
                              hipStream_t stream) {
  Params p;
  p.ax = (const u16*)d_in[0]; p.wr = (const u16*)d_in[1]; p.wi = (const u16*)d_in[2];
  p.x = (const u16*)d_in[3]; p.mem0 = (const u16*)d_in[4]; p.wq = (const u16*)d_in[5]; p.wk = (const u16*)d_in[6];
  p.wv = (const u16*)d_in[7]; p.wo = (const u16*)d_in[8];
  p.out = (u16*)d_out; p.ws = (unsigned char*)d_ws;
  (void)hipFuncSetAttribute((const void*)k_main, hipFuncAttributeMaxDynamicSharedMemorySize, LDS_MAIN);
  k_zero<<<8, 384, 0, stream>>>(p.ws);
  k_main<<<256, 512, LDS_MAIN, stream>>>(p, 7);
}
```

```cpp
#include <hip/hip_runtime.h>
#include <hip/hip_bf16.h>
#include <stdint.h>

typedef unsigned short u16;
typedef unsigned int u32;
typedef unsigned long long u64;
typedef __bf16 bf16x2 __attribute__((ext_vector_type(2)));
typedef __bf16 bf16x8 __attribute__((ext_vector_type(8)));
typedef __bf16 bf16x16 __attribute__((ext_vector_type(16)));
typedef float f32x2 __attribute__((ext_vector_type(2)));
typedef float f32x4 __attribute__((ext_vector_type(4)));
typedef u32 u32x2 __attribute__((ext_vector_type(2)));
typedef u32 u32x4 __attribute__((ext_vector_type(4)));
typedef u32 u32x8 __attribute__((ext_vector_type(8)));
typedef __amdgpu_buffer_rsrc_t rsrc_t;
#define DEV __device__ __forceinline__

constexpr int NSEG = 64;
constexpr u32 MB = 1u << 20;
constexpr u32 OFF_BF = 26 * MB;
constexpr u32 OFF_XF = 51 * MB;
constexpr u32 OFF_KX = 57 * MB;
constexpr u32 OFF_QX = 73 * MB;
constexpr u32 OFF_UX = 89 * MB;
constexpr u32 OFF_UM = 169 * MB;
constexpr u32 OFF_PXX = 189 * MB;
constexpr u32 OFF_PXM = 193 * MB;
constexpr u32 OFF_FX = 194 * MB;
constexpr u32 OFF_FY = 200 * MB;
constexpr u32 OFF_FLAGS = 206 * MB;
constexpr u32 OFF_FW1 = 208 * MB;
constexpr u32 OFF_FW3 = 210 * MB;
constexpr u32 OFF_SIDE = 212 * MB;
constexpr u32 SIDE_STEP = 112 * 128;
constexpr u32 OFF_WT = 213 * MB;
constexpr u32 WS_BYTES = 216 * MB;
constexpr u32 STEP_BYTES = 65536;
constexpr u32 SENT = 0xFFFFFFFFu;
constexpr u32 SPIN_CAP = 1u << 16;
constexpr u32 OFF_PQK = OFF_FLAGS + 0xA000;
constexpr u32 OFF_PVU = OFF_FLAGS + 0xB000;
constexpr u32 OFF_PPX = OFF_FLAGS;
constexpr u32 OFF_PUM = OFF_FLAGS + 0x1000;
constexpr u32 OFF_TICKET = OFF_FLAGS + 0xB400;
constexpr u32 OFF_F1C = OFF_FLAGS + 0xB480;
constexpr u32 OFF_F2C = OFF_FLAGS + 0xB800;
constexpr u32 OFF_FWC = OFF_FLAGS + 0xB880;
constexpr u32 OFF_TICKET2 = OFF_FLAGS + 0xB900;
constexpr u32 OFF_INITC = OFF_FLAGS + 0xBC00;
constexpr u32 OFF_ROLE = OFF_FLAGS + 0x9000;
constexpr u32 OFF_XFC = OFF_FLAGS + 0xBF00;
constexpr u32 OFF_BC = OFF_FLAGS + 0xBE00;
constexpr u32 FLAG_BYTES = 0xC000;
constexpr u32 OFF_VOTE = OFF_FLAGS + 0xBA00;
constexpr int LDS_MAIN = 97 * 1024;
#define HS0 6
#define HS1 16
constexpr int NHELP = (HS1 - HS0) / 2;
static_assert(HS0 >= 2 && HS0 <= 6 && HS0 % 2 == 0 && HS1 % 2 == 0 && NHELP >= 3, "see the x-fragment check in the producers");
__host__ __device__ constexpr int seg_prod(int s) { return (s >= HS0 && s < HS1) ? (s & 1) : 0; }
__host__ __device__ constexpr u32 seg_need(int s) {
  return s < HS0 ? (u32)s + 1 : (s < HS1 ? ((s & 1) ? (u32)((s - HS0) >> 1) + 1 : (u32)(HS0 + ((s - HS0) >> 1)) + 1) : (u32)(s - NHELP) + 1);
}
__host__ __device__ constexpr int tile_seg(int par, int i) {
  return par ? HS0 + 2 * i + 1 : (i < HS0 ? i : (i < HS0 + NHELP ? HS0 + 2 * (i - HS0) : i + NHELP));
}

struct Params {
  const u16* ax; const u16* wr; const u16* wi;
  const u16* x; const u16* mem0; const u16* wq; const u16* wk; const u16* wv; const u16* wo;
  u16* out; unsigned char* ws;
};

DEV rsrc_t mk_rsrc(const void* p, u32 bytes) { return __builtin_amdgcn_make_buffer_rsrc((void*)p, 0, bytes, 0x00020000); }
DEV u32x4 ld16(rsrc_t r, u32 vo, u32 so) { return __builtin_amdgcn_raw_buffer_load_b128(r, vo, so, 0); }
DEV u32x4 ld16c(rsrc_t r, u32 vo, u32 so) { return __builtin_amdgcn_raw_buffer_load_b128(r, vo, so, 16); }
DEV u32x2 ld8c(rsrc_t r, u32 vo, u32 so) { return __builtin_amdgcn_raw_buffer_load_b64(r, vo, so, 16); }
DEV u32 ld4c(rsrc_t r, u32 vo, u32 so) { return __builtin_amdgcn_raw_buffer_load_b32(r, vo, so, 16); }
DEV void st16c(rsrc_t r, u32 vo, u32 so, u32x4 v) { __builtin_amdgcn_raw_buffer_store_b128(v, r, vo + so, 0, 16); }
DEV void st8c(rsrc_t r, u32 vo, u32 so, u32x2 v) { __builtin_amdgcn_raw_buffer_store_b64(v, r, vo, so, 16); }
DEV void st4c(rsrc_t r, u32 vo, u32 so, u32 v) { __builtin_amdgcn_raw_buffer_store_b32(v, r, vo, so, 16); }
DEV u32 pk2(float a, float b) { bf16x2 v = {(__bf16)a, (__bf16)b}; return __builtin_bit_cast(u32, v); }
DEV u32 pk2s(float a, float b) { u32 v = pk2(a, b); return v < 0xFFFFFFFEu ? v : 0xFFFFFFFEu; }
DEV u32 pk2lo(float a, float b, u32 hi) { return pk2(a - __uint_as_float(hi << 16), b - __uint_as_float(hi & 0xFFFF0000u)); }
DEV f32x4 mfma(u32x4 a, u32x4 b, f32x4 c) {
  return __builtin_amdgcn_mfma_f32_16x16x32_bf16(__builtin_bit_cast(bf16x8, a), __builtin_bit_cast(bf16x8, b), c, 0, 0, 0);
}
DEV f32x4 smfma(u32x4 a, u32x8 b, f32x4 c, int idx) {
  return __builtin_amdgcn_smfmac_f32_16x16x64_bf16(__builtin_bit_cast(bf16x8, a), __builtin_bit_cast(bf16x16, b), c, idx, 0, 0);
}
DEV u32x8 cat(u32x4 a, u32x4 b) { return __builtin_shufflevector(a, b, 0, 1, 2, 3, 4, 5, 6, 7); }
DEV u32 comm_base(const unsigned char* ws) { return (u32)((2 * MB - ((uintptr_t)ws & (2 * MB - 1))) & (2 * MB - 1)); }
DEV u64 stamp_cyc() { u64 t; asm volatile("s_memtime %0\n\ts_waitcnt lgkmcnt(0)" : "=s"(t) :: "memory"); return t; }
DEV u32 umax3(u32 a, u32 b, u32 c) { u32 m = a > b ? a : b; return m > c ? m : c; }
DEV float fmx(float a, float b) { return __builtin_elementwise_maximum(a, b); }
#define SCHED_FENCE() __builtin_amdgcn_sched_barrier(0)
#define VM_DRAIN() asm volatile("s_waitcnt vmcnt(0)" ::: "memory")
#define RAW_BARRIER() do { asm volatile("s_waitcnt lgkmcnt(0)" ::: "memory"); __builtin_amdgcn_s_barrier(); asm volatile("" ::: "memory"); } while (0)


typedef short s16x4 __attribute__((ext_vector_type(4)));
#define LAS __attribute__((address_space(3)))
DEV u32x4 tr_frag(const unsigned char* a0, const unsigned char* a1) {
  const u32x2 x = __builtin_bit_cast(u32x2, __builtin_amdgcn_ds_read_tr16_b64_v4i16((LAS s16x4*)a0));
  const u32x2 y = __builtin_bit_cast(u32x2, __builtin_amdgcn_ds_read_tr16_b64_v4i16((LAS s16x4*)a1));
  return (u32x4){x.x, x.y, y.x, y.y};
}
DEV float dpp_ror8(float x) { return __uint_as_float(__builtin_amdgcn_update_dpp(0, (int)__float_as_uint(x), 0x128, 0xF, 0xF, true)); }
DEV float red_max_g(float m) {
  auto a = __builtin_amdgcn_permlane16_swap(__float_as_uint(m), __float_as_uint(m), false, false);
  m = fmx(__uint_as_float(a[0]), __uint_as_float(a[1]));
  auto b = __builtin_amdgcn_permlane32_swap(__float_as_uint(m), __float_as_uint(m), false, false);
  return fmx(__uint_as_float(b[0]), __uint_as_float(b[1]));
}
DEV float red_sum_g(float m) {
  auto a = __builtin_amdgcn_permlane16_swap(__float_as_uint(m), __float_as_uint(m), false, false);
  m = __uint_as_float(a[0]) + __uint_as_float(a[1]);
  auto b = __builtin_amdgcn_permlane32_swap(__float_as_uint(m), __float_as_uint(m), false, false);
  return __uint_as_float(b[0]) + __uint_as_float(b[1]);
}
DEV float red_max(float m) { return red_max_g(fmx(m, dpp_ror8(m))); }
DEV float red_sum(float m) { return red_sum_g(m + dpp_ror8(m)); }

#define NPROBE 3
DEV u32 probe(rsrc_t ws, u32 cb, int idx, int page, int l) {
  const u32 vo = (l == 0) ? 0u : 0x80000000u;
  const u32 so = cb + (u32)(page * 32 + 30) * STEP_BYTES + (u32)(2 + (idx >> 6)) * 16384 + (u32)(idx & 63) * 128;
  u32 b0 = ~0u, b1 = ~0u;
#pragma nounroll
  for (int rep = 0; rep < 2 * NPROBE; ++rep) {
    u64 t0 = stamp_cyc();
    st4c(ws, vo, so + (u32)(rep & 1) * 8192, SENT);
    VM_DRAIN();
    u64 t1 = stamp_cyc();
    u32 d = (u32)(t1 - t0);
    if (rep & 1) b1 = d < b1 ? d : b1; else b0 = d < b0 ? d : b0;
  }
  return b1 < b0 ? 1u : 0u;
}

DEV u32 xcc_group() { return ((u32)__builtin_amdgcn_s_getreg(20 | (3 << 11)) >> 2) & 3u; }
DEV void vote(const Params& p, int page, u32 r, int l) {
  if (l == 0) __hip_atomic_fetch_add((u32*)(p.ws + OFF_VOTE + (xcc_group() * 2 + page) * 64), r + 0x10000u, __ATOMIC_RELAXED, __HIP_MEMORY_SCOPE_AGENT);
}
DEV u32 vote_result(rsrc_t ws, int l) {
  const u32 x = ld4c(ws, (u32)(l & 1) * 64, OFF_VOTE + xcc_group() * 128);
  const u32 m = (2 * (x & 0xFFFFu) > (x >> 16)) ? 1u : 0u;
  return (u32)__builtin_amdgcn_readlane(m, 0) | ((u32)__builtin_amdgcn_readlane(m, 1) << 1);
}
#define D1 130
#define D2 14
#define D3 56
#define D4 12
#define DV 15
#define DX 25
#define DL 5
#define DLS 16
#define THR 6
#define THR_S2(x) #x
#define THR_S(x) THR_S2(x)
#define THROTTLE(i) asm volatile("s_waitcnt vmcnt(" THR_S(THR) ")" ::: "memory")
#define UMPOS 0
#define XPERM 0x76325410u
#define PFD 4
#define PFW 1
#define PFQ 4
#define PFS 3
#define PFSTR 128
#define PROT v
#define KQPOS 0
DEV u32 rtime() { return (u32)__builtin_amdgcn_s_memrealtime(); }
DEV void wait_until(u32 T) {
  u32 r = rtime();
  if ((int)(T - r) < 28) {
#pragma nounroll
    for (int i = 0; i < 64 && (int)(T - r) > 0; ++i) r = rtime();
    return;
  }
#pragma nounroll
  for (int i = 0; i < 4096 && (int)(T - r) > 35; ++i) r = rtime();
  u32 n;
  asm volatile(
    "s_memrealtime s[80:81]\n\ts_nop 2\n\ts_memrealtime s[82:83]\n\ts_nop 2\n\ts_memrealtime s[84:85]\n\ts_nop 2\n\ts_memrealtime s[86:87]\n\ts_nop 2\n\t"
    "s_memrealtime s[88:89]\n\ts_nop 2\n\ts_memrealtime s[90:91]\n\ts_nop 2\n\ts_memrealtime s[92:93]\n\ts_nop 2\n\ts_memrealtime s[94:95]\n\ts_waitcnt lgkmcnt(0)\n\t"
    "s_mov_b32 s81, 0\n\t"
    "s_cmp_eq_u32 s82, s80\n\ts_addc_u32 s81, s81, 0\n\t"
    "s_cmp_eq_u32 s84, s80\n\ts_addc_u32 s81, s81, 0\n\t"
    "s_cmp_eq_u32 s86, s80\n\ts_addc_u32 s81, s81, 0\n\t"
    "s_cmp_eq_u32 s88, s80\n\ts_addc_u32 s81, s81, 0\n\t"
    "s_cmp_eq_u32 s90, s80\n\ts_addc_u32 s81, s81, 0\n\t"
    "s_cmp_eq_u32 s92, s80\n\ts_addc_u32 s81, s81, 0\n\t"
    "s_cmp_eq_u32 s94, s80\n\ts_addc_u32 s81, s81, 0\n\t"
    "s_sub_u32 %0, %1, s80\n\t"
    "s_mul_i32 %0, %0, 3\n\t"
    "s_lshl1_add_u32 %0, s81, %0\n\t"
    "s_sub_i32 %0, %0, 64\n\t"
    "s_max_i32 %0, %0, 0\n\t"
    "s_min_i32 %0, %0, 63\n\t"
    "s_bitcmp1_b32 %0, 0\n\ts_cbranch_scc0 1f\n\ts_nop 5\n\t1:\n\t"
    "s_bitcmp1_b32 %0, 1\n\ts_cbranch_scc0 2f\n\ts_nop 7\n\t2:\n\t"
    "s_bitcmp1_b32 %0, 2\n\ts_cbranch_scc0 3f\n\ts_nop 11\n\t3:\n\t"
    "s_bitcmp1_b32 %0, 3\n\ts_cbranch_scc0 4f\n\ts_nop 15\n\ts_nop 3\n\t4:\n\t"
    "s_bitcmp1_b32 %0, 4\n\ts_cbranch_scc0 5f\n\ts_nop 15\n\ts_nop 15\n\ts_nop 3\n\t5:\n\t"
    "s_bitcmp1_b32 %0, 5\n\ts_cbranch_scc0 6f\n\ts_nop 15\n\ts_nop 15\n\ts_nop 15\n\ts_nop 15\n\ts_nop 3\n\t6:\n\t"
    : "=&s"(n) : "s"(T)
    : "scc", "memory", "s80", "s81", "s82", "s83", "s84", "s85", "s86", "s87", "s88", "s89", "s90", "s91", "s92", "s93", "s94", "s95");
}
template <int CTRL> DEV int dpp_i(int x) { return __builtin_amdgcn_update_dpp(x, x, CTRL, 0xF, 0xF, false); }
DEV int row_max(int d) {
  d = max(d, dpp_i<0x121>(d));
  d = max(d, dpp_i<0x122>(d));
  d = max(d, dpp_i<0x124>(d));
  return max(d, dpp_i<0x128>(d));
}
DEV int wave_max(int d) {
  d = row_max(d);
  return max(max(__builtin_amdgcn_readlane(d, 0), __builtin_amdgcn_readlane(d, 16)), max(__builtin_amdgcn_readlane(d, 32), __builtin_amdgcn_readlane(d, 48)));
}
DEV u32 fine_now() {
  u32 n;
  asm volatile(
    "s_memrealtime s[80:81]\n\ts_nop 2\n\ts_memrealtime s[82:83]\n\ts_nop 2\n\ts_memrealtime s[84:85]\n\ts_nop 2\n\ts_memrealtime s[86:87]\n\ts_nop 2\n\t"
    "s_memrealtime s[88:89]\n\ts_nop 2\n\ts_memrealtime s[90:91]\n\ts_nop 2\n\ts_memrealtime s[92:93]\n\ts_waitcnt lgkmcnt(0)\n\t"
    "s_mov_b32 s81, 0\n\t"
    "s_cmp_eq_u32 s82, s80\n\ts_addc_u32 s81, s81, 0\n\t"
    "s_cmp_eq_u32 s84, s80\n\ts_addc_u32 s81, s81, 0\n\t"
    "s_cmp_eq_u32 s86, s80\n\ts_addc_u32 s81, s81, 0\n\t"
    "s_cmp_eq_u32 s88, s80\n\ts_addc_u32 s81, s81, 0\n\t"
    "s_cmp_eq_u32 s90, s80\n\ts_addc_u32 s81, s81, 0\n\t"
    "s_cmp_eq_u32 s92, s80\n\ts_addc_u32 s81, s81, 0\n\t"
    "s_mul_i32 s81, s81, 11\n\t"
    "s_lshr_b32 s81, s81, 4\n\t"
    "s_add_u32 %0, s80, 4\n\t"
    "s_sub_u32 %0, %0, s81\n\t"
    : "=&s"(n) :
    : "scc", "memory", "s80", "s81", "s82", "s83", "s84", "s85", "s86", "s87", "s88", "s89", "s90", "s91", "s92", "s93");
  return n;
}
struct Side { u32x2 a, b; };
DEV Side load_side(rsrc_t ws, int s, int l) {
  Side t;
  t.a = ld8c(ws, (u32)l * 128, OFF_SIDE + (u32)s * SIDE_STEP);
  t.b = ld8c(ws, (u32)(64 + (l & 15)) * 128, OFF_SIDE + (u32)s * SIDE_STEP);
  return t;
}
DEV void schedule(rsrc_t ws, int s, int l, Side& t, u32& Rm, u32& Rp, bool& dead) {
  if (__builtin_expect(__any(umax3(t.a.x, t.a.y, t.b.x > t.b.y ? t.b.x : t.b.y) == SENT), 0)) {
#pragma nounroll
    for (u32 spins = 0; !dead; ++spins) {
      asm volatile("" ::: "memory");
      t = load_side(ws, s, l);
      if (__all(umax3(t.a.x, t.a.y, t.b.x > t.b.y ? t.b.x : t.b.y) != SENT)) break;
      if (spins > SPIN_CAP) dead = true;
    }
  }
  u32 B = Rm;
  if (__builtin_expect(__any((t.a.x | t.b.x) != 0), 0)) {
    const u32 ref = rtime();
    const int dx = t.a.x ? (int)(t.a.x - ref) : -(1 << 30), dy = t.b.x ? (int)(t.b.x - ref) : -(1 << 30);
    B = ref + (u32)wave_max(max(dx, dy)) + D4 + DX;
  }
  const u32 d = __builtin_amdgcn_readfirstlane(t.a.y);
  Rp = B + ((d >> 10) & 1023u);
  Rm = Rp + ((d >> 20) & 1023u);
}

DEV u32 gather_mem(rsrc_t ws, u32 sbp, int wr, unsigned char* dA, unsigned char* dB, int l, bool& dead) {
  u32x4 a = ld16c(ws, (u32)l * 16, sbp + (u32)wr * 1024);
  u32 b = ld4c(ws, (u32)l * 4, sbp + 16384 + (u32)wr * 256);
  SCHED_FENCE();
  *(u32x4*)dA = a;
  *(u32*)dB = b;
  u32 m = umax3(umax3(a.x, a.y, a.z), a.w, b);
  u32 spins = 0;
  if (__builtin_expect(__any(m == SENT), 0)) {
#pragma nounroll
    while (!dead) {
      ++spins;
      asm volatile("" ::: "memory");
      a = ld16c(ws, (u32)l * 16, sbp + (u32)wr * 1024);
      b = ld4c(ws, (u32)l * 4, sbp + 16384 + (u32)wr * 256);
      m = umax3(umax3(a.x, a.y, a.z), a.w, b);
      if (__all(m != SENT)) break;
      if (spins > SPIN_CAP) dead = true;
    }
    *(u32x4*)dA = a;
    *(u32*)dB = b;
  }
  return spins;
}
DEV u32 wait_prog(rsrc_t ws, u32 off, int n, u32 need, int l, bool& dead, int slp, u32 stride = 4) {
  u32 mn = 0;
#pragma nounroll
  for (u32 spins = 0;; ++spins) {
    asm volatile("" ::: "memory");
    const u32 x = ld4c(ws, (u32)(l < n ? l : 0) * stride, off);
    int d = -(int)x;
    d = row_max(d);
    mn = (u32)(-max(__builtin_amdgcn_readlane(d, 0), __builtin_amdgcn_readlane(d, 16)));
    if (mn >= need || dead) break;
    if (spins > SPIN_CAP) { dead = true; break; }
    if (slp == 2) __builtin_amdgcn_s_sleep(32); else if (slp) __builtin_amdgcn_s_sleep(6); else __builtin_amdgcn_s_sleep(1);
  }
  asm volatile("" ::: "memory");
  return mn;
}

DEV int wrow(int r) { return r * 288 + (r >> 3) * 128; }
DEV void load_qk(const Params& p, int h, int w, int l, unsigned char* lds, u32x4 (&wt)[2][20]) {
  const rsrc_t src = mk_rsrc((w >> 2) ? p.wk : p.wq, 640 * 8192);
  u32x4 st[40];
#pragma unroll
  for (int i = 0; i < 40; ++i)
    st[i] = ld16(src, (u32)((l >> 4) * 8192 + (l & 15) * 16), (u32)((32 * (w & 3) + (i >> 3) * 128 + (i & 7) * 4) * 8192 + h * 256));
  unsigned char* img = lds + (w >> 2) * 38912;
  unsigned char* wp = img + wrow(32 * (w & 3)) + (l >> 4) * 288 + (l & 15) * 16;
  const unsigned char* rp = img + wrow(8 * (l >> 4)) + ((l >> 2) & 3) * 288 + (w & 3) * 64 + (l & 3) * 8;
#pragma unroll
  for (int r = 0; r < 5; ++r) {
#pragma unroll
    for (int i = 0; i < 8; ++i) *(u32x4*)(wp + wrow(4 * i)) = st[r * 8 + i];
    __syncthreads();
#pragma unroll
    for (int kk = 0; kk < 4; ++kk)
#pragma unroll
      for (int i = 0; i < 2; ++i) wt[i][4 * r + kk] = tr_frag(rp + wrow(32 * kk) + i * 32, rp + wrow(32 * kk) + 4 * 288 + i * 32);
    __syncthreads();
  }
}
DEV void load_vu(const Params& p, int h, int w, int l, unsigned char* lds, u32x4 (&wv)[20], u32x4 (&wo)[5][4]) {
  const rsrc_t sv = mk_rsrc(p.wv, 640 * 8192), so = mk_rsrc(p.wo, 640 * 8192);
  u32x4 st[40];
#pragma unroll
  for (int i = 0; i < 20; ++i)
    st[i] = ld16(sv, (u32)((l >> 4) * 8192 + (l & 15) * 16), (u32)((40 * w + (i / 10) * 320 + (i % 10) * 4) * 8192 + h * 256));
#pragma unroll
  for (int i = 0; i < 20; ++i) st[20 + i] = ld16(so, (u32)l * 16, (u32)(h * 163840 + (w * 640 + (i / 10) * 5120 + (i % 10) * 64) * 16));
  unsigned char* wp = lds + wrow(40 * w) + (l >> 4) * 288 + (l & 15) * 16;
  const unsigned char* rp = lds + wrow(8 * (l >> 4)) + ((l >> 2) & 3) * 288 + w * 32 + (l & 3) * 8;
#pragma unroll
  for (int r = 0; r < 2; ++r) {
#pragma unroll
    for (int i = 0; i < 10; ++i) *(u32x4*)(wp + wrow(4 * i)) = st[r * 10 + i];
    __syncthreads();
#pragma unroll
    for (int kk = 0; kk < 10; ++kk) wv[10 * r + kk] = tr_frag(rp + wrow(32 * kk), rp + wrow(32 * kk) + 4 * 288);
    __syncthreads();
  }
  const unsigned char* ro = lds + (4 * (l >> 4) + ((l >> 2) & 3)) * 1312 + w * 160 + (l & 3) * 8;
#pragma unroll
  for (int r = 0; r < 2; ++r) {
#pragma unroll
    for (int i = 0; i < 10; ++i) {
      const int c = (w * 10 + i) * 64 + l;
      *(u32x4*)(lds + (c / 80) * 1312 + (c % 80) * 16) = st[20 + r * 10 + i];
    }
    __syncthreads();
#pragma unroll
    for (int kk = 0; kk < 2; ++kk)
#pragma unroll
      for (int ct = 0; ct < 5; ++ct) wo[ct][2 * r + kk] = tr_frag(ro + 32 * kk * 1312 + ct * 32, ro + (32 * kk + 16) * 1312 + ct * 32);
    __syncthreads();
  }
}

DEV void count_done(const Params& p, u32 off) {
  VM_DRAIN();
  __syncthreads();
  if (threadIdx.x == 0) __hip_atomic_fetch_add((u32*)(p.ws + off), 1u, __ATOMIC_RELAXED, __HIP_MEMORY_SCOPE_AGENT);
}
DEV void wait_init(rsrc_t ws) {
  if (threadIdx.x < 64) { bool dead = false; wait_prog(ws, OFF_XFC, 1, 16, threadIdx.x, dead, 0); }
  __syncthreads();
}
DEV void b_unit(const Params& p, rsrc_t ws, int u, unsigned char* lds) {
  const int tid = threadIdx.x, l = tid & 63, c16 = l & 15, g = l >> 4;
  const int w = __builtin_amdgcn_readfirstlane(tid >> 6);
  const int h = u >> 2, dh = (u >> 1) & 1, ch = u & 1;
  const rsrc_t rv = mk_rsrc(p.wv, 640 * 8192), ro = mk_rsrc(p.wo, 640 * 8192);
  u32x4 wo_r[10];
#pragma unroll
  for (int i = 0; i < 10; ++i) {
    const int c = tid + 512 * i, e = c / 40, cc = c % 40;
    wo_r[i] = ld16(ro, (u32)(e * 1280 + cc * 16), (u32)(h * 163840 + ch * 640));
  }
  u32x4 a[3][2][4];
#pragma unroll
  for (int i = 0; i < 3; ++i) {
    const int bl = (w & 3) + 4 * i;
    const u32 so = bl < 10 ? (u32)((32 * (10 * dh + bl)) * 8192 + h * 256) : 0x80000000u;
#pragma unroll
    for (int t = 0; t < 2; ++t)
#pragma unroll
      for (int ks = 0; ks < 4; ++ks)
        a[i][t][ks] = ld16(rv, (u32)((8 * (c16 >> 2) + (c16 & 3) + 4 * t) * 8192 + g * 16 + ks * 64), so);
  }
  SCHED_FENCE();
#pragma unroll
  for (int i = 0; i < 10; ++i) {
    const int c = tid + 512 * i, e = c / 40, cc = c % 40;
    *(u32x4*)(lds + e * 672 + (e >> 3) * 128 + cc * 16) = wo_r[i];
  }
  __syncthreads();
  const u32 vo = (u32)(((c16 >> 1) & 1) * (16 * 20 * 1024) + (16 * g + 8 * (h & 1) + 2 * (c16 >> 2) + (c16 & 1)) * 16);
#pragma nounroll
  for (int half = 0; half < 2; ++half) {
    u32x4 bf[5][4];
    const unsigned char* rp = lds + (8 * g + ((l >> 2) & 3)) * 672 + g * 128 + half * 320 + (w >> 2) * 160 + (l & 3) * 8;
#pragma unroll
    for (int ct = 0; ct < 5; ++ct)
#pragma unroll
      for (int ks = 0; ks < 4; ++ks) bf[ct][ks] = tr_frag(rp + ks * (32 * 672 + 512) + ct * 32, rp + ks * (32 * 672 + 512) + 4 * 672 + ct * 32);
#pragma unroll
    for (int i = 0; i < 3; ++i) {
      const int bl = (w & 3) + 4 * i;
      if (bl < 10) {
#pragma unroll
        for (int ct = 0; ct < 5; ++ct) {
          f32x4 c0 = {0, 0, 0, 0}, c1 = {0, 0, 0, 0};
#pragma unroll
          for (int ks = 0; ks < 4; ++ks) { c0 = mfma(a[i][0][ks], bf[ct][ks], c0); c1 = mfma(a[i][1][ks], bf[ct][ks], c1); }
          u32x4 o = {pk2(c0[0], c0[1]), pk2(c0[2], c0[3]), pk2(c1[0], c1[1]), pk2(c1[2], c1[3])};
          st16c(ws, vo, OFF_BF + (u32)(((2 * (20 * ch + 10 * half + 5 * (w >> 2) + ct) * 16 + (h >> 1)) * 20 + 10 * dh + bl) * 1024), o);
        }
      }
    }
  }
  count_done(p, OFF_BC + (u32)ch * 128);
}
DEV void init_part(const Params& p, rsrc_t ws, u32 cb, int k) {
  const int tid = threadIdx.x;
  u32x4 xw[20];
#pragma unroll
  for (int i = 0; i < 20; ++i) {
    const int seg = k + 16 * (i / 5), r = (i % 5) * 512 + tid, tt = r / 1280, ks = (r % 1280) >> 6, ln = r & 63;
    xw[i] = *(const u32x4*)(p.x + (32 * seg + 16 * tt + (ln & 15)) * 640 + 32 * ks + 8 * (ln >> 4));
    if (i == 4) SCHED_FENCE();
  }
  if (tid < 256) {
    const u32 o = (u32)(k * 256 + tid) * 16;
    const u32 L = (o >> 14) * 8192 + (o & 8191);
    u32x4 v = {SENT, SENT, SENT, SENT};
    if (L < 10240) {
      const int vv = L >> 7, t = (L >> 4) & 7;
      const u16* m = p.mem0 + t * 640 + 16 * (vv >> 1) + 2 * (vv & 1);
      for (int q = 0; q < 4; ++q) { u32 x = *(const u32*)(m + 4 * q); v[q] = x < 0xFFFFFFFEu ? x : 0xFFFFFFFEu; }
    }
    st16c(ws, cb + o, 0, v);
  }
#pragma unroll
  for (int i = 0; i < 5; ++i) st16c(ws, (u32)(i * 512 + tid) * 16, OFF_XF + (u32)k * 40960, xw[i]);
  count_done(p, OFF_XFC);
#pragma unroll
  for (int i = 5; i < 20; ++i) st16c(ws, (u32)((i % 5) * 512 + tid) * 16, OFF_XF + (u32)(k + 16 * (i / 5)) * 40960, xw[i]);
  count_done(p, OFF_XFC + 64);
}

constexpr float CEXP = 0.08838834764831845f * 1.4426950408889634f;
constexpr float NEGS = -1.0e9f / 0.08838834764831845f;
constexpr int QS = 288;

DEV void read_memf(const unsigned char* memf, int l, u32x4 (&a)[10]) {
#pragma unroll
  for (int ks = 0; ks < 10; ++ks) a[ks] = *(const u32x4*)(memf + ks * 1024 + l * 16);
}
DEV void project2(const u32x4 (&a)[10], const u32x8 (&wt)[2][10], int sidx, unsigned char* dst) {
  f32x4 c0 = {0, 0, 0, 0}, c1 = {0, 0, 0, 0};
#pragma unroll
  for (int ks = 0; ks < 10; ++ks) {
    c0 = smfma(a[ks], wt[0][ks], c0, sidx);
    c1 = smfma(a[ks], wt[1][ks], c1, sidx);
  }
  *(__bf16*)(dst) = (__bf16)(c0[0] + c0[1]);
  *(__bf16*)(dst + QS) = (__bf16)(c0[2] + c0[3]);
  *(__bf16*)(dst + 8) = (__bf16)(c1[0] + c1[1]);
  *(__bf16*)(dst + 8 + QS) = (__bf16)(c1[2] + c1[3]);
}

DEV void score_role(const Params& p, rsrc_t ws, u32 cb, int h, unsigned char* lds) {
  const int tid = threadIdx.x, l = tid & 63, c16 = l & 15, g = l >> 4;
  const int w = __builtin_amdgcn_readfirstlane(tid >> 6);
  unsigned char* memf = lds;
  unsigned char* qs = lds + 10240;
  unsigned char* kms = qs + 8 * QS;
  u32* ctl = (u32*)(lds + 15360);
  unsigned char* kq = lds + 16384;
  u32x8 wt[2][10];
  {
    u32x4 wf[2][20];
    load_qk(p, h, w, l, lds, wf);
#pragma unroll
    for (int i = 0; i < 2; ++i)
#pragma unroll
      for (int ks = 0; ks < 10; ++ks) wt[i][ks] = cat(wf[i][2 * ks], wf[i][2 * ks + 1]);
  }
  bool dead = false;
  if (w == 0) wait_prog(ws, OFF_INITC, 8, 32, l, dead, 0, 64);
  if (w == 1) wait_prog(ws, OFF_XFC, 1, 16, l, dead, 0);
  __syncthreads();
  if (tid < 2) ctl[2 + tid] = 0;
  u32 seen0 = 0;
  const u32 kq_src = ((w & 2) ? OFF_QX : OFF_KX) + (u32)(h * 8192 + (w & 1) * 4096);
  if (w < 2) { u32 r = probe(ws, cb, h, w, l); vote(p, w, r, l); if (l == 0) ctl[4 + w] = r; }
  else if (w >= 4) {
    seen0 = wait_prog(ws, OFF_PQK + (u32)h * 128, 1, 1, l, dead, 0);
#pragma unroll
    for (int i = 0; i < 4; ++i) *(u32x4*)(kq + (w - 4) * 4096 + i * 1024 + l * 16) = ld16c(ws, l * 16 + i * 1024, kq_src);
  }
  __syncthreads();
  u32 parmask = __builtin_amdgcn_readfirstlane(ctl[4] | (ctl[5] << 1));
  const int sidx = (l & 1) ? 0xEEEE : 0x4444;
  unsigned char* qk_w = qs + (w >> 2) * 8 * QS + 2 * g * QS + (32 * (w & 3) + 8 * (c16 >> 2) + (c16 & 3)) * 2;
  const unsigned char* km_r = kms + (2 * (c16 >> 2) + (c16 & 1)) * QS + g * 16;
  const int wr = (w + h) & 7;
  unsigned char* gA = memf + wr * 1024 + g * 256 + (l & 7) * 32 + ((l >> 3) & 1) * 16;
  unsigned char* gB = memf + 8192 + wr * 256 + ((l >> 2) & 7) * 32 + (l >> 5) * 16 + (l & 3) * 4;
  if (w == 0) {
    const unsigned char* q_r = qs + (c16 & 7) * QS + g * 16;
    const bool hb = c16 >= 8;
    const u32 xo = (u32)(((h >> 1) & 7) * 1024 + g * 256 + (h & 1) * 128 + (c16 & 7) * 16 + (c16 >> 3) * 8);
    const u32 mo = (u32)((c16 >> 3) * 8192 + h * 128 + (c16 & 7) * 16 + g * 4);
    for (int s = 0; s < NSEG; ++s) {
      const u32 sb = cb + (u32)s * STEP_BYTES;
      if (s == 1) parmask = vote_result(ws, l);
      const u32 par = ((parmask >> (s >> 5)) & 1) * 8192;
      const unsigned char* kxs = kq + (s & 1) * 16384;
      __syncthreads();
      { u32 sp = gather_mem(ws, sb + par, wr, gA, gB, l, dead); if (sp && l == 0) ctl[2 + (s & 1)] = (u32)s; }
      __syncthreads();
      { u32x4 a[10]; read_memf(memf, l, a); SCHED_FENCE(); project2(a, wt, sidx, qk_w); }
      u32x4 kx[8];
#pragma unroll
      for (int i = 0; i < 8; ++i) kx[i] = *(const u32x4*)(kxs + i * 1024 + l * 16);
      __syncthreads();
      if (s == NSEG - 1) break;
      u32x4 qf[4], km[4];
#pragma unroll
      for (int ks = 0; ks < 4; ++ks) { qf[ks] = *(const u32x4*)(q_r + ks * 64); km[ks] = *(const u32x4*)(km_r + ks * 64); }
      SCHED_FENCE();
      f32x4 s0 = {0, 0, 0, 0}, s1 = {0, 0, 0, 0}, s2 = {0, 0, 0, 0};
#pragma unroll
      for (int ks = 0; ks < 4; ++ks) {
        s1 = mfma(kx[ks], qf[ks], s1);
        s2 = mfma(kx[4 + ks], qf[ks], s2);
        s0 = mfma(km[ks], qf[ks], s0);
      }
      float v[4];
#pragma unroll
      for (int r = 0; r < 4; ++r) v[r] = hb ? s2[r] : s1[r];
      float m = fmx(fmx(fmx(v[0], v[1]), v[2]), fmx(fmx(v[3], s0[0]), s0[1]));
      m = red_max(m);
      const float mc = -m * CEXP;
#pragma unroll
      for (int r = 0; r < 4; ++r) v[r] = __builtin_amdgcn_exp2f(fmaf(v[r], CEXP, mc));
      const float e0 = __builtin_amdgcn_exp2f(fmaf(s0[0], CEXP, mc)), e1 = __builtin_amdgcn_exp2f(fmaf(s0[1], CEXP, mc));
      float sum = ((v[0] + v[1]) + (v[2] + v[3])) + (hb ? e1 : e0);
      sum = red_sum(sum);
      const float inv = __builtin_amdgcn_rcpf(sum);
      u32x2 px = {pk2s(v[0] * inv, v[1] * inv), pk2s(v[2] * inv, v[3] * inv)};
      const u32 pm = pk2s(e0 * inv, e1 * inv);
      const u32 xs = sb + (u32)(2 + (h >> 4)) * 16384;
      st8c(ws, xo, xs + (par ^ 8192), px);
      st4c(ws, mo, sb + 16384 + 2048, pm);
      st8c(ws, xo, xs + par, px);
      const u32 tnow = fine_now();
      const u32 rm = __builtin_amdgcn_readfirstlane(ctl[s & 1]), bad = __builtin_amdgcn_readfirstlane(ctl[2 + (s & 1)]);
      const u32 d1 = (s >= 2 && bad != (u32)s) ? tnow - rm : 0u;
      st4c(ws, l < 32 ? (u32)l * 4 : 0x80000000u, OFF_SIDE + (u32)s * SIDE_STEP + (u32)(80 + h) * 128, d1 < 1000u ? d1 : 0u);
    }
  } else if (w < 3) {
    const int tx = 16 * (w - 1) + c16;
    for (int s = 0; s < NSEG; ++s) {
      const unsigned char* kxs = kq + (s & 1) * 16384;
      const u32 sb = cb + (u32)s * STEP_BYTES;
      if (s == 1) parmask = vote_result(ws, l);
      const u32 par = ((parmask >> (s >> 5)) & 1) * 8192;
      __syncthreads();
      { u32 sp = gather_mem(ws, sb + par, wr, gA, gB, l, dead); if (sp && l == 0) ctl[2 + (s & 1)] = (u32)s; }
      __syncthreads();
      { u32x4 a[10]; read_memf(memf, l, a); SCHED_FENCE(); project2(a, wt, sidx, qk_w); }
      __syncthreads();
      if (w == 1) st4c(ws, l == 0 ? 0u : 0x80000000u, OFF_PPX + (u32)h * 128, (u32)s);
      f32x4 s0 = {0, 0, 0, 0}, s1 = {0, 0, 0, 0}, s2 = {0, 0, 0, 0};
#pragma unroll
      for (int ks = 0; ks < 4; ++ks) {
        u32x4 qf = *(const u32x4*)(kxs + 8192 + ((w - 1) * 4 + ks) * 1024 + l * 16);
        s0 = mfma(*(const u32x4*)(km_r + ks * 64), qf, s0);
        s1 = mfma(*(const u32x4*)(kxs + ks * 1024 + l * 16), qf, s1);
        s2 = mfma(*(const u32x4*)(kxs + (4 + ks) * 1024 + l * 16), qf, s2);
      }
      float m = fmx(s0[0], s0[1]);
#pragma unroll
      for (int r = 0; r < 4; ++r) {
        s1[r] = (4 * g + r <= tx) ? s1[r] : NEGS;
        s2[r] = (16 + 4 * g + r <= tx) ? s2[r] : NEGS;
        m = fmx(m, fmx(s1[r], s2[r]));
      }
      m = red_max_g(m);
      const float mc = -m * CEXP;
      const float e0 = __builtin_amdgcn_exp2f(fmaf(s0[0], CEXP, mc)), e1 = __builtin_amdgcn_exp2f(fmaf(s0[1], CEXP, mc));
      float sum = e0 + e1;
#pragma unroll
      for (int r = 0; r < 4; ++r) {
        s1[r] = __builtin_amdgcn_exp2f(fmaf(s1[r], CEXP, mc));
        s2[r] = __builtin_amdgcn_exp2f(fmaf(s2[r], CEXP, mc));
        sum += s1[r] + s2[r];
      }
      sum = red_sum_g(sum);
      const float inv = __builtin_amdgcn_rcpf(sum);
      u32x4 px = {pk2(s1[0] * inv, s1[1] * inv), pk2(s1[2] * inv, s1[3] * inv), pk2(s2[0] * inv, s2[1] * inv), pk2(s2[2] * inv, s2[3] * inv)};
      st16c(ws, l * 16, OFF_PXX + (u32)(((s * 32 + h) * 2 + (w - 1)) * 1024), px);
      st4c(ws, c16 * 16 + g * 4, OFF_PXM + (u32)((((s * 2 + (w - 1)) * 32 + h) * 16) * 16), pk2(e0 * inv, e1 * inv));
      VM_DRAIN();
    }
  } else if (w == 3) {
    u32 Rm = 0, Rp;
    for (int s = 0; s < NSEG; ++s) {
      const u32 sb = cb + (u32)s * STEP_BYTES;
      if (s == 1) parmask = vote_result(ws, l);
      const u32 par = ((parmask >> (s >> 5)) & 1) * 8192;
      if (s >= 2) {
        if (l == 0) { ctl[s & 1] = Rm; if ((int)(Rm - rtime()) < 28) ctl[2 + (s & 1)] = (u32)s; }
        wait_until(Rm);
      }
      __syncthreads();
      { u32 sp = gather_mem(ws, sb + par, wr, gA, gB, l, dead); if (sp && l == 0) ctl[2 + (s & 1)] = (u32)s; }
      __syncthreads();
      u32x4 a[10];
      read_memf(memf, l, a);
      Side sd = load_side(ws, s, l);
      SCHED_FENCE();
      project2(a, wt, sidx, qk_w);
      __syncthreads();
      if (s >= 1 && s < NSEG - 1) schedule(ws, s, l, sd, Rm, Rp, dead);
    }
  } else {
    for (int s = 0; s < NSEG; ++s) {
      const u32 sb = cb + (u32)s * STEP_BYTES;
      if (s == 1) parmask = vote_result(ws, l);
      const u32 par = ((parmask >> (s >> 5)) & 1) * 8192;
      const int sn = s + 1 < NSEG ? s + 1 : s;
      __syncthreads();
      { u32 sp = gather_mem(ws, sb + par, wr, gA, gB, l, dead); if (sp && l == 0) ctl[2 + (s & 1)] = (u32)s; }
      __syncthreads();
      u32x4 a[10];
      read_memf(memf, l, a);
      SCHED_FENCE();
      u32x4 pk[4];
      f32x4 c0 = {0, 0, 0, 0}, c1 = {0, 0, 0, 0};
#pragma unroll
      for (int ks = 0; ks < 10; ++ks) {
        c0 = smfma(a[ks], wt[0][ks], c0, sidx);
        c1 = smfma(a[ks], wt[1][ks], c1, sidx);
        if (ks == KQPOS) {
          SCHED_FENCE();
          if (s == 0 && seen0 < 2) continue;
#pragma unroll
          for (int i = 0; i < 4; ++i) pk[i] = ld16c(ws, l * 16 + i * 1024, kq_src + (u32)sn * (32 * 8192));
          SCHED_FENCE();
        }
      }
      *(__bf16*)(qk_w) = (__bf16)(c0[0] + c0[1]);
      *(__bf16*)(qk_w + QS) = (__bf16)(c0[2] + c0[3]);
      *(__bf16*)(qk_w + 8) = (__bf16)(c1[0] + c1[1]);
      *(__bf16*)(qk_w + 8 + QS) = (__bf16)(c1[2] + c1[3]);
      __syncthreads();
      if (s == 0 && seen0 < 2) {
        seen0 = wait_prog(ws, OFF_PQK + (u32)h * 128, 1, 2, l, dead, 0);
#pragma unroll
        for (int i = 0; i < 4; ++i) pk[i] = ld16c(ws, l * 16 + i * 1024, kq_src + (u32)sn * (32 * 8192));
      }
      unsigned char* kn = kq + ((s + 1) & 1) * 16384 + (w - 4) * 4096 + l * 16;
#pragma unroll
      for (int i = 0; i < 4; ++i) *(u32x4*)(kn + i * 1024) = pk[i];
      const int s2 = s + 2;
      if (s2 < NSEG) {
        if (seen0 < (u32)s2 + 1) { __builtin_amdgcn_s_sleep(12); seen0 = wait_prog(ws, OFF_PQK + (u32)h * 128, 1, (u32)s2 + 1, l, dead, 0); }
      }
    }
  }
  __syncthreads();
  if (tid == 0) st4c(ws, 0u, OFF_PPX + (u32)h * 128, (u32)NSEG);
}

DEV u32x4 finish_mem(f32x4 acc) {
  auto x = __builtin_amdgcn_permlane32_swap(__float_as_uint(acc[0]), __float_as_uint(acc[2]), false, false);
  auto y = __builtin_amdgcn_permlane32_swap(__float_as_uint(acc[1]), __float_as_uint(acc[3]), false, false);
  float t0, t1;
  asm("s_nop 1\n\tv_add_f32_dpp %0, %2, %3 row_ror:8 row_mask:0xf bank_mask:0xf\n\tv_add_f32_dpp %1, %4, %5 row_ror:8 row_mask:0xf bank_mask:0xf"
      : "=&v"(t0), "=&v"(t1) : "v"(x[1]), "v"(x[0]), "v"(y[1]), "v"(y[0]));
  const u32 d = pk2s(t0, t1);
  auto s16 = __builtin_amdgcn_permlane16_swap(d, d, false, false);
  auto sa = __builtin_amdgcn_permlane32_swap(s16[0], s16[0], false, false);
  auto sb = __builtin_amdgcn_permlane32_swap(s16[1], s16[1], false, false);
  return (u32x4){sa[0], sa[1], sb[0], sb[1]};
}

DEV u32 second4(u32 a, u32 b, u32 c, u32 d) {
  const u32 h0 = max(a, b), l0 = min(a, b), h1 = max(c, d), l1 = min(c, d);
  return max(min(h0, h1), h0 > h1 ? l0 : l1);
}
DEV void value_role(const Params& p, rsrc_t ws, u32 cb, int v, unsigned char* lds) {
  const int tid = threadIdx.x, l = tid & 63, c16 = l & 15, g = l >> 4;
  const int w = __builtin_amdgcn_readfirstlane(tid >> 6);
  unsigned char* memf = lds;
  unsigned char* um = lds + 10240;
  u32* ctl = (u32*)(lds + 15360);
  unsigned char* w0s = lds + 16384;
  unsigned char* uxs = lds + 57344;
  const int sidx = (l & 1) ? 0xEEEE : 0x4444;
  const u32 um_w = (u32)((4 * w + (c16 >> 3)) * 128 + (c16 & 7) * 16 + g * 4);
  const int wr = (w + v) & 7;
  unsigned char* gA = memf + wr * 1024 + g * 256 + (l & 7) * 32 + ((l >> 3) & 1) * 16;
  unsigned char* gB = memf + 8192 + wr * 256 + ((l >> 2) & 7) * 32 + (l >> 5) * 16 + (l & 3) * 4;
  bool dead = false;
  b_unit(p, ws, v, lds);
  if (w == 2 || w == 3) { u32 r = probe(ws, cb, 32 + v, w - 2, l); vote(p, w - 2, r, l); if (l == 0) ctl[2 + w] = r; }
  if (w == 0) wait_prog(ws, OFF_BC + (u32)(v / 40) * 128, 1, 64, l, dead, 0);
  if (w == 1) { wait_prog(ws, OFF_INITC, 8, 32, l, dead, 0, 64); wait_prog(ws, OFF_XFC, 1, 16, l, dead, 0); }
  __syncthreads();
  if (w == 0) {
#pragma unroll
    for (int i = 0; i < 40; ++i) *(u32x4*)(w0s + i * 1024 + l * 16) = ld16c(ws, l * 16, OFF_BF + (u32)((v * 16 * 20 + i) * 1024));
    __syncthreads();
    __syncthreads();
    u32 parmask = __builtin_amdgcn_readfirstlane(ctl[4] | (ctl[5] << 1));
    const u32 offX = (u32)((c16 >> 3) * 512 + g * 128 + (c16 & 7) * 16);
    const u32 L = (u32)(v * 128 + (c16 & 7) * 16);
    const u32 pub = (c16 < 8 && !(g & 1)) ? (L >> 13) * 16384 + (u32)(g >> 1) * 8192 + (L & 8191) : 0x80000000u;
    u32 Rg = 0, d3 = 0, Rm = 0, dcur = v == 0 ? ((D1 + D2) << 10) | ((D3 + D4) << 20) : 0u;
    u32 a0 = D1, a1 = D1, a2 = D1, a3 = D1, b0 = D3, b1 = D3, b2 = D3, b3 = D3;
    const bool sidel = (g == 1 && c16 < 8);
    for (int s = 0; s < NSEG; ++s) {
      const u32 sb = cb + (u32)s * STEP_BYTES;
      if (s == 1) parmask = vote_result(ws, l);
      const u32 par = ((parmask >> (s >> 5)) & 1) * 8192;
      if (s >= 1) wait_until(Rg);
      __syncthreads();
      const u32x4 ga = ld16c(ws, (u32)l * 16, sb + par + (u32)wr * 1024);
      const u32 gb = ld4c(ws, (u32)l * 4, sb + par + 16384 + (u32)wr * 256);
      SCHED_FENCE();
      __builtin_amdgcn_s_sleep(1);
      Side sd = load_side(ws, s, l);
      u32 d1s = 0;
      if (v == 0 && s >= 1) d1s = ld4c(ws, (u32)(80 + (l & 31)) * 128, OFF_SIDE + (u32)(s - 1) * SIDE_STEP);
      u32x4 uf[20];
#pragma unroll
      for (int i = 0; i < 16; ++i) uf[i] = *(const u32x4*)(uxs + (((i >> 2) + PROT) & 3) * 4096 + (i & 3) * 1024 + offX);
      SCHED_FENCE();
      *(u32x4*)gA = ga;
      *(u32*)gB = gb;
      if (__builtin_expect(__any(umax3(umax3(ga.x, ga.y, ga.z), ga.w, gb) == SENT), 0)) gather_mem(ws, sb + par, wr, gA, gB, l, dead);
      __syncthreads();
      if (s == NSEG - 1) {
        __syncthreads();
        __syncthreads();
        break;
      }
      u32 so[5];
#pragma unroll
      for (int i = 0; i < 4; ++i) { const u32 qd = (u32)(i + PROT) & 3; so[i] = sb + par + (2 + (qd >> 1)) * 16384 + (qd & 1) * 4096; }
      so[4] = sb + par + 16384 + 2048;
      asm volatile("" : "+s"(so[0]), "+s"(so[1]), "+s"(so[2]), "+s"(so[3]), "+s"(so[4]));
      const u32 pubo = sidel ? OFF_SIDE + (u32)(s + 1) * SIDE_STEP + (u32)(v * 128 + c16 * 16) : pub + sb + STEP_BYTES;
      u32 Rp = 0;
      if (s >= 1) schedule(ws, s, l, sd, Rm, Rp, dead);
      const u32 sy = d3 | dcur;
      const int lead = (int)(Rp - rtime());
      bool ontime = s >= 1 && lead > 0, pfail = false;
      SCHED_FENCE();
      if (s >= 1) wait_until(Rp);
      SCHED_FENCE();
      u32x4 pf[20];
#pragma unroll
      for (int i = 0; i < 20; ++i) { pf[i] = ld16c(ws, (u32)l * 16 + (i & 3) * 1024, so[i >> 2]); SCHED_FENCE(); }
      f32x4 acc = {0, 0, 0, 0};
#pragma unroll
      for (int i = 0; i < 20; ++i) {
        if (i == UMPOS) {
          __syncthreads();
#pragma unroll
          for (int q = 0; q < 4; ++q) uf[16 + q] = *(const u32x4*)(um + q * 1024 + l * 16);
          SCHED_FENCE();
        }
        acc = mfma(uf[i], pf[i], acc);
        SCHED_FENCE();
      }
      const u32 tl = rtime();
      u32x4 o = finish_mem(acc);
      if (__builtin_expect(__any(acc[0] != acc[0]), 0)) {
        u32 spins = 0;
#pragma nounroll
        while (!dead) {
          u32 m = 0;
#pragma unroll
          for (int i = 0; i < 20; ++i) m = umax3(umax3(m, pf[i].x, pf[i].y), pf[i].z, pf[i].w);
          if (__all(m != SENT)) break;
          ontime = false; pfail = true;
          if (++spins > SPIN_CAP) dead = true;
          asm volatile("" ::: "memory");
#pragma unroll
          for (int i = 0; i < 20; ++i) pf[i] = ld16c(ws, (u32)l * 16 + (i & 3) * 1024, so[i >> 2]);
        }
        acc = (f32x4){0, 0, 0, 0};
#pragma unroll
        for (int i = 0; i < 20; ++i) acc = mfma(uf[i], pf[i], acc);
        o = finish_mem(acc);
      }
      u32 sx = 0;
      if ((int)(tl + DLS - Rm) > 0) sx = (tl + DL + 4) | 1u;
      if (__builtin_expect(!ontime, 0)) sx = (rtime() + DL) | 1u;
      if (sx == SENT) sx = 1u;
      o.x = sidel ? sx : o.x;
      o.y = sidel ? sy : o.y;
      st16c(ws, pubo, 0u, o);
      const u32 tp = fine_now();
      __syncthreads();
      d3 = (ontime && lead >= 28) ? tp - Rp : 0u;
      d3 = d3 < 1000u ? d3 : 0u;
      Rg = sx == 0 ? Rm + DV : tp + D4 + DV;
      if (v == 0) {
        const u32 ma = (u32)wave_max(d1s == SENT ? 0 : (int)d1s), mb = s >= 1 ? (u32)wave_max((int)max(sd.a.y & 1023u, sd.b.y & 1023u)) : 0u;
        if (ma) { a3 = a2; a2 = a1; a1 = a0; a0 = ma; }
        if (pfail && s >= 2) { a0 = a1 = min(second4(a0, a1, a2, a3) + 8, 300u); }
        if (mb) { b3 = b2; b2 = b1; b1 = b0; b0 = mb; }
        dcur = (min(second4(a0, a1, a2, a3) + D2, 1000u) << 10) | (min(second4(b0, b1, b2, b3) + D4, 1000u) << 20);
      }
    }
  } else {
    u32x8 wb[2][10];
#pragma unroll
    for (int t = 0; t < 2; ++t)
#pragma unroll
      for (int ks = 0; ks < 10; ++ks) {
        u32 o = OFF_BF + (u32)(((v * 16 + 2 * w + t) * 20 + 2 * ks) * 1024);
        wb[t][ks] = cat(ld16c(ws, l * 16, o), ld16c(ws, l * 16 + 1024, o));
      }
    __builtin_amdgcn_s_waitcnt(0x0F70);
    __syncthreads();
    const bool pfw = w == 3 || w >= 5;
    const u32 uxo = (u32)((w == 3 ? 0 : w - 4) * 4096 + l * 16);
    u32 seen0 = 0, seen1 = 0;
    if (pfw) {
      seen0 = wait_prog(ws, OFF_PVU, 32, 2, l, dead, 0);
#pragma unroll
      for (int i = 0; i < 4; ++i) *(u32x4*)(uxs + uxo + i * 1024) = ld16c(ws, uxo + i * 1024, OFF_UX + (u32)(v * 16384));
    }
    __syncthreads();
    u32 parmask = __builtin_amdgcn_readfirstlane(ctl[4] | (ctl[5] << 1));
    if (w == 4) {
      for (int s = 0; s < NSEG; ++s) {
        const u32 sb = cb + (u32)s * STEP_BYTES;
        if (s == 1) parmask = vote_result(ws, l);
      const u32 par = ((parmask >> (s >> 5)) & 1) * 8192;
        u32x8 sb0[2], sb1[2];
        sb0[0] = cat(*(const u32x4*)(w0s + l * 16), *(const u32x4*)(w0s + 1024 + l * 16));
        sb1[0] = cat(*(const u32x4*)(w0s + 20480 + l * 16), *(const u32x4*)(w0s + 21504 + l * 16));
        __syncthreads();
        gather_mem(ws, sb + par, wr, gA, gB, l, dead);
        __syncthreads();
        u32x4 a[3];
        a[0] = *(const u32x4*)(memf + l * 16);
        a[1] = *(const u32x4*)(memf + 1024 + l * 16);
        f32x4 a0 = {0, 0, 0, 0}, a1 = {0, 0, 0, 0}, a2 = {0, 0, 0, 0}, a3 = {0, 0, 0, 0};
#pragma unroll
        for (int ks = 0; ks < 10; ++ks) {
          if (ks + 2 < 10) a[(ks + 2) % 3] = *(const u32x4*)(memf + (ks + 2) * 1024 + l * 16);
          if (ks + 1 < 10) {
            const int kn = ks + 1;
            sb0[kn & 1] = cat(*(const u32x4*)(w0s + (2 * kn) * 1024 + l * 16), *(const u32x4*)(w0s + (2 * kn + 1) * 1024 + l * 16));
            sb1[kn & 1] = cat(*(const u32x4*)(w0s + (20 + 2 * kn) * 1024 + l * 16), *(const u32x4*)(w0s + (21 + 2 * kn) * 1024 + l * 16));
          }
          SCHED_FENCE();
          a0 = smfma(a[ks % 3], wb[0][ks], a0, sidx);
          a1 = smfma(a[ks % 3], wb[1][ks], a1, sidx);
          a2 = smfma(a[ks % 3], sb0[ks & 1], a2, sidx);
          a3 = smfma(a[ks % 3], sb1[ks & 1], a3, sidx);
          SCHED_FENCE();
        }
        *(u32*)(um + um_w) = pk2(a0[0] + a0[1], a0[2] + a0[3]);
        *(u32*)(um + um_w + 256) = pk2(a1[0] + a1[1], a1[2] + a1[3]);
        *(u32*)(um + um_w - 2048) = pk2(a2[0] + a2[1], a2[2] + a2[3]);
        *(u32*)(um + um_w - 2048 + 256) = pk2(a3[0] + a3[1], a3[2] + a3[3]);
        __syncthreads();
        __syncthreads();
      }
    } else {
      for (int s = 0; s < NSEG; ++s) {
        const u32 sb = cb + (u32)s * STEP_BYTES;
        if (s == 1) parmask = vote_result(ws, l);
      const u32 par = ((parmask >> (s >> 5)) & 1) * 8192;
        __syncthreads();
        gather_mem(ws, sb + par, wr, gA, gB, l, dead);
        __syncthreads();
        u32x4 a[10];
        read_memf(memf, l, a);
        u32x4 pu[4];
        if (pfw && s + 1 < NSEG) {
#pragma unroll
          for (int i = 0; i < 4; ++i) pu[i] = ld16c(ws, uxo + i * 1024, OFF_UX + (u32)(((s + 1) * 80 + v) * 16384));
        }
        SCHED_FENCE();
        f32x4 a0 = {0, 0, 0, 0}, a1 = {0, 0, 0, 0};
#pragma unroll
        for (int ks = 0; ks < 10; ++ks) {
          a0 = smfma(a[ks], wb[0][ks], a0, sidx);
          a1 = smfma(a[ks], wb[1][ks], a1, sidx);
        }
        *(u32*)(um + um_w) = pk2(a0[0] + a0[1], a0[2] + a0[3]);
        *(u32*)(um + um_w + 256) = pk2(a1[0] + a1[1], a1[2] + a1[3]);
        if (w == 1) st4c(ws, l == 0 ? 0u : 0x80000000u, OFF_PUM + (u32)v * 128, (u32)s);
        __syncthreads();
        __syncthreads();
        if (pfw && s + 1 < NSEG) {
#pragma unroll
          for (int i = 0; i < 4; ++i) *(u32x4*)(uxs + uxo + i * 1024) = pu[i];
          const int sn = s + 2;
          if (sn < NSEG) {
            if (seg_prod(sn)) { if (seen1 < seg_need(sn)) seen1 = wait_prog(ws, OFF_PVU + 128, 32, seg_need(sn), l, dead, 0); }
            else { if (seen0 < seg_need(sn)) seen0 = wait_prog(ws, OFF_PVU, 32, seg_need(sn), l, dead, 0); }
          }
        }
        if (w == 1 || w == 2) {
#pragma unroll
          for (int i = 0; i < 2; ++i) {
            u32 o = (u32)(((w - 1) * 2 + i) * 1024 + l * 16);
            st16c(ws, o, OFF_UM + (u32)((s * 80 + v) * 4096), *(const u32x4*)(um + o));
          }
          VM_DRAIN();
        }
      }
    }
  }
  __syncthreads();
  if (tid == 0) st4c(ws, 0u, OFF_PUM + (u32)v * 128, (u32)NSEG);
}

template <bool SC1> DEV void glds16(const void* gsrc, u32 lds_dst) {
  u32 keep;
  if (!SC1) {
    asm volatile("s_mov_b32 %0, m0\n\ts_mov_b32 m0, %2\n\ts_nop 0\n\tglobal_load_lds_dwordx4 %1, off\n\ts_mov_b32 m0, %0"
                 : "=&s"(keep) : "v"(gsrc), "s"(lds_dst) : "memory");
    return;
  }
  asm volatile("s_mov_b32 %0, m0\n\ts_mov_b32 m0, %2\n\ts_nop 0\n\tglobal_load_lds_dwordx4 %1, off sc1\n\ts_mov_b32 m0, %0"
               : "=&s"(keep) : "v"(gsrc), "s"(lds_dst) : "memory");
}
template <int NST, bool ROWS, class Off, class Body, class Pub>
DEV void stream_tiles(const unsigned char* src, Off off, int n, int nbody, unsigned char* lds, Body body, Pub pub) {
  const int l = threadIdx.x & 63, w = __builtin_amdgcn_readfirstlane(threadIdx.x >> 6);
  const u32 lbase = (u32)(uintptr_t)lds + (u32)w * 5120;
  constexpr int QS_ = ROWS ? 64 : 1024;
  const unsigned char* g0 = ROWS ? src + (16 * (w >> 2) + (l & 15)) * 1280 + (w & 3) * 320 + (l >> 4) * 16 : src + w * 5120 + l * 16;
#pragma unroll
  for (int q = 0; q < 5; ++q) glds16<!ROWS>(g0 + off(0) + q * QS_, lbase + q * 1024);
#pragma nounroll
  for (int i = 0; i < nbody; ++i) {
    if (i <= 3) VM_DRAIN(); else asm volatile("s_waitcnt vmcnt(%0)" :: "n"(NST) : "memory");
    RAW_BARRIER();
    if (i >= 1) pub(i <= 3 ? i : i - 1);
    const unsigned char* g = g0 + off(i + 1 < n ? i + 1 : n - 1);
    const u32 lb = lbase + (u32)((i + 1) & 1) * 40960;
    body(i, lds + (i & 1) * 40960, [&](int q) { glds16<!ROWS>(g + q * QS_, lb + q * 1024); });
  }
  VM_DRAIN();
  RAW_BARRIER();
  pub(nbody);
}
template <int W, class Dma, class Use>
DEV void sweep40(const unsigned char* t, Dma dma, Use use) {
  u32x4 xb[W];
#pragma unroll
  for (int i = 0; i < W; ++i) xb[i] = *(const u32x4*)(t + ((i & 1) * 20 + (i >> 1)) * 1024);
  SCHED_FENCE();
#pragma unroll
  for (int i = 0; i < 40; ++i) {
    use(i, xb[i % W]);
    if (i + W < 40) xb[i % W] = *(const u32x4*)(t + (((i + W) & 1) * 20 + ((i + W) >> 1)) * 1024);
    if (i % 2 == 1 && i < 10) dma(i >> 1);
    SCHED_FENCE();
  }
}

DEV void qk_role(const Params& p, rsrc_t ws, int h, int par, unsigned char* lds) {
  const int tid = threadIdx.x, l = tid & 63;
  const int w = __builtin_amdgcn_readfirstlane(tid >> 6);
  const int mat = w >> 2;
  u32x4 wt[2][20];
  load_qk(p, h, w, l, lds, wt);
  const u32 dst = (mat ? OFF_KX : OFF_QX) + (u32)((h * 8 + (w & 3)) * 1024);
  const int n = NSEG;
  wait_init(ws);
  stream_tiles<2, false>(p.ws + OFF_XF, [&](int i) { return i * 40960; }, n, n, lds,
    [&](int i, const unsigned char* t, auto dma) {
      f32x4 acc[2][2] = {};
      sweep40<8>(t + l * 16, dma, [&](int f, u32x4 xb) {
        acc[0][f & 1] = mfma(wt[0][f >> 1], xb, acc[0][f & 1]);
        acc[1][f & 1] = mfma(wt[1][f >> 1], xb, acc[1][f & 1]);
      });
#pragma unroll
      for (int tt = 0; tt < 2; ++tt) {
        u32x4 o = {pk2(acc[0][tt][0], acc[0][tt][1]), pk2(acc[0][tt][2], acc[0][tt][3]), pk2(acc[1][tt][0], acc[1][tt][1]), pk2(acc[1][tt][2], acc[1][tt][3])};
        st16c(ws, l * 16 + tt * 4096, dst + (u32)i * (32 * 8192), o);
      }
    },
    [&](int k) {
      if (w == 0) {
        if (k == 2) { bool dead = false; wait_prog(ws, OFF_XFC + 64, 1, 16, l, dead, 0); }
        st4c(ws, l == 0 ? 0u : 0x80000000u, OFF_PQK + (u32)(h * 128 + par * 4), (u32)k);
      }
    });
}

DEV void vu_role(const Params& p, rsrc_t ws, int h, int par, unsigned char* lds) {
  const int tid = threadIdx.x, l = tid & 63, c16 = l & 15, g = l >> 4;
  const int w = __builtin_amdgcn_readfirstlane(tid >> 6);
  u32x4 wv[20], wo[5][4];
  load_vu(p, h, w, l, lds, wv, wo);
  unsigned char* vs = lds + 81920;
  const u32 uoff = (u32)(((c16 >> 1) & 1) * 16384 + h * 512 + g * 128 + (2 * (c16 >> 2) + (c16 & 1)) * 16);
  const int n = par ? NHELP : NSEG - NHELP;
  wait_init(ws);
  stream_tiles<5, false>(p.ws + OFF_XF, [&](int i) { return tile_seg(par, i) * 40960; }, n, n + 1, lds,
    [&](int i, const unsigned char* t, auto dma) {
      u32x4 vf[2][4];
#pragma unroll
      for (int tt = 0; tt < 2; ++tt)
#pragma unroll
        for (int ks = 0; ks < 4; ++ks) vf[tt][ks] = *(const u32x4*)(vs + ((i + 1) & 1) * 8192 + ((tt * 4 + ks) * 64 + l) * 16);
      const u32 uo = i ? uoff + OFF_UX + (u32)((tile_seg(par, i - 1) * 80 + 10 * w) * 16384) : 0x80000000u;
      f32x4 av[2] = {}, cu[2] = {};
      u32x4 ou[2];
      sweep40<6>(t + l * 16, dma, [&](int f, u32x4 xb) {
        av[f & 1] = mfma(wv[f >> 1], xb, av[f & 1]);
        cu[f & 1] = mfma(vf[f & 1][(f >> 1) & 3], wo[f >> 3][(f >> 1) & 3], cu[f & 1]);
        if ((f & 7) == 7) {
          const int ct = f >> 3;
          u32x4 o = {pk2(cu[0][0], cu[0][1]), pk2(cu[0][2], cu[0][3]), pk2(cu[1][0], cu[1][1]), pk2(cu[1][2], cu[1][3])};
          cu[0] = (f32x4){0, 0, 0, 0}; cu[1] = (f32x4){0, 0, 0, 0};
          if (ct == 0) ou[0] = o;
          else { if (ct == 1) st16c(ws, uo, 0u, ou[0]); st16c(ws, uo, (u32)ct * 32768, o); }
        }
      });
#pragma unroll
      for (int tt = 0; tt < 2; ++tt) {
        u32x2 o = {pk2(av[tt][0], av[tt][1]), pk2(av[tt][2], av[tt][3])};
        *(u32x2*)(vs + (i & 1) * 8192 + ((tt * 4 + (w >> 1)) * 64 + l) * 16 + (w & 1) * 8) = o;
      }
    },
    [&](int k) {
      if (w == 0 && k >= 2) {
        st4c(ws, l == 0 ? 0u : 0x80000000u, OFF_PVU + (u32)(par * 128 + h * 4), (u32)(k - 1));
        if (k == 3) { bool dead = false; wait_prog(ws, OFF_XFC + 64, 1, 16, l, dead, 0); }
      }
    });
}

DEV f32x2 cmul(f32x2 a, f32x2 b) { return (f32x2){a.x * b.x - a.y * b.y, a.x * b.y + a.y * b.x}; }
DEV f32x2 cmulc(f32x2 a, f32x2 b) { return (f32x2){a.x * b.x + a.y * b.y, a.y * b.x - a.x * b.y}; }
template <bool INV> DEV f32x2 rot90(f32x2 a) { return INV ? (f32x2){-a.y, a.x} : (f32x2){a.y, -a.x}; }
template <bool INV> DEV void fft4(f32x2& a, f32x2& b, f32x2& c, f32x2& d) {
  f32x2 s0 = a + c, d0 = a - c, s1 = b + d, d1 = rot90<INV>(b - d);
  a = s0 + s1; b = d0 + d1; c = s0 - s1; d = d0 - d1;
}
template <bool INV> DEV f32x2 twc(f32x2 a, float c, float s) {
  return INV ? (f32x2){a.x * c - a.y * s, a.y * c + a.x * s} : (f32x2){a.x * c + a.y * s, a.y * c - a.x * s};
}
template <bool INV> DEV void fft16(f32x2 (&v)[16]) {
  constexpr float C1 = 0.92387953251128674f, S1 = 0.38268343236508977f, R = 0.70710678118654752f;
#pragma unroll
  for (int i = 0; i < 4; ++i) fft4<INV>(v[i], v[i + 4], v[i + 8], v[i + 12]);
  v[5] = twc<INV>(v[5], C1, S1);  v[9] = twc<INV>(v[9], R, R);     v[13] = twc<INV>(v[13], S1, C1);
  v[6] = twc<INV>(v[6], R, R);    v[10] = rot90<INV>(v[10]);       v[14] = twc<INV>(v[14], -R, R);
  v[7] = twc<INV>(v[7], S1, C1);  v[11] = twc<INV>(v[11], -R, R);  v[15] = twc<INV>(v[15], -C1, -S1);
#pragma unroll
  for (int k1 = 0; k1 < 4; ++k1) fft4<INV>(v[4 * k1], v[4 * k1 + 1], v[4 * k1 + 2], v[4 * k1 + 3]);
  f32x2 t;
#define SWP(a, b) t = v[a]; v[a] = v[b]; v[b] = t;
  SWP(1, 4) SWP(2, 8) SWP(3, 12) SWP(6, 9) SWP(7, 13) SWP(11, 14)
#undef SWP
}
template <bool INV> DEV void fft8(f32x2 (&v)[8]) {
  constexpr float R = 0.70710678118654752f;
  fft4<INV>(v[0], v[2], v[4], v[6]);
  fft4<INV>(v[1], v[3], v[5], v[7]);
  f32x2 o1 = twc<INV>(v[3], R, R), o2 = rot90<INV>(v[5]), o3 = twc<INV>(v[7], -R, R), o0 = v[1];
  f32x2 e0 = v[0], e1 = v[2], e2 = v[4], e3 = v[6];
  v[0] = e0 + o0; v[4] = e0 - o0; v[1] = e1 + o1; v[5] = e1 - o1; v[2] = e2 + o2; v[6] = e2 - o2; v[3] = e3 + o3; v[7] = e3 - o3;
}

DEV void fft_cols(const Params& p, rsrc_t ws, int q, unsigned char* lds) {
  const int tid = threadIdx.x, c = tid >> 7, t = tid & 127;
  f32x2* tw = (f32x2*)lds;
  f32x2* buf = (f32x2*)(lds + 16384) + c * (16 * 136);
  f32x2* fin = (f32x2*)(lds + 16384);
  __syncthreads();
  for (int m = tid; m < 2048; m += 512) { float sn, cs; sincospif((float)m * (1.0f / 1024.0f), &sn, &cs); tw[m] = (f32x2){cs, -sn}; }
  f32x2 keep[16];
  const int npass = (q == 0) ? 2 : 1;
  for (int pass = npass - 1; pass >= 0; --pass) {
    f32x2 v[16];
#pragma unroll
    for (int a = 0; a < 16; ++a)
      v[a] = __builtin_bit_cast(f32x2, ld8c(ws, (u32)(t * 16 + (c & 1) * 8 + (c >> 1) * 32768), OFF_FX + (u32)(2 * q * 32768 + a * 2048)));
    if (q == 0 && c == 0) {
#pragma unroll
      for (int a = 0; a < 16; ++a) v[a] = (f32x2){pass ? v[a].y : v[a].x, 0.f};
    }
    u32 wreg[16];
#pragma unroll
    for (int i = 0; i < 2; ++i)
#pragma unroll
      for (int kc = 0; kc < 8; ++kc) {
        const int pp = t + 128 * i, k = (pp >> 4) + 16 * (pp & 15) + 256 * kc;
        wreg[i * 8 + kc] = ld4c(ws, (u32)(k * 16 + c * 4), OFF_WT + (u32)(pass ? 80 : q) * 32768);
      }
    __syncthreads();
    fft16<false>(v);
#pragma unroll
    for (int ka = 0; ka < 16; ++ka) buf[ka * 136 + t] = ka ? cmul(v[ka], tw[t * ka]) : v[ka];
    __syncthreads();
    const int ka2 = t >> 3, c2 = t & 7;
#pragma unroll
    for (int b = 0; b < 16; ++b) v[b] = buf[ka2 * 136 + 8 * b + c2];
    fft16<false>(v);
#pragma unroll
    for (int kb = 0; kb < 16; ++kb) buf[ka2 * 136 + 8 * kb + c2] = kb ? cmul(v[kb], tw[16 * c2 * kb]) : v[kb];
    __syncthreads();
#pragma unroll
    for (int i = 0; i < 2; ++i) {
      const int pp = t + 128 * i;
      f32x2* e = buf + (pp >> 4) * 136 + 8 * (pp & 15);
      f32x2 u[8];
#pragma unroll
      for (int j = 0; j < 8; ++j) u[j] = e[j];
      fft8<false>(u);
#pragma unroll
      for (int kc = 0; kc < 8; ++kc) {
        const u32 wv = wreg[i * 8 + kc];
        const f32x2 wf = {__uint_as_float(wv << 16) * (1.0f / (2048.0f * 640.0f)), __uint_as_float(wv & 0xFFFF0000u) * (1.0f / (2048.0f * 640.0f))};
        u[kc] = cmul(u[kc], wf);
      }
      fft8<true>(u);
#pragma unroll
      for (int j = 0; j < 8; ++j) e[j] = u[j];
    }
    __syncthreads();
#pragma unroll
    for (int kb = 0; kb < 16; ++kb) { f32x2 x = buf[ka2 * 136 + 8 * kb + c2]; v[kb] = kb ? cmulc(x, tw[16 * c2 * kb]) : x; }
    fft16<true>(v);
#pragma unroll
    for (int b = 0; b < 16; ++b) buf[ka2 * 136 + 8 * b + c2] = v[b];
    __syncthreads();
#pragma unroll
    for (int ka = 0; ka < 16; ++ka) { f32x2 x = buf[ka * 136 + t]; v[ka] = ka ? cmulc(x, tw[t * ka]) : x; }
    fft16<true>(v);
    if (pass == 1) {
#pragma unroll
      for (int a = 0; a < 16; ++a) keep[a] = v[a];
    } else {
      if (q == 0 && c == 0) {
#pragma unroll
        for (int a = 0; a < 16; ++a) v[a].y = keep[a].x;
      }
      __syncthreads();
#pragma unroll
      for (int a = 0; a < 16; ++a) fin[(128 * a + t) * 4 + c] = v[a];
      __syncthreads();
    }
  }
#pragma unroll
  for (int i = 0; i < 4; ++i) {
    const int n = tid + 512 * i;
    const f32x4 x0 = *(const f32x4*)(fin + n * 4), x1 = *(const f32x4*)(fin + n * 4 + 2);
    u32x4 hi = {pk2(x0[0], x0[1]), pk2(x0[2], x0[3]), pk2(x1[0], x1[1]), pk2(x1[2], x1[3])};
    u32x4 lo = {pk2(x0[0] - __uint_as_float(hi[0] << 16), x0[1] - __uint_as_float(hi[0] & 0xFFFF0000u)),
                pk2(x0[2] - __uint_as_float(hi[1] << 16), x0[3] - __uint_as_float(hi[1] & 0xFFFF0000u)),
                pk2(x1[0] - __uint_as_float(hi[2] << 16), x1[1] - __uint_as_float(hi[2] & 0xFFFF0000u)),
                pk2(x1[2] - __uint_as_float(hi[3] << 16), x1[3] - __uint_as_float(hi[3] & 0xFFFF0000u))};
    const u32 o = (u32)((n >> 4) * 40960 + (q >> 2) * 1024 + ((q & 3) * 16 + (n & 15)) * 16);
    st16c(ws, o, OFF_FY, hi);
    st16c(ws, o + 20480, OFF_FY, lo);
  }
}

DEV void fw_task(const Params& p, rsrc_t ws, int k, unsigned char* lds) {
  float* tab = (float*)lds;
  for (int m = threadIdx.x; m < 640; m += 512) { float sn, cs; sincospif((float)m * (1.0f / 320.0f), &sn, &cs); tab[m] = cs; tab[640 + m] = -sn; }
  __syncthreads();
  for (int q = k * 5120 + threadIdx.x; q < (k + 1) * 5120; q += 512) {
    const int which = q / 51200, r = q % 51200, tile = r / 1280, ks = (r % 1280) >> 6, ln = r & 63;
    u32x4 hi, lo;
    float val[8];
#pragma unroll
    for (int j = 0; j < 8; ++j) {
      const int aa = 16 * tile + (ln & 15), bb = 32 * ks + 8 * (ln >> 4) + j;
      const int kp = which ? bb : aa, d = which ? aa : bb;
      const int kk = (kp == 1) ? 320 : (kp >> 1);
      float x = tab[(kk * d) % 640 + (((kp & 1) && kp != 1) ? 640 : 0)];
      if (which && kp > 1) x *= 2.0f;
      val[j] = x;
    }
#pragma unroll
    for (int j = 0; j < 4; ++j) { hi[j] = pk2(val[2 * j], val[2 * j + 1]); lo[j] = pk2lo(val[2 * j], val[2 * j + 1], hi[j]); }
    const u32 o = (which ? OFF_FW3 : OFF_FW1) + (u32)((tile * 40 + ks) * 1024 + ln * 16);
    st16c(ws, o, 0, hi);
    st16c(ws, o + 20480, 0, lo);
  }
  VM_DRAIN();
  __syncthreads();
  if (threadIdx.x == 0) __hip_atomic_fetch_add((u32*)(p.ws + OFF_FWC), 1u, __ATOMIC_RELAXED, __HIP_MEMORY_SCOPE_AGENT);
}
DEV void wt_task(const Params& p, rsrc_t ws, int k, unsigned char* lds) {
  const int tid = threadIdx.x;
  const rsrc_t rr = mk_rsrc(p.wr, 2048 * 642), ri = mk_rsrc(p.wi, 2048 * 642);
  {
    const rsrc_t ra = mk_rsrc(p.ax, 2048 * 1280);
    u32 t[3];
#pragma unroll
    for (int i = 0; i < 3; ++i) t[i] = __builtin_amdgcn_raw_buffer_load_b32(ra, (i * 512 + tid) < 1280 ? (u32)(i * 512 + tid) * 128 : 0x80000000u, (u32)k * 163840, 0);
#pragma unroll
    for (int i = 0; i < 3; ++i) asm volatile("" :: "v"(t[i]));
  }
  for (int blk = 0; blk < 2; ++blk) {
    const int row0 = k * 128 + blk * 64;
    __syncthreads();
#pragma unroll
    for (int i = 0; i < 6; ++i) {
      const int c = tid + 512 * i;
      if (c < 2568) {
        *(u32x4*)(lds + c * 16) = ld16(rr, (u32)c * 16, (u32)row0 * 642);
        *(u32x4*)(lds + 41088 + c * 16) = ld16(ri, (u32)c * 16, (u32)row0 * 642);
      }
    }
    __syncthreads();
    const int r = tid & 63;
    const u16* a = (const u16*)lds + r * 321;
    for (int q = tid >> 6; q < 81; q += 8) {
      u32x4 o;
#pragma unroll
      for (int j = 0; j < 4; ++j) { const int col = q < 80 ? 4 * q + j : 320; o[j] = (u32)a[col] | ((u32)a[20544 + col] << 16); }
      st16c(ws, (u32)(row0 + r) * 16, OFF_WT + (u32)q * 32768, o);
    }
  }
  VM_DRAIN();
  __syncthreads();
  if (tid == 0) __hip_atomic_fetch_add((u32*)(p.ws + OFF_FWC), 1u, __ATOMIC_RELAXED, __HIP_MEMORY_SCOPE_AGENT);
}
DEV void wait_fw(rsrc_t ws) {
  if (threadIdx.x < 64) { bool dead = false; wait_prog(ws, OFF_FWC, 1, 36, threadIdx.x, dead, 1); }
  __syncthreads();
}
DEV void f1_task(const Params& p, rsrc_t ws, int type, int rg, unsigned char* lds) {
  const int tid = threadIdx.x, l = tid & 63, c16 = l & 15, g = l >> 4;
  const int w = __builtin_amdgcn_readfirstlane(tid >> 6);
  const int tile = type * 8 + w;
  wait_fw(ws);
  u32x4 wt[2][20];
#pragma unroll
  for (int i = 0; i < 2; ++i)
#pragma unroll
    for (int ks = 0; ks < 20; ++ks) { wt[i][ks] = ld16c(ws, l * 16, OFF_FW1 + (u32)((tile * 40 + i * 20 + ks) * 1024)); THROTTLE(ks); }
  __builtin_amdgcn_s_waitcnt(0x0F70);
  stream_tiles<2, true>((const unsigned char*)p.ax + rg * 8 * 40960, [](int i) { return i * 40960; }, 8, 8, lds,
    [&](int i, const unsigned char* t, auto dma) {
      f32x4 acc[2][2] = {};
      sweep40<8>(t + l * 16, dma, [&](int f, u32x4 xb) {
        acc[0][f & 1] = mfma(wt[0][f >> 1], xb, acc[0][f & 1]);
        acc[1][f & 1] = mfma(wt[1][f >> 1], xb, acc[1][f & 1]);
      });
#pragma unroll
      for (int tt = 0; tt < 2; ++tt) {
        f32x4 o = acc[0][tt] + acc[1][tt];
        st16c(ws, (u32)(g * 32768 + c16 * 16 + tt * 256), OFF_FX + (u32)(tile * 4 * 32768 + (rg * 8 + i) * 512), __builtin_bit_cast(u32x4, o));
      }
    },
    [&](int k) { if (k == 8 && tid == 0) __hip_atomic_fetch_add((u32*)(p.ws + OFF_F1C + type * 128), 1u, __ATOMIC_RELAXED, __HIP_MEMORY_SCOPE_AGENT); });
}
DEV void f3_task(const Params& p, rsrc_t ws, int type, int rg, unsigned char* lds) {
  const int tid = threadIdx.x, l = tid & 63, c16 = l & 15, g = l >> 4;
  const int w = __builtin_amdgcn_readfirstlane(tid >> 6);
  const int tile = type * 8 + w;
  wait_fw(ws);
  u32x4 wt[2][20];
#pragma unroll
  for (int i = 0; i < 2; ++i)
#pragma unroll
    for (int ks = 0; ks < 20; ++ks) { wt[i][ks] = ld16c(ws, l * 16, OFF_FW3 + (u32)((tile * 40 + i * 20 + ks) * 1024)); THROTTLE(ks); }
  __builtin_amdgcn_s_waitcnt(0x0F70);
  if (w == 0) {
    bool dead = false;
    wait_prog(ws, OFF_F2C, 1, 80, l, dead, 1);
    __builtin_amdgcn_fence(__ATOMIC_ACQUIRE, "agent");
    VM_DRAIN();
  }
  __syncthreads();
  u16* outp = p.out + (rg * 256 + c16) * 1280 + tile * 16 + 4 * g;
  stream_tiles<1, false>(p.ws + OFF_FY + rg * 16 * 40960, [](int i) { return i * 40960; }, 16, 16, lds,
    [&](int i, const unsigned char* t, auto dma) {
      f32x4 a0 = {0, 0, 0, 0}, a1 = {0, 0, 0, 0}, a2 = {0, 0, 0, 0};
      sweep40<10>(t + l * 16, dma, [&](int f, u32x4 y) {
        if (f & 1) a2 = mfma(wt[0][f >> 1], y, a2);
        else { a0 = mfma(wt[0][f >> 1], y, a0); a1 = mfma(wt[1][f >> 1], y, a1); }
      });
      f32x4 o = a0 + (a1 + a2);
      u32x2 ov = {pk2(o[0], o[1]), pk2(o[2], o[3])};
      *(u32x2*)(outp + i * 16 * 1280) = ov;
    },
    [&](int) {});
}

DEV void out_task(const Params& p, rsrc_t ws, int s, int cg, unsigned char* lds) {
  const int tid = threadIdx.x, l = tid & 63, c16 = l & 15, g = l >> 4;
  const int w = __builtin_amdgcn_readfirstlane(tid >> 6);
  const int G = 8 * cg + w;
  const int pp = (c16 >> 1) & 1, j = 2 * (c16 >> 2) + (c16 & 1);
  const u32 ox = (u32)(pp * 16384 + g * 128 + j * 16), om = (u32)(pp * 4096 + g * 128 + j * 16);
  bool dead = false;
  if (w == 0) wait_prog(ws, OFF_PVU + (u32)seg_prod(s) * 128, 32, seg_need(s), l, dead, 1);
  __syncthreads();
  u32x4 ua[32];
#pragma unroll
  for (int h = 0; h < 32; ++h) { ua[h] = ld16c(ws, ox + (h & 7) * 512, OFF_UX + (u32)((s * 80 + 2 * G) * 16384 + (h >> 3) * 4096)); THROTTLE(h); }
  SCHED_FENCE();
  if (w < 2) {
    wait_prog(ws, OFF_PPX + (u32)(cg + 5 * (s & 3)) * 128, 1, (u32)s, l, dead, 2);
    if (w == 0) wait_prog(ws, OFF_PPX, 32, (u32)s + 1, l, dead, 1, 128);
    else wait_prog(ws, OFF_PUM + (u32)cg * 2048, 16, (u32)s + 1, l, dead, 1, 128);
  }
  __syncthreads();
  u32x4 pr[10], ub[8];
#pragma unroll
  for (int i = 0; i < 8; ++i) pr[i] = ld16c(ws, tid * 16, OFF_PXX + (u32)(s * 65536 + i * 8192));
#pragma unroll
  for (int i = 0; i < 2; ++i) pr[8 + i] = ld16c(ws, tid * 16, OFF_PXM + (u32)(s * 16384 + i * 8192));
#pragma unroll
  for (int q = 0; q < 8; ++q) ub[q] = ld16c(ws, om + q * 512, OFF_UM + (u32)((s * 80 + 2 * G) * 4096));
#pragma unroll
  for (int i = 0; i < 10; ++i) *(u32x4*)(lds + (i * 512 + tid) * 16) = pr[i];
  __syncthreads();
  f32x4 acc[2] = {};
  {
    const unsigned char* px = lds + l * 16;
    const unsigned char* pm = lds + 65536 + (g * 16 + c16) * 16;
    auto rd = [&](int i) { return i < 64 ? *(const u32x4*)(px + i * 1024) : *(const u32x4*)(pm + (i & 1) * 8192 + ((i - 64) >> 1) * 1024); };
    u32x4 pb[12];
#pragma unroll
    for (int i = 0; i < 12; ++i) pb[i] = rd(i);
    SCHED_FENCE();
#pragma unroll
    for (int i = 0; i < 80; ++i) {
      acc[i & 1] = mfma(i < 64 ? ua[i >> 1] : ub[(i - 64) >> 1], pb[i % 12], acc[i & 1]);
      if (i + 12 < 80) pb[i % 12] = rd(i + 12);
      SCHED_FENCE();
    }
  }
#pragma unroll
  for (int tt = 0; tt < 2; ++tt) {
    u32x2 o = {pk2(acc[tt][0], acc[tt][1]), pk2(acc[tt][2], acc[tt][3])};
    *(u32x2*)(p.out + (32 * s + 16 * tt + c16) * 1280 + 640 + 16 * G + 4 * g) = o;
  }
}

DEV void out_fast(const Params& p, rsrc_t ws, int s, int G, unsigned char* lds) {
  const int tid = threadIdx.x, l = tid & 63, c16 = l & 15, g = l >> 4;
  const int w = __builtin_amdgcn_readfirstlane(tid >> 6);
  const int pp = (c16 >> 1) & 1, j = 2 * (c16 >> 2) + (c16 & 1);
  const u32 ox = (u32)(pp * 16384 + g * 128 + j * 16), om = (u32)(pp * 4096 + g * 128 + j * 16);
  bool dead = false;
  if (w == 0) wait_prog(ws, OFF_PVU + (u32)seg_prod(s) * 128, 32, seg_need(s), l, dead, 1);
  __syncthreads();
  u32x4 ua[4];
#pragma unroll
  for (int h = 0; h < 4; ++h) ua[h] = ld16c(ws, ox + h * 512, OFF_UX + (u32)((s * 80 + 2 * G) * 16384 + w * 2048));
  SCHED_FENCE();
  wait_prog(ws, OFF_PPX + (u32)(G & 31) * 128, 1, (u32)s, l, dead, 2);
  wait_prog(ws, OFF_PUM + (u32)G * 256, 2, (u32)s + 1, l, dead, 0, 128);
  const u32x4 ub = ld16c(ws, om, OFF_UM + (u32)((s * 80 + 2 * G) * 4096 + w * 512));
  wait_prog(ws, OFF_PPX + (u32)w * 512, 4, (u32)s + 1, l, dead, 0, 128);
  u32x4 pf[10];
#pragma unroll
  for (int i = 0; i < 8; ++i) pf[i] = ld16c(ws, l * 16 + (i & 3) * 1024, OFF_PXX + (u32)(s * 65536 + w * 8192 + (i >> 2) * 4096));
#pragma unroll
  for (int tt = 0; tt < 2; ++tt) pf[8 + tt] = ld16c(ws, l * 16, OFF_PXM + (u32)(s * 16384 + tt * 8192 + w * 1024));
  f32x4 acc[2] = {};
#pragma unroll
  for (int i = 0; i < 10; ++i) acc[i & 1] = mfma(i < 8 ? ua[i >> 1] : ub, pf[i], acc[i & 1]);
  *(f32x4*)(lds + (w * 2) * 1024 + l * 16) = acc[0];
  *(f32x4*)(lds + (w * 2 + 1) * 1024 + l * 16) = acc[1];
  __syncthreads();
  if (w < 2) {
    f32x4 t = *(const f32x4*)(lds + w * 1024 + l * 16);
#pragma unroll
    for (int k = 1; k < 8; ++k) t += *(const f32x4*)(lds + (k * 2 + w) * 1024 + l * 16);
    u32x2 o = {pk2(t[0], t[1]), pk2(t[2], t[3])};
    *(u32x2*)(p.out + (32 * s + 16 * w + c16) * 1280 + 640 + 16 * G + 4 * g) = o;
  }
}

DEV void prefetch_role(rsrc_t ws, int x, int k, unsigned char* lds) {
  const int l = threadIdx.x & 63;
  const int w = __builtin_amdgcn_readfirstlane(threadIdx.x >> 6);
  if (w >= PFW) return;
  bool dead = false;
  u32 seen0 = 0, seen1 = 0;
  for (int s = 1; s < NSEG; ++s) {
    const u32 prog = wait_prog(ws, OFF_PUM + (u32)(x * 16 + k) * 128, 1, (u32)(s > PFD ? s - PFD : 0), l, dead, 1);
    if ((u32)s <= prog + 1) { s = (int)prog + 1; continue; }
    const u32 need = seg_need(s);
    if (seg_prod(s)) { if (seen1 < need) seen1 = wait_prog(ws, OFF_PVU + 128, 32, need, l, dead, 1); }
    else { if (seen0 < need) seen0 = wait_prog(ws, OFF_PVU, 32, need, l, dead, 1); }
#pragma nounroll
    for (int i = w; i < 14 * (8192 / (64 * PFSTR)); i += PFW) {
      const int u = k * 14 + i / (8192 / (64 * PFSTR));
      u32 o;
      if (u >= 40) o = ((u & 1) ? OFF_QX : OFF_KX) + (u32)((s * 32 + x * 8 + ((u - 40) >> 1)) * 8192);
      else o = OFF_UX + (u32)((s * 80 + x * 20) * 16384 + u * 8192);
      u32 keep;
      asm volatile("s_mov_b32 %0, m0\n\ts_mov_b32 m0, %4\n\ts_nop 0\n\tbuffer_load_dword %1, %2, %3 offen sc1 lds\n\ts_mov_b32 m0, %0\n\ts_waitcnt vmcnt(%5)"
                   : "=&s"(keep) : "v"(l * PFSTR), "s"(ws), "s"(o + (u32)(i % (8192 / (64 * PFSTR))) * (64 * PFSTR)),
                     "s"((u32)(uintptr_t)lds + (u32)w * 256), "n"(PFQ) : "memory");
      __builtin_amdgcn_s_sleep(PFS);
    }
  }
  VM_DRAIN();
}

constexpr int T_SET = 36, T_F1 = 40, T_F2 = 80, T_F3 = 40, T_OUT = 310, T_FAST = 80;
DEV int next_ticket(const Params& p, unsigned char* lds, u32 off) {
  u32* tk = (u32*)(lds + LDS_MAIN - 16);
  __syncthreads();
  if (threadIdx.x == 0) *tk = __hip_atomic_fetch_add((u32*)(p.ws + off), 1u, __ATOMIC_RELAXED, __HIP_MEMORY_SCOPE_AGENT);
  __syncthreads();
  return __builtin_amdgcn_readfirstlane(*tk);
}

DEV u32 claim_role(const Params& p, rsrc_t ws, unsigned char* lds) {
  u32* tk = (u32*)(lds + LDS_MAIN - 16);
  if (threadIdx.x < 64) {
    const int l = threadIdx.x;
    const u32 xcc = (XPERM >> (4 * ((u32)__builtin_amdgcn_s_getreg(20 | (3 << 11)) & 7u))) & 7u, sl = blockIdx.x >> 3;
    u32 old = 0;
    if (l == 0) old = __hip_atomic_fetch_or((u32*)(p.ws + OFF_ROLE + xcc * 128), 1u << sl, __ATOMIC_RELAXED, __HIP_MEMORY_SCOPE_AGENT);
    old = __builtin_amdgcn_readfirstlane(old);
    u32 pos = xcc * 32 + sl;
    if (__builtin_expect((old >> sl) & 1u, 0)) {
      u32 k = 0;
      if (l == 0) k = __hip_atomic_fetch_add((u32*)(p.ws + OFF_ROLE + 1024), 1u, __ATOMIC_RELAXED, __HIP_MEMORY_SCOPE_AGENT);
      k = __builtin_amdgcn_readfirstlane(k);
      u32 c = 0;
#pragma nounroll
      for (u32 spins = 0; spins < SPIN_CAP; ++spins) {
        asm volatile("" ::: "memory");
        c = ld4c(ws, (u32)(l < 8 ? l : 8) * 128, OFF_ROLE);
        u32 sum = (u32)__builtin_amdgcn_readlane(c, 8);
        for (int j = 0; j < 8; ++j) sum += (u32)__builtin_popcount((u32)__builtin_amdgcn_readlane(c, j));
        if (sum >= 256) break;
        __builtin_amdgcn_s_sleep(4);
      }
      pos = 255;
#pragma nounroll
      for (u32 q = 0; q < 256; ++q) {
        const u32 m = (u32)__builtin_amdgcn_readlane(c, q >> 5);
        if (!((m >> (q & 31)) & 1u)) { if (k == 0) { pos = q; break; } --k; }
      }
    }
    if (l == 0) *tk = pos;
  }
  VM_DRAIN();
  __syncthreads();
  if (threadIdx.x == 0) __hip_atomic_fetch_add((u32*)(p.ws + OFF_INITC + (blockIdx.x & 7) * 64), 1u, __ATOMIC_RELAXED, __HIP_MEMORY_SCOPE_AGENT);
  return __builtin_amdgcn_readfirstlane(*tk);
}

__global__ __launch_bounds__(512, 2) void k_main(Params p) {
  extern __shared__ __attribute__((aligned(16))) unsigned char lds[];
  const rsrc_t ws = mk_rsrc(p.ws, WS_BYTES);
  const u32 cb = comm_base(p.ws);
  const int b = blockIdx.x;
  if (b >= 4) {
    st16c(ws, cb + (u32)(b * 1024 + threadIdx.x) * 16, 0, (u32x4){SENT, SENT, SENT, SENT});
    st16c(ws, cb + (u32)(b * 1024 + 512 + threadIdx.x) * 16, 0, (u32x4){SENT, SENT, SENT, SENT});
  }
  if (b < 112) st16c(ws, OFF_SIDE + (u32)(b * 512 + threadIdx.x) * 16, 0, (u32x4){SENT, SENT, SENT, SENT});
  const u32 pos = claim_role(p, ws, lds);
  const int x = (int)(pos >> 5), slot = (int)(pos & 31);
  const int cidx = (x >= 4 || slot >= 28) ? 1000 : (slot < 8 ? x * 8 + slot : 32 + x * 20 + slot - 8);
  if (cidx < 112) {
    if (cidx < 32) score_role(p, ws, cb, cidx, lds);
    else value_role(p, ws, cb, cidx - 32, lds);
    return;
  }
  if (x < 4) {
    init_part(p, ws, cb, x * 4 + slot - 28);
    prefetch_role(ws, x, slot - 28, lds);
    return;
  }
  {
    const int h = (x - 4) * 8 + (slot >> 2);
    const bool bw = (slot & 3) == 1 || (slot & 7) == 7;
    if (bw) b_unit(p, ws, 80 + (x - 4) * 12 + ((slot & 2) ? 8 + (slot >> 3) : (slot >> 2)), lds);
    if (slot & 2) vu_role(p, ws, h, slot & 1, lds);
    else if (!(slot & 1)) qk_role(p, ws, h, 0, lds);
  }
  const bool outonly = (slot & 3) == 3 && (slot & 4);
  if (!outonly) {
    int t = next_ticket(p, lds, OFF_TICKET) - T_SET;
    while (t < 0) { const int k = t + T_SET; if (k < 20) fw_task(p, ws, k, lds); else wt_task(p, ws, k - 20, lds); t = next_ticket(p, lds, OFF_TICKET) - T_SET; }
    while (t < T_F1) { f1_task(p, ws, t % 5, t / 5, lds); t = next_ticket(p, lds, OFF_TICKET) - T_SET; }
    while (t < T_F1 + T_F2) {
      const int q = t - T_F1;
      if (threadIdx.x < 64) { bool dead = false; wait_prog(ws, OFF_F1C + (u32)(q >> 4) * 128, 1, 8, threadIdx.x, dead, 1); }
      wait_fw(ws);
      fft_cols(p, ws, q, lds);
      VM_DRAIN();
      __syncthreads();
      if (threadIdx.x == 0) __hip_atomic_fetch_add((u32*)(p.ws + OFF_F2C), 1u, __ATOMIC_RELAXED, __HIP_MEMORY_SCOPE_AGENT);
      t = next_ticket(p, lds, OFF_TICKET) - T_SET;
    }
    while (t < T_F1 + T_F2 + T_F3) { const int q = t - T_F1 - T_F2; f3_task(p, ws, q % 5, q / 5, lds); t = next_ticket(p, lds, OFF_TICKET) - T_SET; }
  }
  int t2 = next_ticket(p, lds, OFF_TICKET2);
  for (; t2 < T_OUT; t2 = next_ticket(p, lds, OFF_TICKET2)) out_task(p, ws, t2 / 5, t2 % 5, lds);
  for (; t2 < T_OUT + T_FAST; t2 = next_ticket(p, lds, OFF_TICKET2)) out_fast(p, ws, NSEG - 2 + (t2 - T_OUT) / 40, (t2 - T_OUT) % 40, lds);
}

__global__ __launch_bounds__(384) void k_zero(unsigned char* ws) {
  const rsrc_t r = mk_rsrc(ws, WS_BYTES);
  st16c(r, OFF_FLAGS + (u32)(blockIdx.x * 384 + threadIdx.x) * 16, 0, (u32x4){0, 0, 0, 0});
}

extern "C" void kernel_launch(void* const* d_in, const int* in_sizes, int n_in, void* d_out, int out_size, void* d_ws, size_t ws_size,
                              hipStream_t stream) {
  Params p;
  p.ax = (const u16*)d_in[0]; p.wr = (const u16*)d_in[1]; p.wi = (const u16*)d_in[2];
  p.x = (const u16*)d_in[3]; p.mem0 = (const u16*)d_in[4]; p.wq = (const u16*)d_in[5]; p.wk = (const u16*)d_in[6];
  p.wv = (const u16*)d_in[7]; p.wo = (const u16*)d_in[8];
  p.out = (u16*)d_out; p.ws = (unsigned char*)d_ws;
  (void)hipFuncSetAttribute((const void*)k_main, hipFuncAttributeMaxDynamicSharedMemorySize, LDS_MAIN);
  k_zero<<<FLAG_BYTES / (384 * 16), 384, 0, stream>>>(p.ws);
  k_main<<<256, 512, LDS_MAIN, stream>>>(p);
}
```
